```python
import torch

def _softmax(x, axis=-1):
    return torch.softmax(x, dim=axis)

def _gelu(x):
    return torch.nn.functional.gelu(x, approximate='tanh')

def _silu(x):
    return torch.nn.functional.silu(x)

def _sigmoid(x):
    return torch.sigmoid(x)

def _layernorm(x, axis=-1, eps=1e-05):
    m = x.mean(dim=axis, keepdim=True)
    v = x.var(dim=axis, keepdim=True, unbiased=False)
    return (x - m) / torch.sqrt(v + eps)

def _scatter_add(tgt, idx, src, axis=0):
    return tgt.clone().index_add_(axis, idx, src)

def _segment_sum(src, seg, n):
    out = torch.zeros((n,) + src.shape[1:], dtype=src.dtype)
    return out.index_add_(0, seg, src)

def _pad(x, pw):
    flat = tuple((v for pair in reversed(pw) for v in pair))
    return torch.nn.functional.pad(x, flat)
kernel_name = 'recurrent_scan__ssm_conv_h3__mixer_gmlp__prompt'

def setup_inputs():

    _w = lambda a: (lambda t: t.to(torch.bfloat16) if t.is_floating_point() else t)(torch.as_tensor(a))

    def _A_setup():
        B, S, D = (1, 2048, 576)
        ks = 4
        return {'q': _w((lambda *s: torch.randn(*s, dtype=torch.bfloat16) * 0.05)(B, S, D)), 'k': _w((lambda *s: torch.randn(*s, dtype=torch.bfloat16) * 0.2)(B, S, D)), 'v': _w((lambda *s: torch.randn(*s, dtype=torch.bfloat16) * 0.2)(B, S, D)), 'shift_k': _w((lambda *s: torch.randn(*s, dtype=torch.bfloat16) * 0.2)(D, ks) * 0.5), 'ssm_k': _w((lambda *s: torch.randn(*s, dtype=torch.bfloat16) * 0.2)(D, S) * (1.0 / S ** 0.5)), 'ssm_D': _w((lambda *s: torch.randn(*s, dtype=torch.bfloat16) * 0.2)(D))}
    _A_in = _A_setup()

    def _B_setup():
        B, S, D, F = (1, 2048, 576, 4608)
        Fh = F // 2
        return {'x': _w((lambda *s: torch.randn(*s, dtype=torch.bfloat16) * 0.05)(B, S, D)), 'w_in': _w((lambda *s: torch.randn(*s, dtype=torch.bfloat16) * 0.4)(D, 2 * Fh)), 'w_sp': _w(0.01 * (lambda *s: torch.randn(*s, dtype=torch.bfloat16) * 0.4)(S, S)), 'b_sp': _w(1.0 + 0.01 * (lambda *s: torch.randn(*s, dtype=torch.bfloat16) * 0.1)(S)), 'w_out': _w((lambda *s: torch.randn(*s, dtype=torch.bfloat16) * 0.1)(Fh, D))}
    _B_in = _B_setup()
    return {'A_q': _A_in['q'], 'A_k': _A_in['k'], 'A_v': _A_in['v'], 'A_shift_k': _A_in['shift_k'], 'A_ssm_k': _A_in['ssm_k'], 'A_ssm_D': _A_in['ssm_D'], 'B_x': _B_in['x'], 'B_w_in': _B_in['w_in'], 'B_w_sp': _B_in['w_sp'], 'B_b_sp': _B_in['b_sp'], 'B_w_out': _B_in['w_out']}

def reference(A_q, A_k, A_v, A_shift_k, A_ssm_k, A_ssm_D, B_x, B_w_in, B_w_sp, B_b_sp, B_w_out):

    def _A_ref(q, k, v, shift_k, ssm_k, ssm_D):
        B, S, D = q.shape
        ks = shift_k.shape[-1]
        k_pad = _pad(k, ((0, 0), (ks - 1, 0), (0, 0)))
        k_shift = torch.zeros_like(k)
        for t in range(ks):
            k_shift = k_shift + k_pad[:, t:t + S, :] * shift_k[:, ks - 1 - t][None, None, :]
        kv = k_shift * v
        L = 2 * S
        kv_pad = _pad(kv, ((0, 0), (0, S), (0, 0)))
        kf_pad = _pad(ssm_k, ((0, 0), (0, S)))
        kv_f = (lambda x, axes=(-2, -1): torch.fft.rfft2(x.float(), dim=axes))(kv_pad[:, None, :, :], axes=(1, 2))
        kf_f = (lambda x, axes=(-2, -1): torch.fft.rfft2(x.float(), dim=axes))(kf_pad[:, None, :], axes=(1, 2))
        kf_f = (lambda x, *d: x.permute(*d))(kf_f, 1, 2, 0)
        y_f = kv_f * kf_f[None, :, :, :]
        y = (lambda x, s, axes=(-2, -1): torch.fft.irfft2(x, s=s, dim=axes).to(torch.bfloat16))(y_f, (1, L), axes=(1, 2))[:, 0, :S, :]
        y = y + kv * ssm_D[None, None, :]
        return q * y

    def _B_ref(x, w_in, w_sp, b_sp, w_out):
        B, S, D = x.shape
        F = w_in.shape[-1]
        Fh = F // 2
        z = _gelu(torch.einsum('bsd,df->bsf', x, w_in))
        z1 = z[..., :Fh].clone()
        z2 = _layernorm(z[..., Fh:], axis=-1)
        g = torch.einsum('ts,bsf->btf', w_sp, z2) + b_sp[None, :, None]
        return torch.einsum('bsf,fd->bsd', z1 * g, w_out)
    x = A_q
    xn = _layernorm(x, axis=-1)
    s = _A_ref(xn, A_k, A_v, A_shift_k, A_ssm_k, A_ssm_D)
    g = _silu(_B_ref(xn, B_w_in, B_w_sp, B_b_sp, B_w_out))
    return x + s * g
```

```cpp
#include <hip/hip_runtime.h>
#include <hip/hip_bf16.h>
#include <stdint.h>

typedef unsigned short u16;
constexpr int S = 2048, D = 576, F = 4608, FH = 2304;

__device__ __forceinline__ float bf2f(u16 b) { return __uint_as_float(((unsigned)b) << 16); }
__device__ __forceinline__ u16 f2bf(float f) { __hip_bfloat16 h = __float2bfloat16(f); return *reinterpret_cast<u16*>(&h); }
__device__ __forceinline__ float rbf(float f) { return bf2f(f2bf(f)); }

__global__ void ln_kernel(const u16* in, int ld, int n, u16* out, int ldo) {
  int row = blockIdx.x; int tid = threadIdx.x;
  __shared__ float red[4];
  const u16* p = in + (size_t)row * ld;
  float s = 0.f;
  for (int i = tid; i < n; i += 256) s += bf2f(p[i]);
  for (int o = 32; o >= 1; o >>= 1) s += __shfl_xor(s, o);
  if ((tid & 63) == 0) red[tid >> 6] = s;
  __syncthreads();
  float mean = (red[0] + red[1] + red[2] + red[3]) / n;
  __syncthreads();
  float q = 0.f;
  for (int i = tid; i < n; i += 256) { float d = bf2f(p[i]) - mean; q += d * d; }
  for (int o = 32; o >= 1; o >>= 1) q += __shfl_xor(q, o);
  if ((tid & 63) == 0) red[tid >> 6] = q;
  __syncthreads();
  float var = (red[0] + red[1] + red[2] + red[3]) / n;
  float mb = rbf(mean), vb = rbf(var);
  float den = rbf(sqrtf(rbf(vb + 1e-5f)));
  for (int i = tid; i < n; i += 256) out[(size_t)row * ldo + i] = f2bf(rbf(bf2f(p[i]) - mb) / den);
}

__global__ void kv_kernel(const u16* k, const u16* v, const u16* shift_k, u16* kv) {
  int idx = blockIdx.x * 256 + threadIdx.x; if (idx >= S * D) return;
  int s = idx / D, d = idx % D;
  float acc = 0.f;
  for (int t = 0; t < 4; ++t) {
    int u = s + t - 3;
    float kk = u >= 0 ? bf2f(k[(size_t)u * D + d]) : 0.f;
    acc = rbf(acc + rbf(kk * bf2f(shift_k[d * 4 + 3 - t])));
  }
  kv[idx] = f2bf(acc * bf2f(v[idx]));
}

__global__ void conv_kernel(const u16* kv, const u16* ssm_k, const u16* ssm_D, const u16* xn, u16* sout) {
  int idx = blockIdx.x * 256 + threadIdx.x; if (idx >= S * D) return;
  int s = idx / D, d = idx % D;
  float acc = 0.f;
  for (int u = 0; u <= s; ++u) acc += bf2f(kv[(size_t)u * D + d]) * bf2f(ssm_k[(size_t)d * S + (s - u)]);
  float y = rbf(acc);
  y = rbf(y + rbf(bf2f(kv[idx]) * bf2f(ssm_D[d])));
  sout[idx] = f2bf(bf2f(xn[idx]) * y);
}

template <int MODE>
__global__ void gemm_kernel(const u16* A, int lda, const u16* B, int ldb, int M, int N, int K, u16* C, int ldc,
                            const u16* e0, const u16* e1, const u16* e2) {
  __shared__ float As[16][65], Bs[16][65];
  int tx = threadIdx.x & 15, ty = threadIdx.x >> 4;
  int m0 = blockIdx.y * 64, n0 = blockIdx.x * 64;
  float acc[4][4] = {};
  for (int k0 = 0; k0 < K; k0 += 16) {
    for (int i = threadIdx.x; i < 64 * 16; i += 256) {
      int r = i >> 4, c = i & 15; As[c][r] = bf2f(A[(size_t)(m0 + r) * lda + k0 + c]);
      int kk = i >> 6, nn = i & 63; Bs[kk][nn] = bf2f(B[(size_t)(k0 + kk) * ldb + n0 + nn]);
    }
    __syncthreads();
    for (int kk = 0; kk < 16; ++kk) {
      float a[4], b[4];
      for (int i = 0; i < 4; ++i) { a[i] = As[kk][ty * 4 + i]; b[i] = Bs[kk][tx * 4 + i]; }
      for (int i = 0; i < 4; ++i) for (int j = 0; j < 4; ++j) acc[i][j] += a[i] * b[j];
    }
    __syncthreads();
  }
  for (int i = 0; i < 4; ++i) for (int j = 0; j < 4; ++j) {
    int m = m0 + ty * 4 + i, n = n0 + tx * 4 + j;
    float x = rbf(acc[i][j]);
    float o;
    if (MODE == 0) {
      float u = 0.7978845608028654f * (x + 0.044715f * x * x * x);
      o = 0.5f * x * (1.f + tanhf(u));
    } else if (MODE == 1) {
      float g = rbf(x + bf2f(e0[m]));
      o = bf2f(e1[(size_t)m * F + n]) * g;
    } else {
      float g = rbf(x / (1.f + expf(-x)));
      o = bf2f(e0[(size_t)m * D + n]) + rbf(bf2f(e1[(size_t)m * D + n]) * g);
    }
    C[(size_t)m * ldc + n] = f2bf(o);
  }
}

extern "C" void kernel_launch(void* const* d_in, const int* in_sizes, int n_in, void* d_out, int out_size, void* d_ws, size_t ws_size, hipStream_t stream) {
  const u16* q = (const u16*)d_in[0]; const u16* k = (const u16*)d_in[1]; const u16* v = (const u16*)d_in[2];
  const u16* shift_k = (const u16*)d_in[3]; const u16* ssm_k = (const u16*)d_in[4]; const u16* ssm_D = (const u16*)d_in[5];
  const u16* w_in = (const u16*)d_in[7]; const u16* w_sp = (const u16*)d_in[8]; const u16* b_sp = (const u16*)d_in[9]; const u16* w_out = (const u16*)d_in[10];
  u16* ws = (u16*)d_ws;
  u16* xn = ws; u16* kv = xn + S * D; u16* sb = kv + S * D; u16* z = sb + S * D; u16* z2n = z + (size_t)S * F; u16* u = z2n + (size_t)S * FH;
  ln_kernel<<<S, 256, 0, stream>>>(q, D, D, xn, D);
  kv_kernel<<<S * D / 256, 256, 0, stream>>>(k, v, shift_k, kv);
  conv_kernel<<<S * D / 256, 256, 0, stream>>>(kv, ssm_k, ssm_D, xn, sb);
  gemm_kernel<0><<<dim3(F / 64, S / 64), 256, 0, stream>>>(xn, D, w_in, F, S, F, D, z, F, nullptr, nullptr, nullptr);
  ln_kernel<<<S, 256, 0, stream>>>(z + FH, F, FH, z2n, FH);
  gemm_kernel<1><<<dim3(FH / 64, S / 64), 256, 0, stream>>>(w_sp, S, z2n, FH, S, FH, S, u, FH, b_sp, z, nullptr);
  gemm_kernel<2><<<dim3(D / 64, S / 64), 256, 0, stream>>>(u, FH, w_out, D, S, D, FH, (u16*)d_out, D, q, sb, nullptr);
}
```

```cpp
#include <hip/hip_runtime.h>
#include <hip/hip_bf16.h>
#include <stdint.h>

#define LAS __attribute__((address_space(3)))
typedef unsigned short u16;
typedef short bf16x8 __attribute__((ext_vector_type(8)));
typedef short bf16x4 __attribute__((ext_vector_type(4)));
typedef float f32x4 __attribute__((ext_vector_type(4)));
typedef float f32x2 __attribute__((ext_vector_type(2)));
typedef unsigned u32x4 __attribute__((ext_vector_type(4)));
typedef unsigned u32x2 __attribute__((ext_vector_type(2)));
typedef __bf16 bf2_t __attribute__((ext_vector_type(2)));

constexpr int S = 2048, D = 576, F = 4608, FH = 2304;

constexpr size_t WS_Z1 = 0;
constexpr size_t WS_Z2 = WS_Z1 + (size_t)S * FH * 2;
constexpr size_t WS_U = WS_Z2 + (size_t)S * FH * 2;
constexpr size_t WS_WO = WS_U + (size_t)S * FH * 2;
constexpr size_t WS_KVT = WS_WO + (size_t)FH * D * 2;
constexpr size_t WS_Y2T = WS_KVT + (size_t)S * D * 2;
constexpr size_t WS_ST2 = WS_Y2T + (size_t)S * D * 2;
constexpr size_t WS_ST1 = WS_ST2 + (size_t)8 * S * 8;

__device__ __forceinline__ float bf2f(u16 b) { return __uint_as_float(((unsigned)b) << 16); }
__device__ __forceinline__ unsigned pk_bf16(float lo, float hi) { f32x2 f = {lo, hi}; return __builtin_bit_cast(unsigned, __builtin_convertvector(f, bf2_t)); }
__device__ __forceinline__ float rbf(float f) { return __uint_as_float(pk_bf16(f, f) << 16); }
__device__ __forceinline__ float lo_f(unsigned d) { return __uint_as_float(d << 16); }
__device__ __forceinline__ float hi_f(unsigned d) { return __uint_as_float(d & 0xffff0000u); }
template <int N> __device__ __forceinline__ void wait_vm() { asm volatile("s_waitcnt vmcnt(%0)" :: "n"(N) : "memory"); }
__device__ __forceinline__ void wait_lgkm0() { asm volatile("s_waitcnt lgkmcnt(0)" ::: "memory"); }
__device__ __forceinline__ void bar() { asm volatile("" ::: "memory"); __builtin_amdgcn_s_barrier(); asm volatile("" ::: "memory"); }
__device__ __forceinline__ f32x4 mfma32(bf16x8 a, bf16x8 b, f32x4 c) { return __builtin_amdgcn_mfma_f32_16x16x32_bf16(a, b, c, 0, 0, 0); }
__device__ __forceinline__ f32x4 mfma16(bf16x4 a, bf16x4 b, f32x4 c) { return __builtin_amdgcn_mfma_f32_16x16x16bf16_1k(a, b, c, 0, 0, 0); }
__device__ __forceinline__ float dot2(unsigned a, unsigned b, float c) { return __builtin_amdgcn_fdot2_f32_bf16(__builtin_bit_cast(bf2_t, a), __builtin_bit_cast(bf2_t, b), c, false); }
#define GLDS(rsrc, ldsoff, voff, soff) __builtin_amdgcn_raw_ptr_buffer_load_lds(rsrc, (LAS void*)(smem + (ldsoff)), 16, voff, soff, 0, 0)

__device__ __forceinline__ float gelu_f(float x) {
  const float c0 = -2.0f * 0.7978845608028654f * 1.4426950408889634f, c1 = c0 * 0.044715f;
  float e = __builtin_amdgcn_exp2f(x * (x * x * c1 + c0));
  return x * __builtin_amdgcn_rcpf(1.0f + e);
}
__device__ __forceinline__ f32x2 ln_finish(float sum, float sumsq, float inv_n) {
  float mean = sum * inv_n; float var = fmaxf(sumsq * inv_n - mean * mean, 0.f);
  float den = rbf(__builtin_sqrtf(rbf(rbf(var) + 1e-5f)));
  return (f32x2){rbf(mean), den};
}

__global__ __launch_bounds__(256) void k_prep(const u16* __restrict__ k, const u16* __restrict__ v, const u16* __restrict__ shift_k,
                                              const u16* __restrict__ w_out, char* __restrict__ ws) {
  const int idx = blockIdx.x * 256 + threadIdx.x;
  u16* kvt = (u16*)(ws + WS_KVT);
  if (idx < S * D) {
    int s = idx / D, d = idx % D;
    float acc = 0.f;
    for (int t = 0; t < 4; ++t) { int u = s + t - 3; float kk = u >= 0 ? bf2f(k[(size_t)u * D + d]) : 0.f; acc = rbf(acc + rbf(kk * bf2f(shift_k[d * 4 + 3 - t]))); }
    kvt[(size_t)d * S + s] = (u16)pk_bf16(acc * bf2f(v[idx]), 0.f);
  }
  if (idx < FH * D) {
    int f = idx / D, d = idx % D;
    int j = d / 72, dd = d % 72, df = dd >> 4, c = dd & 15, tn = f / 144, ff = f % 144;
    size_t base = WS_WO + (size_t)(j * 16 + tn) * 20736 + df * 4608;
    int nc = df < 4 ? 16 : 8;
    size_t off;
    if (ff < 128) { int p = ff >> 5, r = ff & 31, g = (r & 15) >> 2, j8 = (r >> 4) * 4 + (r & 3); off = (size_t)p * (nc * 64) + (g * nc + c) * 16 + j8 * 2; }
    else { int r = ff - 128, g = r >> 2, j4 = r & 3; off = (size_t)4 * (nc * 64) + (g * nc + c) * 8 + j4 * 2; }
    *(u16*)(ws + base + off) = w_out[idx];
  }
}

__global__ __launch_bounds__(256) void k_conv_naive(const u16* __restrict__ ssm_k, const u16* __restrict__ ssm_D, char* __restrict__ ws) {
  __shared__ float kv[S], h[S];
  const int d = blockIdx.x;
  const u16* kvt = (const u16*)(ws + WS_KVT) + (size_t)d * S;
  for (int i = threadIdx.x; i < S; i += 256) { kv[i] = bf2f(kvt[i]); h[i] = bf2f(ssm_k[(size_t)d * S + i]); }
  __syncthreads();
  u16* y = (u16*)(ws + WS_Y2T) + (size_t)d * S;
  const float dd = bf2f(ssm_D[d]);
  for (int s = threadIdx.x; s < S; s += 256) {
    float acc = 0.f;
    for (int u = 0; u <= s; ++u) acc += kv[u] * h[s - u];
    y[s] = (u16)pk_bf16(rbf(acc) + rbf(kv[s] * dd), 0.f);
  }
}

constexpr int K1_BSLOT = 20480, K1_ASLOT = 16384, K1_BOFF = 2 * K1_ASLOT;
constexpr int K1_TAB = K1_BOFF + 4 * K1_BSLOT;
constexpr int K1_CW = K1_TAB;
constexpr int K1_RS = K1_CW + 4 * 288 * 4;
constexpr int K1_ST = K1_RS + 2 * 128 * 8;
constexpr int K1_FL = K1_ST + 2 * 128 * 8;
constexpr int K1_LDS = K1_FL + 16;

template <bool Z2>
__device__ __forceinline__ void k1_consumer(char* smem, char* ws, int tm, int tn1, int wid) {
  int lane;
  asm volatile("v_mbcnt_lo_u32_b32 %0, -1, 0\n\tv_mbcnt_hi_u32_b32 %0, -1, %0" : "=v"(lane));
  const int wm = wid & 3, wn = wid >> 2;
  const int r = lane & 15, g = lane >> 4, q = r >> 2, p = lane & 3;
  const int abase = (wm * 32 + r) * 128;
  const int asw0 = abase + ((g ^ ((r >> 1) & 7)) * 16), asw1 = abase + (((4 + g) ^ ((r >> 1) & 7)) * 16);
  const int nn0 = 9 * wn + ((tn1 & 1) ? 2 : 0);
  const int swz = ((q >> 1) & 1) | ((g & 1) << 1);
  int bb[4];
#pragma unroll
  for (int j = 0; j < 4; ++j) { int nn = nn0 + j; bb[j] = K1_BOFF + (nn >> 2) * 4096 + (8 * g + q) * 128 + (((nn & 3) ^ swz) * 32) + p * 8; }
  f32x4 acc[2][9];
#pragma unroll
  for (int m = 0; m < 2; ++m)
#pragma unroll
    for (int n = 0; n < 9; ++n) acc[m][n] = (f32x4){0.f, 0.f, 0.f, 0.f};
  float cw[9], rs[2], rq[2];
#pragma unroll
  for (int n = 0; n < 9; ++n) cw[n] = 0.f;
  rs[0] = rs[1] = rq[0] = rq[1] = 0.f;
  if (wid == 0 && lane < 4) *(LAS unsigned*)(smem + K1_FL + lane * 4) = 0u;
  bf16x8 ah[2], bh[3];
#pragma unroll
  for (int m = 0; m < 2; ++m) ah[m] = (bf16x8){0, 0, 0, 0, 0, 0, 0, 0};
#pragma unroll
  for (int n = 0; n < 3; ++n) bh[n] = (bf16x8){0, 0, 0, 0, 0, 0, 0, 0};
  auto step = [&](const int j) __attribute__((always_inline)) {
    bar();
    bf16x8 a[2], b[9];
    const int ao = (j >> 1) * K1_ASLOT + ((j & 1) ? asw1 : asw0);
    a[0] = *(const LAS bf16x8*)(smem + ao); a[1] = *(const LAS bf16x8*)(smem + ao + 2048);
#pragma unroll
    for (int n = 0; n < 9; ++n) {
      const int bo = j * K1_BSLOT + bb[n & 3] + (n >> 2) * 4096;
      bf16x4 lo = __builtin_amdgcn_ds_read_tr16_b64_v4i16((LAS bf16x4*)(smem + bo));
      bf16x4 hi = __builtin_amdgcn_ds_read_tr16_b64_v4i16((LAS bf16x4*)(smem + bo + 512));
      b[n] = __builtin_shufflevector(lo, hi, 0, 1, 2, 3, 4, 5, 6, 7);
    }
#pragma unroll
    for (int n = 0; n < 3; ++n)
#pragma unroll
      for (int m = 0; m < 2; ++m) acc[m][6 + n] = Z2 ? mfma32(ah[m], bh[n], acc[m][6 + n]) : mfma32(bh[n], ah[m], acc[m][6 + n]);
#pragma unroll
    for (int n = 0; n < 6; ++n)
#pragma unroll
      for (int m = 0; m < 2; ++m) acc[m][n] = Z2 ? mfma32(a[m], b[n], acc[m][n]) : mfma32(b[n], a[m], acc[m][n]);
    if (j == wm) {
#pragma unroll
      for (int n = 0; n < 9; ++n) { u32x4 w = __builtin_bit_cast(u32x4, b[n]);
#pragma unroll
        for (int e = 0; e < 4; ++e) cw[n] = dot2(w[e], 0x3f803f80u, cw[n]); }
    }
    if ((j & 1) == wn) {
#pragma unroll
      for (int m = 0; m < 2; ++m) { u32x4 w = __builtin_bit_cast(u32x4, a[m]);
#pragma unroll
        for (int e = 0; e < 4; ++e) { rs[m] = dot2(w[e], 0x3f803f80u, rs[m]); rq[m] = dot2(w[e], w[e], rq[m]); } }
    }
    ah[0] = a[0]; ah[1] = a[1]; bh[0] = b[6]; bh[1] = b[7]; bh[2] = b[8];
    wait_lgkm0();
  };
#pragma unroll 1
  for (int it = 0; it < 4; ++it) { step(0); step(1); step(2); step(3); }
  step(0); step(1);
#pragma unroll
  for (int n = 0; n < 3; ++n)
#pragma unroll
    for (int m = 0; m < 2; ++m) acc[m][6 + n] = Z2 ? mfma32(ah[m], bh[n], acc[m][6 + n]) : mfma32(bh[n], ah[m], acc[m][6 + n]);
#pragma unroll
  for (int n = 0; n < 9; ++n) { cw[n] += __shfl_xor(cw[n], 16); cw[n] += __shfl_xor(cw[n], 32); }
#pragma unroll
  for (int m = 0; m < 2; ++m) { rs[m] += __shfl_xor(rs[m], 16); rs[m] += __shfl_xor(rs[m], 32); rq[m] += __shfl_xor(rq[m], 16); rq[m] += __shfl_xor(rq[m], 32); }
  if (g == 0) {
#pragma unroll
    for (int n = 0; n < 9; ++n) *(LAS float*)(smem + K1_CW + (wm * 288 + wn * 144 + n * 16 + r) * 4) = cw[n];
#pragma unroll
    for (int m = 0; m < 2; ++m) *(LAS f32x2*)(smem + K1_RS + (wn * 128 + wm * 32 + m * 16 + r) * 8) = (f32x2){rs[m], rq[m]};
  }
  wait_lgkm0();
  bar();
  if (!Z2) {
    float mu[2], ri[2];
#pragma unroll
    for (int m = 0; m < 2; ++m) {
      const int row = wm * 32 + m * 16 + r;
      f32x2 s0 = *(const LAS f32x2*)(smem + K1_RS + row * 8), s1 = *(const LAS f32x2*)(smem + K1_RS + (128 + row) * 8);
      f32x2 st = ln_finish(s0.x + s1.x, s0.y + s1.y, 1.0f / D);
      mu[m] = st.x; ri[m] = 1.0f / st.y;
      if (tn1 == 0 && wn == 0 && g == 0) *(f32x2*)(ws + WS_ST1 + (size_t)(tm * 128 + row) * 8) = st;
    }
    unsigned pk[2][9][2];
#pragma unroll
    for (int n = 0; n < 9; ++n) {
      f32x4 c = (f32x4){0.f, 0.f, 0.f, 0.f};
#pragma unroll
      for (int w = 0; w < 4; ++w) c += *(const LAS f32x4*)(smem + K1_CW + (w * 288 + wn * 144 + n * 16 + 4 * g) * 4);
#pragma unroll
      for (int m = 0; m < 2; ++m) {
        float z[4];
#pragma unroll
        for (int e = 0; e < 4; ++e) z[e] = gelu_f((acc[m][n][e] - mu[m] * c[e]) * ri[m]);
        pk[m][n][0] = pk_bf16(z[0], z[1]); pk[m][n][1] = pk_bf16(z[2], z[3]);
      }
    }
#pragma unroll
    for (int m = 0; m < 2; ++m) {
      char* dst = ws + WS_Z1 + (size_t)((tm * 8 + wm * 2 + m) * 16 + (tn1 * 2 + wn)) * 4608;
#pragma unroll
      for (int pp = 0; pp < 4; ++pp) *(u32x4*)(dst + pp * 1024 + lane * 16) = (u32x4){pk[m][2 * pp][0], pk[m][2 * pp][1], pk[m][2 * pp + 1][0], pk[m][2 * pp + 1][1]};
      *(u32x2*)(dst + 4096 + lane * 8) = (u32x2){pk[m][8][0], pk[m][8][1]};
    }
  } else {
    float mu[2][4], ri[2][4];
#pragma unroll
    for (int m = 0; m < 2; ++m)
#pragma unroll
      for (int e = 0; e < 4; ++e) {
        const int row = wm * 32 + m * 16 + 4 * g + e;
        f32x2 s0 = *(const LAS f32x2*)(smem + K1_RS + row * 8), s1 = *(const LAS f32x2*)(smem + K1_RS + (128 + row) * 8);
        f32x2 st = ln_finish(s0.x + s1.x, s0.y + s1.y, 1.0f / D);
        mu[m][e] = st.x; ri[m][e] = 1.0f / st.y;
      }
    float ss[2][4], sq[2][4];
#pragma unroll
    for (int m = 0; m < 2; ++m)
#pragma unroll
      for (int e = 0; e < 4; ++e) { ss[m][e] = 0.f; sq[m][e] = 0.f; }
    const int tn = (tn1 - 8) * 2 + wn;
    char* dst = ws + WS_Z2 + ((size_t)(tn * 32 + tm * 2 + (wm >> 1)) * 2 + (wm & 1)) * 9216 + lane * 16;
#pragma unroll
    for (int n = 0; n < 9; ++n) {
      float c = 0.f;
#pragma unroll
      for (int w = 0; w < 4; ++w) c += *(const LAS float*)(smem + K1_CW + (w * 288 + wn * 144 + n * 16 + r) * 4);
      unsigned pk[4];
#pragma unroll
      for (int m = 0; m < 2; ++m) {
        float z[4];
#pragma unroll
        for (int e = 0; e < 4; ++e) z[e] = gelu_f((acc[m][n][e] - mu[m][e] * c) * ri[m][e]);
        pk[2 * m] = pk_bf16(z[0], z[1]); pk[2 * m + 1] = pk_bf16(z[2], z[3]);
        z[0] = lo_f(pk[2 * m]); z[1] = hi_f(pk[2 * m]); z[2] = lo_f(pk[2 * m + 1]); z[3] = hi_f(pk[2 * m + 1]);
#pragma unroll
        for (int e = 0; e < 4; ++e) { ss[m][e] += z[e]; sq[m][e] += z[e] * z[e]; }
      }
      *(u32x4*)(dst + n * 1024) = (u32x4){pk[0], pk[1], pk[2], pk[3]};
    }
#pragma unroll
    for (int m = 0; m < 2; ++m)
#pragma unroll
      for (int e = 0; e < 4; ++e) {
#pragma unroll
        for (int o = 1; o < 16; o <<= 1) { ss[m][e] += __shfl_xor(ss[m][e], o); sq[m][e] += __shfl_xor(sq[m][e], o); }
      }
    float mys = 0.f, myq = 0.f;
#pragma unroll
    for (int m = 0; m < 2; ++m)
#pragma unroll
      for (int e = 0; e < 4; ++e) if (r == m * 4 + e) { mys = ss[m][e]; myq = sq[m][e]; }
    const int sl = wm * 32 + 8 * g + r;
    if (r < 8) *(LAS f32x2*)(smem + K1_ST + (wn * 128 + sl) * 8) = (f32x2){mys, myq};
    wait_lgkm0();
    unsigned old = 0;
    if (lane == 0) old = __hip_atomic_fetch_add((LAS unsigned*)(smem + K1_FL + wm * 4), 1u, __ATOMIC_RELAXED, __HIP_MEMORY_SCOPE_WORKGROUP);
    old = __builtin_amdgcn_readfirstlane(old);
    asm volatile("" ::: "memory");
    if (old == 1u && r < 8) {
      f32x2 o = *(const LAS f32x2*)(smem + K1_ST + ((wn ^ 1) * 128 + sl) * 8);
      *(f32x2*)(ws + WS_ST2 + ((size_t)(tn1 - 8) * S + tm * 128 + sl) * 8) = (f32x2){mys + o.x, myq + o.y};
    }
  }
}

__global__ __launch_bounds__(768) void k1_gemm1(const u16* __restrict__ x, const u16* __restrict__ w_in, char* __restrict__ ws) {
  extern __shared__ __attribute__((aligned(16))) char smem[];
  const int tid = threadIdx.x, lane = tid & 63, wid = __builtin_amdgcn_readfirstlane(tid >> 6);
  const int b = blockIdx.x, xcd = b & 7, i = b >> 3;
  const int im = i >> 2, in = i & 3;
  const int tm = (xcd & 1) * 8 + im, tn1 = (xcd >> 1) * 4 + in;
  if (wid < 8) {
    if (tn1 < 8) k1_consumer<false>(smem, ws, tm, tn1, wid); else k1_consumer<true>(smem, ws, tm, tn1, wid);
  } else {
    const int lw = wid - 8, lr = lane >> 3, sl = lane & 7;
    const bool z2 = tn1 >= 8;
    auto ra = __builtin_amdgcn_make_buffer_rsrc((void*)(x + (size_t)tm * 128 * D), 0, 128 * D * 2, 0x00020000);
    auto rb = __builtin_amdgcn_make_buffer_rsrc((void*)w_in, 0, D * F * 2, 0x00020000);
    const int arow = z2 ? (8 * (lr >> 2) + (lr & 3)) : lr;
    const int voa0 = arow * (D * 2) + ((sl ^ (lr >> 1)) * 16), voa1 = voa0 ^ 64;
    const int pr0 = z2 ? (32 * (lw >> 1) + 4 * (lw & 1)) : 16 * lw;
    const int pr1 = z2 ? pr0 + 16 : pr0 + 8;
    const int swz = ((lr >> 1) & 1) | ((lw & 1) << 1);
    const int vob = lr * (F * 2) + (((sl >> 1) ^ swz) * 32) + (sl & 1) * 16;
    const int colb = ((tn1 * 9) >> 1) * 128;
    const int rot = (2 * in + im) % 9;
    auto issueB = [&](int j) {
      int st = (j >> 1) + rot; if (st >= 9) st -= 9;
      const int so = (st * 64 + (j & 1) * 32 + lw * 8) * (F * 2) + colb;
#pragma unroll
      for (int cb = 0; cb < 5; ++cb) GLDS(rb, K1_BOFF + (j & 3) * K1_BSLOT + cb * 4096 + lw * 1024, vob, so + cb * 128);
    };
    auto issueA = [&](int ii) {
      int st = ii + rot; if (st >= 9) st -= 9;
#pragma unroll
      for (int hh = 0; hh < 2; ++hh) {
        GLDS(ra, (ii & 1) * K1_ASLOT + (8 * hh + 2 * lw) * 1024, voa0, (64 * hh + pr0) * (D * 2) + st * 128);
        GLDS(ra, (ii & 1) * K1_ASLOT + (8 * hh + 2 * lw + 1) * 1024, voa1, (64 * hh + pr1) * (D * 2) + st * 128);
      }
    };
    issueA(0); issueB(0); issueB(1); issueA(1); issueB(2);
    wait_vm<14>();
    bar();
#pragma unroll
    for (int j = 0; j < 17; ++j) {
      if (j <= 14) { if (j >= 2 && !(j & 1)) issueA((j >> 1) + 1); issueB(j + 3); }
      if (j <= 14) { if (j & 1) wait_vm<10>(); else wait_vm<14>(); }
      else if (j == 15) wait_vm<5>();
      else wait_vm<0>();
      bar();
    }
    bar();
  }
}

constexpr int K2_SLOT = 34816, K2_R = 4 * K2_SLOT, K2_MU = K2_R + S * 4, K2_LDS = K2_MU + S * 2;

__global__ __launch_bounds__(768) void k2_gemm2(const u16* __restrict__ w_sp, const u16* __restrict__ b_sp, char* __restrict__ ws) {
  extern __shared__ __attribute__((aligned(16))) char smem[];
  const int tid = threadIdx.x, lane = tid & 63, wid = __builtin_amdgcn_readfirstlane(tid >> 6);
  const int b = blockIdx.x, xcd = b & 7, i = b >> 3;
  const int tm = (xcd >> 1) * 4 + (i >> 3), tn = (xcd & 1) * 8 + (i & 7);
  if (wid < 8) {
    const int wm = wid & 3, kh = wid >> 2, r = lane & 15, g = lane >> 4;
#pragma unroll
    for (int rr = 0; rr < 2; ++rr) {
      const int s0 = (rr * 512 + tid) * 2;
      f32x4 t = (f32x4){0.f, 0.f, 0.f, 0.f};
#pragma unroll
      for (int j = 0; j < 8; ++j) t += *(const f32x4*)(ws + WS_ST2 + ((size_t)j * S + s0) * 8);
      f32x2 a = ln_finish(t[0], t[1], 1.0f / FH), c = ln_finish(t[2], t[3], 1.0f / FH);
      *(LAS f32x2*)(smem + K2_R + s0 * 4) = (f32x2){1.0f / a.y, 1.0f / c.y};
      *(LAS unsigned*)(smem + K2_MU + s0 * 2) = pk_bf16(a.x, c.x);
    }
    wait_lgkm0();
    const int ao = (wm * 32 + r) * 128 + (((4 * kh + g) ^ ((r >> 1) & 7)) * 16);
    const int bo = 16384 + kh * 9216 + lane * 16;
    const int so = kh * 32 + g * 8;
    f32x4 acc[2][9], cacc[2];
#pragma unroll
    for (int m = 0; m < 2; ++m) { cacc[m] = (f32x4){0.f, 0.f, 0.f, 0.f};
#pragma unroll
      for (int n = 0; n < 9; ++n) acc[m][n] = (f32x4){0.f, 0.f, 0.f, 0.f}; }
    bf16x8 ah[2], bh[3];
#pragma unroll
    for (int m = 0; m < 2; ++m) ah[m] = (bf16x8){0, 0, 0, 0, 0, 0, 0, 0};
#pragma unroll
    for (int n = 0; n < 3; ++n) bh[n] = (bf16x8){0, 0, 0, 0, 0, 0, 0, 0};
    for (int st0 = 0; st0 < 32; st0 += 4) {
#pragma unroll
      for (int j = 0; j < 4; ++j) {
        const int st = st0 + j;
        bar();
        bf16x8 a[2], bb[9], mu;
        a[0] = *(const LAS bf16x8*)(smem + j * K2_SLOT + ao); a[1] = *(const LAS bf16x8*)(smem + j * K2_SLOT + ao + 2048);
        f32x4 r0 = *(const LAS f32x4*)(smem + K2_R + (st * 64 + so) * 4), r1 = *(const LAS f32x4*)(smem + K2_R + (st * 64 + so) * 4 + 16);
        mu = *(const LAS bf16x8*)(smem + K2_MU + (st * 64 + so) * 2);
#pragma unroll
        for (int n = 0; n < 9; ++n) bb[n] = *(const LAS bf16x8*)(smem + j * K2_SLOT + bo + n * 1024);
#pragma unroll
        for (int n = 0; n < 3; ++n)
#pragma unroll
          for (int m = 0; m < 2; ++m) acc[m][6 + n] = mfma32(bh[n], ah[m], acc[m][6 + n]);
#pragma unroll
        for (int m = 0; m < 2; ++m) {
          u32x4 w = __builtin_bit_cast(u32x4, a[m]);
          w[0] = pk_bf16(lo_f(w[0]) * r0[0], hi_f(w[0]) * r0[1]); w[1] = pk_bf16(lo_f(w[1]) * r0[2], hi_f(w[1]) * r0[3]);
          w[2] = pk_bf16(lo_f(w[2]) * r1[0], hi_f(w[2]) * r1[1]); w[3] = pk_bf16(lo_f(w[3]) * r1[2], hi_f(w[3]) * r1[3]);
          a[m] = __builtin_bit_cast(bf16x8, w);
          cacc[m] = mfma32(mu, a[m], cacc[m]);
        }
#pragma unroll
        for (int n = 0; n < 6; ++n)
#pragma unroll
          for (int m = 0; m < 2; ++m) acc[m][n] = mfma32(bb[n], a[m], acc[m][n]);
        ah[0] = a[0]; ah[1] = a[1]; bh[0] = bb[6]; bh[1] = bb[7]; bh[2] = bb[8];
        wait_lgkm0();
      }
    }
#pragma unroll
    for (int n = 0; n < 3; ++n)
#pragma unroll
      for (int m = 0; m < 2; ++m) acc[m][6 + n] = mfma32(bh[n], ah[m], acc[m][6 + n]);
    const int tf = tm * 8 + wm * 2 + kh;
    const char* z1p = ws + WS_Z1 + (size_t)(tf * 16 + tn) * 4608;
    u32x4 z1v[4]; u32x2 z1t;
#pragma unroll
    for (int pp = 0; pp < 4; ++pp) z1v[pp] = *(const u32x4*)(z1p + pp * 1024 + lane * 16);
    z1t = *(const u32x2*)(z1p + 4096 + lane * 8);
    const float bias = bf2f(b_sp[tf * 16 + r]);
    bar();
    {
      const int xo = wid * 10240 + lane * 16;
#pragma unroll
      for (int n = 0; n < 9; ++n) *(LAS f32x4*)(smem + xo + n * 1024) = kh ? acc[0][n] : acc[1][n];
      *(LAS f32x4*)(smem + xo + 9216) = kh ? cacc[0] : cacc[1];
    }
    wait_lgkm0();
    bar();
    {
      const int xo = (wid ^ 4) * 10240 + lane * 16;
      const float c = (kh ? cacc[1][0] : cacc[0][0]) + (*(const LAS f32x4*)(smem + xo + 9216))[0];
      unsigned pk[9][2];
#pragma unroll
      for (int n = 0; n < 9; ++n) {
        f32x4 v = (kh ? acc[1][n] : acc[0][n]) + *(const LAS f32x4*)(smem + xo + n * 1024);
        unsigned z0, z1;
        if (n < 8) { z0 = z1v[n >> 1][(n & 1) * 2]; z1 = z1v[n >> 1][(n & 1) * 2 + 1]; } else { z0 = z1t[0]; z1 = z1t[1]; }
        float gv[4];
#pragma unroll
        for (int e = 0; e < 4; ++e) gv[e] = rbf(rbf(v[e] - c) + bias);
        pk[n][0] = pk_bf16(lo_f(z0) * gv[0], hi_f(z0) * gv[1]); pk[n][1] = pk_bf16(lo_f(z1) * gv[2], hi_f(z1) * gv[3]);
      }
      char* dst = ws + WS_U + (size_t)(tf * 16 + tn) * 4608;
#pragma unroll
      for (int pp = 0; pp < 4; ++pp) *(u32x4*)(dst + pp * 1024 + lane * 16) = (u32x4){pk[2 * pp][0], pk[2 * pp][1], pk[2 * pp + 1][0], pk[2 * pp + 1][1]};
      *(u32x2*)(dst + 4096 + lane * 8) = (u32x2){pk[8][0], pk[8][1]};
    }
  } else {
    const int lw = wid - 8, lr = lane >> 3, sl = lane & 7;
    auto ra = __builtin_amdgcn_make_buffer_rsrc((void*)(w_sp + (size_t)tm * 128 * S), 0, 128 * S * 2, 0x00020000);
    auto rb = __builtin_amdgcn_make_buffer_rsrc((void*)(ws + WS_Z2 + (size_t)tn * 32 * 18432), 0, 32 * 18432, 0x00020000);
    const int voa0 = lr * (S * 2) + ((sl ^ (lr >> 1)) * 16), voa1 = voa0 ^ 64, vob = lane * 16;
    auto issue = [&](int st, int slot) {
#pragma unroll
      for (int e = 0; e < 4; ++e) GLDS(ra, slot * K2_SLOT + (4 * lw + e) * 1024, (e & 1) ? voa1 : voa0, (4 * lw + e) * 8 * (S * 2) + st * 128);
#pragma unroll
      for (int e = 0; e < 4; ++e) GLDS(rb, slot * K2_SLOT + 16384 + (4 * lw + e) * 1024, vob, st * 18432 + (4 * lw + e) * 1024);
      if (lw < 2) GLDS(rb, slot * K2_SLOT + 16384 + (16 + lw) * 1024, vob, st * 18432 + (16 + lw) * 1024);
    };
    issue(0, 0); issue(1, 1); issue(2, 2);
    if (lw < 2) wait_vm<18>(); else wait_vm<16>();
    bar();
    for (int st0 = 0; st0 < 28; st0 += 4) {
#pragma unroll
      for (int j = 0; j < 4; ++j) {
        issue(st0 + j + 3, (j + 3) & 3);
        if (lw < 2) wait_vm<18>(); else wait_vm<16>();
        bar();
      }
    }
    issue(31, 3);
    if (lw < 2) wait_vm<18>(); else wait_vm<16>();
    bar();
    if (lw < 2) wait_vm<9>(); else wait_vm<8>();
    bar();
    wait_vm<0>();
    bar();
  }
}

constexpr int K3_LDS = 8 * 20480;
__global__ __launch_bounds__(512) void k3_gemm3(const u16* __restrict__ x, char* __restrict__ ws, u16* __restrict__ out) {
  extern __shared__ __attribute__((aligned(16))) char smem[];
  const int tid = threadIdx.x, lane = tid & 63, wid = __builtin_amdgcn_readfirstlane(tid >> 6);
  const int b = blockIdx.x, xcd = b & 7, i = b >> 3;
  const int tm = (xcd >> 1) * 8 + (i >> 2), j = (xcd & 1) * 4 + (i & 3);
  const int r = lane & 15, g = lane >> 4;
  f32x4 acc[4][5];
#pragma unroll
  for (int m = 0; m < 4; ++m)
#pragma unroll
    for (int n = 0; n < 5; ++n) acc[m][n] = (f32x4){0.f, 0.f, 0.f, 0.f};
  const int hl = g * 8 + (r & 7);
#pragma unroll
  for (int h = 0; h < 2; ++h) {
    const int tn = wid * 2 + h;
    const char* up = ws + WS_U + (size_t)(tm * 4 * 16 + tn) * 4608;
    const char* wp = ws + WS_WO + (size_t)(j * 16 + tn) * 20736;
#pragma unroll
    for (int p = 0; p < 4; ++p) {
      bf16x8 a[4], bb[5];
#pragma unroll
      for (int m = 0; m < 4; ++m) a[m] = *(const bf16x8*)(up + m * 16 * 4608 + p * 1024 + lane * 16);
#pragma unroll
      for (int n = 0; n < 4; ++n) bb[n] = *(const bf16x8*)(wp + n * 4608 + p * 1024 + lane * 16);
      bb[4] = *(const bf16x8*)(wp + 4 * 4608 + p * 512 + hl * 16);
#pragma unroll
      for (int m = 0; m < 4; ++m)
#pragma unroll
        for (int n = 0; n < 5; ++n) acc[m][n] = mfma32(bb[n], a[m], acc[m][n]);
    }
    {
      bf16x4 a[4], bb[5];
#pragma unroll
      for (int m = 0; m < 4; ++m) a[m] = *(const bf16x4*)(up + m * 16 * 4608 + 4096 + lane * 8);
#pragma unroll
      for (int n = 0; n < 4; ++n) bb[n] = *(const bf16x4*)(wp + n * 4608 + 4096 + lane * 8);
      bb[4] = *(const bf16x4*)(wp + 4 * 4608 + 2048 + hl * 8);
#pragma unroll
      for (int m = 0; m < 4; ++m)
#pragma unroll
        for (int n = 0; n < 5; ++n) acc[m][n] = mfma16(bb[n], a[m], acc[m][n]);
    }
  }
#pragma unroll
  for (int m = 0; m < 4; ++m)
#pragma unroll
    for (int n = 0; n < 5; ++n) *(LAS f32x4*)(smem + wid * 20480 + (m * 5 + n) * 1024 + lane * 16) = acc[m][n];
  __syncthreads();
  for (int fr = wid; fr < 20; fr += 8) {
    const int m = fr / 5, n = fr % 5;
    f32x4 o = (f32x4){0.f, 0.f, 0.f, 0.f};
#pragma unroll
    for (int w = 0; w < 8; ++w) o += *(const LAS f32x4*)(smem + w * 20480 + fr * 1024 + lane * 16);
    const int t = tm * 64 + m * 16 + r, d0 = j * 72 + n * 16 + 4 * g;
    if (n < 4 || g < 2) {
      const u32x2 xv = *(const u32x2*)(x + (size_t)t * D + d0);
      const f32x2 st = *(const f32x2*)(ws + WS_ST1 + (size_t)t * 8);
      const u16* y2 = (const u16*)(ws + WS_Y2T) + (size_t)d0 * S + t;
      float xf[4] = {lo_f(xv[0]), hi_f(xv[0]), lo_f(xv[1]), hi_f(xv[1])}, res[4];
#pragma unroll
      for (int e = 0; e < 4; ++e) {
        const float xn = rbf(rbf(xf[e] - st.x) / st.y);
        const float sv = rbf(xn * bf2f(y2[e * S]));
        const float ob = rbf(o[e]);
        const float gt = rbf(ob * __builtin_amdgcn_rcpf(1.0f + __builtin_amdgcn_exp2f(-1.4426950408889634f * ob)));
        res[e] = xf[e] + rbf(sv * gt);
      }
      *(u32x2*)(out + (size_t)t * D + d0) = (u32x2){pk_bf16(res[0], res[1]), pk_bf16(res[2], res[3])};
    }
  }
}

extern "C" void kernel_launch(void* const* d_in, const int* in_sizes, int n_in, void* d_out, int out_size, void* d_ws, size_t ws_size, hipStream_t stream) {
  const u16* q = (const u16*)d_in[0]; const u16* k = (const u16*)d_in[1]; const u16* v = (const u16*)d_in[2];
  const u16* shift_k = (const u16*)d_in[3]; const u16* ssm_k = (const u16*)d_in[4]; const u16* ssm_D = (const u16*)d_in[5];
  const u16* w_in = (const u16*)d_in[7]; const u16* w_sp = (const u16*)d_in[8]; const u16* b_sp = (const u16*)d_in[9]; const u16* w_out = (const u16*)d_in[10];
  char* ws = (char*)d_ws;
  (void)hipFuncSetAttribute((const void*)k1_gemm1, hipFuncAttributeMaxDynamicSharedMemorySize, K1_LDS);
  (void)hipFuncSetAttribute((const void*)k2_gemm2, hipFuncAttributeMaxDynamicSharedMemorySize, K2_LDS);
  (void)hipFuncSetAttribute((const void*)k3_gemm3, hipFuncAttributeMaxDynamicSharedMemorySize, K3_LDS);
  k_prep<<<FH * D / 256, 256, 0, stream>>>(k, v, shift_k, w_out, ws);
  k_conv_naive<<<D, 256, 0, stream>>>(ssm_k, ssm_D, ws);
  k1_gemm1<<<256, 768, K1_LDS, stream>>>(q, w_in, ws);
  k2_gemm2<<<256, 768, K2_LDS, stream>>>(w_sp, b_sp, ws);
  k3_gemm3<<<256, 512, K3_LDS, stream>>>(q, ws, (u16*)d_out);
}
```

```cpp
#define TLS 2097152
#define EPI 1
#define EP0 16
#define TG0 0
#define TG1 0
#include <hip/hip_runtime.h>
#include <hip/hip_bf16.h>
#include <stdint.h>

#pragma clang diagnostic ignored "-Winline-asm"
#define LAS __attribute__((address_space(3)))
typedef unsigned short u16;
typedef short bf16x8 __attribute__((ext_vector_type(8)));
typedef short bf16x4 __attribute__((ext_vector_type(4)));
typedef float f32x4 __attribute__((ext_vector_type(4)));
typedef float f32x2 __attribute__((ext_vector_type(2)));
typedef unsigned u32x4 __attribute__((ext_vector_type(4)));
typedef unsigned u32x2 __attribute__((ext_vector_type(2)));
typedef unsigned u32x8 __attribute__((ext_vector_type(8)));
typedef unsigned u32x3 __attribute__((ext_vector_type(3)));
typedef float f32x8 __attribute__((ext_vector_type(8)));
typedef unsigned u32x16 __attribute__((ext_vector_type(16)));
typedef __bf16 bf2_t __attribute__((ext_vector_type(2)));
typedef __attribute__((address_space(1))) unsigned gu32;

constexpr int S = 2048, D = 576, F = 4608, FH = 2304;

constexpr size_t WS_ZN = 0;
constexpr size_t WS_U = WS_ZN + (size_t)S * FH * 2;
constexpr int US = 73728;
constexpr size_t WS_WO = WS_U + (size_t)128 * US;
constexpr size_t WS_KVR = WS_WO + (size_t)FH * D * 2;
constexpr size_t WS_Y2T = WS_KVR + (size_t)S * D * 2;
constexpr size_t WS_G0 = WS_Y2T + (size_t)S * D * 4;
constexpr size_t WS_G2 = WS_G0 + (size_t)S * 32;
constexpr size_t WS_F3 = WS_G2 + (size_t)256 * 128 * 16;
constexpr size_t WS_F4 = WS_F3 + 256 * 256;
constexpr size_t WS_F1 = WS_F4 + 256 * 256;
constexpr size_t WS_E = WS_F1 + 1024;
#define WS_ZR WS_ZN

constexpr int T_ROW = 0, T_CW = 2048, T_NM = 4096, T_XS = 2048, T_X9 = 6144, T_B4 = 10240, T_RS = 12288, T_FL = 15360, T_ST = 15616;
constexpr int RING = 16384, SLOT = 36864, CSL = 18432, LDS_BYTES = RING + 4 * SLOT;
constexpr int CV_HS = 4160, CV_HSZ = 4 * CV_HS, CV_XSZ = 9216;
constexpr int CV_H0 = RING + 2 * SLOT, CV_H1 = CV_H0 + CV_HSZ, CV_H2 = RING + 3 * SLOT, CV_X0 = CV_H2 + CV_HSZ, CV_X1 = CV_X0 + CV_XSZ, CV_X2 = 6144;
constexpr int J_K = RING + 1 * SLOT, J_V = J_K + 12096, J_W = J_V + 12096;
constexpr int D_Y = 6144, D_PB = RING + 2 * SLOT;
constexpr int FL_EP = T_FL, FL_UISS = T_FL + 4, FL_F3 = T_FL + 8, FL_CCNT = T_FL + 12, FL_JCNT = T_FL + 16, FL_C9A = T_FL + 20, FL_C9B = T_FL + 24, FL_F1C = T_FL + 28, FL_PB = T_FL + 32, FL_PA = T_FL + 48;
constexpr int FL_YC = T_FL + 192, FL_ALL = T_FL + 196;
constexpr int FL_CP = T_FL + 64;
#define YS0 80
#define NTA 2
#define PSL "1"
#define CPR 0
#define PAUX 0
#define TSA 0
#define ROT 3
#define ROTC (ROT == 0 ? (tn & 7) : (ROT == 1 ? (b & 6) : (ROT == 2 ? (b & 7) : ((b & 1) * 4 + ((b >> 1) & 3)))))
#define TSK 0
#define YO 0
#define CVW 2
#define JSLOT (b >> 3)
#define LPR 3
#define HI() do { if (LPR) __builtin_amdgcn_s_setprio(LPR); } while (0)
#define LO() do { if (LPR) __builtin_amdgcn_s_setprio(0); } while (0)
#define TDA 0
#define SOF 0
#define HLAG 0
#define HKEEP 0
#define EARLY 1
#define YS1 8
#define TWA 2
#define EB1 14
#define EB2 18
#define DT 0
#define YSC YS0
#define XSW 0
#define CVP 0
#define DM 0
#define DTH (DM ? (tn >> 2) & 1 : tn >> 3)
#define DJ (DM ? (tn >> 3) * 4 + (tn & 3) : tn & 7)
#define XST 128
#define S64 0
#define PSG (8 - CVW)
#define NA0 18
#define FK 10
#define XK 18
#define LK 30
#define UP 38
#define LL 2
#define LM 1
#define PB0 8
#define NO8 0
#define DR 0
#define DRX 0
#define TG2 0
#define TPE 1
#define TGM(m) ((m) == 0 ? TG0 : ((m) == 1 ? TG1 : ((m) == 2 ? TG2 : 0)))
#define YF 1
#define DS 0
#define DP0 2
#define DN 2
#define CSPL 2
static_assert(CV_H1 + CV_HSZ <= CV_H2 && CV_X1 + CV_XSZ <= RING + 4 * SLOT && CV_X2 + CV_XSZ <= T_FL && LDS_BYTES <= 163840, "LDS layout");
constexpr int SPIN_G = 1 << 14;
#define HO 0
#define PL 0
#define CM3 7

__device__ __forceinline__ float bf2f(u16 b) { return __uint_as_float(((unsigned)b) << 16); }
__device__ __forceinline__ unsigned pk_bf16(float lo, float hi) { f32x2 f = {lo, hi}; return __builtin_bit_cast(unsigned, __builtin_convertvector(f, bf2_t)); }
__device__ __forceinline__ unsigned pk2(f32x2 v) { return __builtin_bit_cast(unsigned, __builtin_convertvector(v, bf2_t)); }
__device__ __forceinline__ float rbf(float f) { return __uint_as_float(pk_bf16(f, f) << 16); }
__device__ __forceinline__ float lo_f(unsigned d) { return __uint_as_float(d << 16); }
__device__ __forceinline__ float hi_f(unsigned d) { return __uint_as_float(d & 0xffff0000u); }
__device__ __forceinline__ float vmul(float a, float b) { float d; asm volatile("v_mul_f32 %0, %1, %2" : "=v"(d) : "v"(a), "v"(b)); return d; }
__device__ __forceinline__ float vfma(float a, float b, float c) { float d; asm volatile("v_fma_f32 %0, %1, %2, %3" : "=v"(d) : "v"(a), "v"(b), "v"(c)); return d; }
__device__ __forceinline__ float unp(unsigned w, int hi) {
  float d;
  if (hi) asm volatile("v_and_b32 %0, 0xffff0000, %1" : "=v"(d) : "v"(w)); else asm volatile("v_lshlrev_b32 %0, 16, %1" : "=v"(d) : "v"(w));
  return d;
}
__device__ __forceinline__ void st128(u32x4 d, __amdgpu_buffer_rsrc_t r, int voff, int soff) {
  __builtin_amdgcn_raw_buffer_store_b128(d, r, voff, soff, 16);
  asm volatile("s_nop 1" :: "v"(d));
}
__device__ __forceinline__ int lane_id() { int l; asm volatile("v_mbcnt_lo_u32_b32 %0, -1, 0\n\tv_mbcnt_hi_u32_b32 %0, -1, %0" : "=v"(l)); return l; }
template <int N> __device__ __forceinline__ void wait_vm() { asm volatile("s_waitcnt vmcnt(%0)" :: "n"(N) : "memory"); }
__device__ __forceinline__ void wait_lgkm0() { asm volatile("" ::: "memory"); __builtin_amdgcn_s_waitcnt(0xc07f); asm volatile("" ::: "memory"); }
__device__ __forceinline__ void bar() { asm volatile("" ::: "memory"); __builtin_amdgcn_s_barrier(); asm volatile("" ::: "memory"); }
__device__ __forceinline__ f32x4 mfma32(bf16x8 a, bf16x8 b, f32x4 c) { return __builtin_amdgcn_mfma_f32_16x16x32_bf16(a, b, c, 0, 0, 0); }
__device__ __forceinline__ void flag_set(char* p, unsigned ep) { __hip_atomic_store((gu32*)p, ep, __ATOMIC_RELAXED, __HIP_MEMORY_SCOPE_AGENT); }
__device__ __forceinline__ unsigned flag_get(char* p) { return __hip_atomic_load((gu32*)p, __ATOMIC_RELAXED, __HIP_MEMORY_SCOPE_AGENT); }
__device__ __forceinline__ bool all_lanes(bool ok) { return __builtin_amdgcn_ballot_w64(!ok) == 0ull; }
__device__ __forceinline__ void poll_flags(char* p, unsigned ep) {
  _Pragma("unroll 1") for (int spin = 0; spin < SPIN_G; ++spin) { if (all_lanes(flag_get(p) == ep)) break; __builtin_amdgcn_s_sleep(4); }
}
__device__ __forceinline__ void lds_wait1(unsigned adr, unsigned val) {
  unsigned t, c, s;
  asm volatile("s_mov_b32 %1, 0x40000\n\tv_mov_b32 %0, %3\n1:\n\tds_read_b32 %0, %0\n\ts_sub_u32 %1, %1, 1\n\ts_cbranch_scc1 2f\n\ts_waitcnt lgkmcnt(0)\n\tv_readfirstlane_b32 %2, %0\n\tv_mov_b32 %0, %3\n\t"
    "s_cmp_lt_u32 %2, %4\n\ts_cbranch_scc1 1b\n2:\n\ts_waitcnt lgkmcnt(0)" : "=&v"(t), "=&s"(c), "=&s"(s) : "s"(adr), "s"(val) : "memory", "scc");
}
__device__ __forceinline__ unsigned lds_race(unsigned adr, unsigned val) {
  unsigned t, c, s, res;
  asm volatile("s_mov_b32 %1, 0x4000\n\ts_mov_b32 %3, 0\n1:\n\ts_sleep 3\n\tv_mov_b32 %0, %4\n\tds_read_b32 %0, %0\n\ts_getreg_b32 %2, hwreg(HW_REG_IB_STS, 0, 4)\n\ts_cmp_eq_u32 %2, 0\n\ts_cbranch_scc1 2f\n\t"
    "s_sub_u32 %1, %1, 1\n\ts_cbranch_scc1 2f\n\ts_waitcnt lgkmcnt(0)\n\tv_readfirstlane_b32 %2, %0\n\ts_cmp_lt_u32 %2, %5\n\ts_cbranch_scc1 1b\n\ts_mov_b32 %3, 1\n2:\n\ts_waitcnt lgkmcnt(0)"
    : "=&v"(t), "=&s"(c), "=&s"(s), "=&s"(res) : "s"(adr), "s"(val) : "memory", "scc");
  return res;
}
__device__ __forceinline__ unsigned lds_peek(unsigned adr) { const unsigned v = *(volatile LAS unsigned*)(uintptr_t)adr; asm volatile("" ::: "memory"); __builtin_amdgcn_sched_barrier(0); return v; }
__device__ __forceinline__ void lds_add1(unsigned adr) { asm volatile("s_mov_b64 exec, 1\n\tds_add_u32 %0, %1\n\ts_mov_b64 exec, -1" :: "v"(adr), "v"(1u) : "memory"); }
__device__ __forceinline__ unsigned lds_add1r(unsigned adr) {
  unsigned t, s;
  asm volatile("s_mov_b64 exec, 1\n\tds_add_rtn_u32 %0, %2, %3\n\ts_waitcnt lgkmcnt(0)\n\tv_readfirstlane_b32 %1, %0\n\ts_mov_b64 exec, -1" : "=&v"(t), "=s"(s) : "v"(adr), "v"(1u) : "memory");
  return s;
}
#define TS_DECL(n) do {} while (0)
#define TS(i) do {} while (0)
#define TS_FLUSH(kern, role, n) do {} while (0)
#define TQ_DECL do {} while (0)
#define TQ(i) do {} while (0)
#define TQ_FLUSH(kern, role, n) do {} while (0)
#define TS_PAR
#define TS_ARG
#define SB() __builtin_amdgcn_sched_barrier(0)
#define FENCE() do { asm volatile("" ::: "memory"); SB(); } while (0)
#define GLDSO(rsrc, ldsoff, voff, soff, imm) __builtin_amdgcn_raw_ptr_buffer_load_lds(rsrc, (LAS void*)((LAS char*)smem + (ldsoff)), 16, voff, soff, imm, 0)
#define GLDSN(rsrc, ldsoff, voff, soff) __builtin_amdgcn_raw_ptr_buffer_load_lds(rsrc, (LAS void*)((LAS char*)smem + (ldsoff)), 16, voff, soff, 0, NTA)
#define GLDS(rsrc, ldsoff, voff, soff) __builtin_amdgcn_raw_ptr_buffer_load_lds(rsrc, (LAS void*)((LAS char*)smem + (ldsoff)), 16, voff, soff, 0, 0)
#define PREFETCH(r, voff, soff) __builtin_amdgcn_raw_ptr_buffer_load_lds(r, (LAS void*)((LAS char*)smem + T_ST), 4, voff, soff, 0, 0)
#define TOUCH(r, voff, soff, aux) __builtin_amdgcn_raw_ptr_buffer_load_lds(r, (LAS void*)((LAS char*)smem + T_ST), 4, voff, soff, 0, aux)
#define RSRC(p, n) __builtin_amdgcn_make_buffer_rsrc((void*)(p), 0, n, 0x00020000)
#define AINL __attribute__((always_inline))
__device__ __forceinline__ int opq(int x) { asm volatile("" : "+s"(x)); return x; }

template <int CTRL> __device__ __forceinline__ float dpp_add(float x) {
  return x + __uint_as_float(__builtin_amdgcn_update_dpp(0u, __float_as_uint(x), CTRL, 0xf, 0xf, true));
}
__device__ __forceinline__ float rowsum16(float x) { x = dpp_add<0x128>(x); x = dpp_add<0x124>(x); x = dpp_add<0x122>(x); return dpp_add<0x121>(x); }
__device__ __forceinline__ float xsum32(float x) {
  auto p = __builtin_amdgcn_permlane32_swap(__float_as_uint(x), __float_as_uint(x), false, false);
  return __uint_as_float(p[0]) + __uint_as_float(p[1]);
}
__device__ __forceinline__ float xsum16(float x) {
  auto q = __builtin_amdgcn_permlane16_swap(__float_as_uint(x), __float_as_uint(x), false, false);
  return __uint_as_float(q[0]) + __uint_as_float(q[1]);
}
__device__ __forceinline__ float wavesum(float x) { return xsum32(xsum16(rowsum16(x))); }
__device__ __forceinline__ float ln_den(float var) { return rbf(__builtin_amdgcn_sqrtf(rbf(rbf(var) + 1e-5f))); }
__device__ __forceinline__ f32x2 gelu2(f32x2 y, f32x2 k0, f32x2 k1, f32x2 den) {
  f32x2 a = y * (y * y * k1 + k0);
  f32x2 e = {__builtin_amdgcn_exp2f(a.x), __builtin_amdgcn_exp2f(a.y)};
  f32x2 d = e * den + den;
  f32x2 rr = {__builtin_amdgcn_rcpf(d.x), __builtin_amdgcn_rcpf(d.y)};
  return y * rr;
}

struct Args { const u16 *x, *w_in; char* ws; const u16 *w_sp, *k, *v, *shift_k, *ssm_k, *ssm_D, *b_sp, *w_out; u16* out; };
#define GAS __attribute__((address_space(1)))
typedef const GAS u16* gp16;
struct DArgs { gp16 x, w_in; GAS char* ws; gp16 w_sp, k, v, shift_k, ssm_k, ssm_D, b_sp, w_out; GAS u16* out; };
typedef const __attribute__((address_space(4))) DArgs* kap_t;
__device__ __forceinline__ kap_t kargs() { kap_t p = (kap_t)__builtin_amdgcn_kernarg_segment_ptr(); asm volatile("" : "+s"(p)); return p; }
#define A kargs()

__device__ __forceinline__ void red16(float (&s)[8], float (&q)[8], int r, float& os, float& oq) {
#define DA(d, a, c) "v_add_f32_dpp " d ", " a ", " a " " c " row_mask:0xf "
  asm volatile("s_nop 1\n\t"
    DA("%0", "%0", "row_ror:8") "bank_mask:0x3\n\t" DA("%1", "%1", "row_ror:8") "bank_mask:0x3\n\t" DA("%2", "%2", "row_ror:8") "bank_mask:0x3\n\t" DA("%3", "%3", "row_ror:8") "bank_mask:0x3\n\t"
    DA("%0", "%8", "row_ror:8") "bank_mask:0xc\n\t" DA("%1", "%9", "row_ror:8") "bank_mask:0xc\n\t" DA("%2", "%10", "row_ror:8") "bank_mask:0xc\n\t" DA("%3", "%11", "row_ror:8") "bank_mask:0xc\n\t"
    DA("%4", "%4", "row_ror:8") "bank_mask:0x3\n\t" DA("%5", "%5", "row_ror:8") "bank_mask:0x3\n\t" DA("%6", "%6", "row_ror:8") "bank_mask:0x3\n\t" DA("%7", "%7", "row_ror:8") "bank_mask:0x3\n\t"
    DA("%4", "%12", "row_ror:8") "bank_mask:0xc\n\t" DA("%5", "%13", "row_ror:8") "bank_mask:0xc\n\t" DA("%6", "%14", "row_ror:8") "bank_mask:0xc\n\t" DA("%7", "%15", "row_ror:8") "bank_mask:0xc\n\t"
    DA("%0", "%0", "row_shl:4") "bank_mask:0x5\n\t" DA("%1", "%1", "row_shl:4") "bank_mask:0x5\n\t" DA("%0", "%2", "row_shr:4") "bank_mask:0xa\n\t" DA("%1", "%3", "row_shr:4") "bank_mask:0xa\n\t"
    DA("%4", "%4", "row_shl:4") "bank_mask:0x5\n\t" DA("%5", "%5", "row_shl:4") "bank_mask:0x5\n\t" DA("%4", "%6", "row_shr:4") "bank_mask:0xa\n\t" DA("%5", "%7", "row_shr:4") "bank_mask:0xa\n\t"
    DA("%0", "%0", "quad_perm:[1,0,3,2]") "bank_mask:0xf\n\t" DA("%1", "%1", "quad_perm:[1,0,3,2]") "bank_mask:0xf\n\t" DA("%4", "%4", "quad_perm:[1,0,3,2]") "bank_mask:0xf\n\t" DA("%5", "%5", "quad_perm:[1,0,3,2]") "bank_mask:0xf\n\t"
    DA("%0", "%0", "quad_perm:[2,3,0,1]") "bank_mask:0xf\n\t" DA("%1", "%1", "quad_perm:[2,3,0,1]") "bank_mask:0xf\n\t" DA("%4", "%4", "quad_perm:[2,3,0,1]") "bank_mask:0xf\n\t" DA("%5", "%5", "quad_perm:[2,3,0,1]") "bank_mask:0xf"
    : "+v"(s[0]), "+v"(s[1]), "+v"(s[2]), "+v"(s[3]), "+v"(q[0]), "+v"(q[1]), "+v"(q[2]), "+v"(q[3])
    : "v"(s[4]), "v"(s[5]), "v"(s[6]), "v"(s[7]), "v"(q[4]), "v"(q[5]), "v"(q[6]), "v"(q[7]));
#undef DA
  os = (r & 1) ? s[1] : s[0]; oq = (r & 1) ? q[1] : q[0];
}

__device__ __forceinline__ u32x3 z2adr(unsigned lb, int w, int ls = 16) {
  const unsigned a = lb + RING + w * 4096 + (ls ? lane_id() * ls : 0);
  return (u32x3){a, a + 16384 + (w >> 1) * 128, a + (T_X9 - RING) - w * 3072};
}
__device__ __forceinline__ void epi_a(unsigned lb, char* ws, f32x4 (&acc)[2][9], const f32x8 (&rc)[4], const f32x4 (&cs)[3], u32x8 (&z2p)[4], u32x4& z28, int tm, int tn, int wm, unsigned ep TS_PAR) {
  f32x2 ss[4], sq[4];
  const u32x3 ha = z2adr(lb, wm, HO == 3 ? 0 : 16);
  auto zwr = [&](int n) AINL { asm volatile("ds_write_b128 %0, %1 offset:%2" :: "v"(ha[n >> 2]), "v"(n == 8 ? z28 : (n & 1 ? z2p[n >> 1].hi : z2p[n >> 1].lo)), "n"((n & 3) * 1024)); };
#pragma unroll
  for (int n = 0; n < 9; ++n) {
    if (HO == 3 && (n & 3) == 0) asm volatile("s_mov_b32 m0, %0" :: "s"(ha[n >> 2]) : "memory", "m0");
    const float c = n < 8 ? cs[n >> 2][n & 3] : (cs[2][0] + cs[2][1]) + (cs[2][2] + cs[2][3]);
#pragma unroll
    for (int i = 0; i < 4; ++i) {
      const int m = i >> 1, h = (i & 1) * 2;
      const f32x2 z = gelu2((f32x2){rc[0][2 * i], rc[0][2 * i + 1]} * (f32x2){c, c} + (f32x2){acc[m][n][h], acc[m][n][h + 1]},
                            (f32x2){rc[1][2 * i], rc[1][2 * i + 1]}, (f32x2){rc[2][2 * i], rc[2][2 * i + 1]}, (f32x2){rc[3][2 * i], rc[3][2 * i + 1]});
      if (HO == 3) asm volatile("ds_write_addtid_b32 %0 offset:%1" :: "v"(pk2(z)), "n"((n & 3) * 1024 + i * 256) : "memory");
      else if (n < 8) z2p[n >> 1][(n & 1) * 4 + i] = pk2(z); else z28[i] = pk2(z);
      if (n == 0) { ss[i] = z; sq[i] = z * z; } else { ss[i] += z; sq[i] += z * z; }
    }
    asm volatile("" : "+v"(ss[0]), "+v"(ss[1]), "+v"(ss[2]), "+v"(ss[3]), "+v"(sq[0]), "+v"(sq[1]), "+v"(sq[2]), "+v"(sq[3]));
    if (HO == 0 && n >= HLAG) zwr(n - HLAG);
  }
  if (HO == 0) {
#pragma unroll
    for (int n = 9 - HLAG; n < 9; ++n) zwr(n);
    if (HKEEP == 1) asm volatile("" :: "v"(z2p[0]), "v"(z2p[1]), "v"(z2p[2]), "v"(z2p[3]), "v"(z28));
  }
  const int lane = lane_id(), r = lane & 15, g = lane >> 4;
  if (HO == 0 || HO == 3) asm volatile("ds_write_b32 %0, %1" :: "v"(lb + FL_CP + wm * 16 + (lane & 3) * 4), "v"(9u) : "memory");
  float s8[8], q8[8], mys, myq;
#pragma unroll
  for (int i = 0; i < 8; ++i) { s8[i] = ss[i >> 1][i & 1]; q8[i] = sq[i >> 1][i & 1]; }
  const int ro = (r & 2) ? 0x7ffffff0 : (8 * g + (r >> 2) * 2 + (r & 1)) * 16;
  red16(s8, q8, r, mys, myq);
  TS(7);
  st128((u32x4){__float_as_uint(mys), ep, __float_as_uint(myq), ep}, RSRC(ws + WS_G2 + (size_t)(tm * 16 + tn) * 2048 + wm * 512, 512), ro, 0);
}

__device__ __forceinline__ void pub_a(char* ws, f32x8 ss, f32x8 sq, int tm, int tn, int wm, unsigned ep) {
  const int lane = lane_id(), r = lane & 15, g = lane >> 4;
  float s8[8], q8[8], mys, myq;
#pragma unroll
  for (int i = 0; i < 8; ++i) { s8[i] = ss[i]; q8[i] = sq[i]; }
  const int ro = (r & 2) ? 0x7ffffff0 : (8 * g + (r >> 2) * 2 + (r & 1)) * 16;
  red16(s8, q8, r, mys, myq);
  st128((u32x4){__float_as_uint(mys), ep, __float_as_uint(myq), ep}, RSRC(ws + WS_G2 + (size_t)(tm * 16 + tn) * 2048 + wm * 512, 512), ro, 0);
}

constexpr int eb_ord(int k) { return k < 12 ? k + 4 : (k == 14 ? 16 : (k == 17 ? 17 : k - (k < 14 ? 12 : 13))); }
template <int K0, int K1> __device__ __forceinline__ void epi_b(const f32x4 (&ac)[18], const f32x4 (&rc)[8], const f32x4 (&c)[3], u32x2 (&z1)[18]) {
#pragma unroll
  for (int k = K0; k < K1; ++k) {
    const int i = eb_ord(k), m = i < 16 ? i >> 1 : i - 16, n = i < 16 ? i & 1 : 2;
    const f32x2 nm = {rc[m][0], rc[m][0]}, k0 = {rc[m][1], rc[m][1]}, k1 = {rc[m][2], rc[m][2]}, dn = {rc[m][3], rc[m][3]};
    z1[i][0] = pk2(gelu2(nm * (f32x2){c[n][0], c[n][1]} + (f32x2){ac[i][0], ac[i][1]}, k0, k1, dn));
    z1[i][1] = pk2(gelu2(nm * (f32x2){c[n][2], c[n][3]} + (f32x2){ac[i][2], ac[i][3]}, k0, k1, dn));
    if ((k - K0) & 1) asm volatile("" : "+v"(z1[eb_ord(k - 1)]), "+v"(z1[i]));
  }
}

__device__ __forceinline__ int d_rot(int tm, int tn) { const int r = ((DR & 1) ? 2 * (tm & 3) + DRX * ((tm & 3) >> 1) : 0) + ((DR & 2) ? (tn & 7) : 0); return r >= 9 ? r - 9 : r; }
__device__ __forceinline__ int d_step(int rot, int q) { if (!DR) return q; const int s = opq(rot) + (q >> 1); return 2 * (s >= 9 ? s - 9 : s) + (q & 1); }
__device__ __forceinline__ int d_p1(int w) { return RING + w * CSL; }

__device__ __forceinline__ void d_finish(char* smem, f32x4 o, u32x2 xv, int trow, int ty, int dy, GAS u16* __restrict__ op) {
  const f32x4 st = *(const LAS f32x4*)(smem + T_ROW + trow * 16);
  const u32x2 yv = __builtin_bit_cast(u32x2, __builtin_amdgcn_ds_read_tr16_b64_v4i16((LAS bf16x4*)(smem + D_Y + dy * 128 + ty * 2)));
  const float xf[4] = {lo_f(xv[0]), hi_f(xv[0]), lo_f(xv[1]), hi_f(xv[1])}, yf[4] = {lo_f(yv[0]), hi_f(yv[0]), lo_f(yv[1]), hi_f(yv[1])};
  float res[4];
#pragma unroll
  for (int e = 0; e < 4; ++e) {
    const float ee = __builtin_amdgcn_exp2f(-1.4426950408889634f * o[e]);
    res[e] = xf[e] + (xf[e] + st[0]) * yf[e] * o[e] * __builtin_amdgcn_rcpf(ee * st[3] + st[3]);
  }
  *(GAS u32x2*)op = (u32x2){pk_bf16(res[0], res[1]), pk_bf16(res[2], res[3])};
}

struct DPre { f32x4 xf, xs; };
__device__ __forceinline__ DPre d_pre(u32x2 xv, float s0) {
  DPre d;
  d.xf = (f32x4){lo_f(xv[0]), hi_f(xv[0]), lo_f(xv[1]), hi_f(xv[1])};
  d.xs = d.xf + s0;
  asm volatile("" : "+v"(d.xf), "+v"(d.xs));
  return d;
}
__device__ __forceinline__ u32x2 d_y(unsigned ya, int off) { return __builtin_bit_cast(u32x2, __builtin_amdgcn_ds_read_tr16_b64_v4i16((LAS bf16x4*)(uintptr_t)(ya + off))); }
__device__ __forceinline__ f32x4 d_ld(unsigned a, int off) { return *(const LAS f32x4*)(uintptr_t)(a + off); }
__device__ __forceinline__ f32x4 d_gate(f32x4 o, const DPre& d, float s3) {
  f32x4 t = o * -1.4426950408889634f, q;
  asm volatile("" : "+v"(t));
#pragma unroll
  for (int e = 0; e < 4; ++e) q[e] = __builtin_amdgcn_exp2f(t[e]);
  q = q * s3 + s3;
  asm volatile("" : "+v"(q));
#pragma unroll
  for (int e = 0; e < 4; ++e) q[e] = __builtin_amdgcn_rcpf(q[e]);
  f32x4 gt = d.xs * o * q;
  asm volatile("" : "+v"(gt));
  return gt;
}
__device__ __forceinline__ void d_fin(f32x4 gt, u32x2 yv, const DPre& d, GAS u16* __restrict__ op) {
  const f32x4 res = d.xf + gt * (f32x4){lo_f(yv[0]), hi_f(yv[0]), lo_f(yv[1]), hi_f(yv[1])};
  *(GAS u32x2*)op = (u32x2){pk_bf16(res[0], res[1]), pk_bf16(res[2], res[3])};
}
__device__ __forceinline__ void d_post(f32x4 o, u32x2 yv, const DPre& d, float s3, GAS u16* __restrict__ op) { d_fin(d_gate(o, d, s3), yv, d, op); }
__device__ __forceinline__ f32x4 d_sum(const f32x4 (&p)[4]) { return (p[0] + p[1]) + (p[2] + p[3]); }

__device__ __forceinline__ unsigned epoch(unsigned raw) { return __builtin_amdgcn_readfirstlane(raw) * 0x9E3779B1u + 0x7F4A7C15u; }

__device__ __forceinline__ void consumer(char* smem, gp16 px_, char* ws, int b, int tm, int tn, int wm, unsigned long long t00) {
  const unsigned lb = (unsigned)(uintptr_t)(LAS char*)smem;
  TS_DECL(16); TS(0);
  if (CPR) __builtin_amdgcn_s_setprio(CPR);
  unsigned ep;
  u32x4 ad0; u32x8 ad1; u32x2 ad2;
  {
    const int lane = lane_id(), r = lane & 15, g = lane >> 4, row0 = tm * 128 + tn * 8 + 2 * wm;
    const GAS char* xp = (const GAS char*)(px_ + (size_t)row0 * D) + (lane >> 5) * (D * 2) + (lane & 31) * 12;
    u32x3 w[3];
#pragma unroll
    for (int i = 0; i < 3; ++i) w[i] = *(const GAS u32x3*)(xp + i * 384);
    const unsigned eraw = *(const GAS unsigned*)(ws + WS_E);
    const unsigned a0 = lb + RING + (wm * 32 + r) * 128 + ((g ^ ((r >> 1) & 7)) * 16);
    const unsigned b0 = lb + RING + 16384 + (g & 1) * 8320 + (g >> 1) * 2048 + (r >> 2) * 256 + (lane & 3) * 8, b1 = b0 + 2 * SLOT, x = wm ^ (r >> 2);
    const unsigned b8 = lb + RING + 32896 + (g >> 1) * 512 + (g & 1) * 128 + (r >> 2) * 32 + (lane & 3) * 8;
    ad0 = (u32x4){a0, a0 ^ 64, a0 + 2 * SLOT, (a0 ^ 64) + 2 * SLOT};
    ad1 = (u32x8){b0 + (0 ^ x) * 32, b0 + (1 ^ x) * 32, b0 + (2 ^ x) * 32, b0 + (3 ^ x) * 32, b8, b1 + (0 ^ x) * 32, b1 + (1 ^ x) * 32, b1 + (2 ^ x) * 32};
    ad2 = (u32x2){b1 + (3 ^ x) * 32, b8 + 2 * SLOT};
    asm volatile("" : "+v"(ad0), "+v"(ad1), "+v"(ad2));
    float s = 0.f, q = 0.f;
#pragma unroll
    for (int i = 0; i < 9; ++i) s += lo_f(w[i / 3][i % 3]) + hi_f(w[i / 3][i % 3]);
    const float mean = xsum16(rowsum16(s)) * (1.0f / D);
#pragma unroll
    for (int i = 0; i < 9; ++i) { const float a = lo_f(w[i / 3][i % 3]) - mean, b = hi_f(w[i / 3][i % 3]) - mean; q += a * a + b * b; }
    const float den = ln_den(xsum16(rowsum16(q)) * (1.0f / D));
    ep = epoch(eraw);
    if (wm == 0) *(LAS unsigned*)(smem + T_FL + lane * 4) = lane == 0 ? ep : 0u;
    const float ri = __builtin_amdgcn_rcpf(den), c0 = -2.0f * 0.7978845608028654f * 1.4426950408889634f, c1 = c0 * 0.044715f;
    st128((u32x4){__float_as_uint((lane & 1) ? c1 * ri * ri * ri : -rbf(mean)), ep, __float_as_uint((lane & 1) ? den : c0 * ri), ep}, RSRC(ws + WS_G0 + row0 * 32, 64), (lane & 30) ? 0x7ffffff0 : (lane & 32) + (lane & 1) * 16, 0);
  }
  TS(1);
  u32x8 z2p[4]; u32x4 z28;
  {
    f32x4 acc[2][9];
    const int lane = lane_id(), r = lane & 15;
    const unsigned cr = lb + T_CW + r * 4, cw = cr + wm * 64, rs = lb + T_RS + (wm * 32 + (lane >> 4) * 8) * 4;
    const u32x4 ca = {cw, cr + (wm ^ 1) * 64, cr + (wm ^ 2) * 64, cr + (wm ^ 3) * 64};
    f32x8 rc[4]; f32x4 cs[3];
    unsigned tmp;
    asm volatile(
      "v_mov_b32 v172, 0x3f803f80\n\t"
      "v_mov_b32 v173, 0x3f803f80\n\t"
      "v_mov_b32 v174, 0x3f803f80\n\t"
      "v_mov_b32 v175, 0x3f803f80\n\t"
      "v_mov_b32 v168, 0\n\t"
      "v_mov_b32 v169, 0\n\t"
      "v_mov_b32 v170, 0\n\t"
      "v_mov_b32 v171, 0\n\t"
      "s_waitcnt lgkmcnt(0)\n\t"
      "s_barrier\n\t"
      "ds_read_b128 v[72:75], v176 offset:0\n\t"
      "ds_read_b64_tr_b16 v[80:81], v180 offset:0\n\t"
      "ds_read_b64_tr_b16 v[82:83], v180 offset:1024\n\t"
      "ds_read_b128 v[76:79], v176 offset:2048\n\t"
      "ds_read_b64_tr_b16 v[84:85], v181 offset:0\n\t"
      "ds_read_b64_tr_b16 v[86:87], v181 offset:1024\n\t"
      "ds_read_b64_tr_b16 v[88:89], v182 offset:0\n\t"
      "ds_read_b64_tr_b16 v[90:91], v182 offset:1024\n\t"
      "ds_read_b64_tr_b16 v[92:93], v183 offset:0\n\t"
      "ds_read_b64_tr_b16 v[94:95], v183 offset:1024\n\t"
      "ds_read_b64_tr_b16 v[96:97], v180 offset:128\n\t"
      "ds_read_b64_tr_b16 v[98:99], v180 offset:1152\n\t"
      "ds_read_b64_tr_b16 v[100:101], v181 offset:128\n\t"
      "ds_read_b64_tr_b16 v[102:103], v181 offset:1152\n\t"
      "ds_read_b64_tr_b16 v[104:105], v182 offset:128\n\t"
      "ds_read_b64_tr_b16 v[106:107], v182 offset:1152\n\t"
      "ds_read_b64_tr_b16 v[108:109], v183 offset:128\n\t"
      "ds_read_b64_tr_b16 v[110:111], v183 offset:1152\n\t"
      "ds_read_b64_tr_b16 v[112:113], v184 offset:0\n\t"
      "ds_read_b64_tr_b16 v[114:115], v184 offset:256\n\t"
      "s_waitcnt lgkmcnt(14)\n\t"
      "v_mfma_f32_16x16x32_bf16 v[0:3], v[72:75], v[80:83], 0\n\t"
      "ds_read_b128 v[116:119], v177 offset:0\n\t"
      "ds_read_b64_tr_b16 v[124:125], v180 offset:4096\n\t"
      "v_mfma_f32_16x16x32_bf16 v[160:163], v[172:175], v[80:83], 0\n\t"
      "ds_read_b64_tr_b16 v[126:127], v180 offset:5120\n\t"
      "v_mfma_f32_16x16x32_bf16 v[36:39], v[76:79], v[80:83], 0\n\t"
      "ds_read_b128 v[120:123], v177 offset:2048\n\t"
      "v_mfma_f32_16x16x32_bf16 v[4:7], v[72:75], v[84:87], 0\n\t"
      "ds_read_b64_tr_b16 v[128:129], v181 offset:4096\n\t"
      "ds_read_b64_tr_b16 v[130:131], v181 offset:5120\n\t"
      "v_mfma_f32_16x16x32_bf16 v[40:43], v[76:79], v[84:87], 0\n\t"
      "ds_read_b64_tr_b16 v[132:133], v182 offset:4096\n\t"
      "s_waitcnt lgkmcnt(14)\n\t"
      "v_mfma_f32_16x16x32_bf16 v[8:11], v[72:75], v[88:91], 0\n\t"
      "ds_read_b64_tr_b16 v[134:135], v182 offset:5120\n\t"
      "v_mfma_f32_16x16x32_bf16 v[44:47], v[76:79], v[88:91], 0\n\t"
      "ds_read_b64_tr_b16 v[136:137], v183 offset:4096\n\t"
      "ds_read_b64_tr_b16 v[138:139], v183 offset:5120\n\t"
      "v_mfma_f32_16x16x32_bf16 v[12:15], v[72:75], v[92:95], 0\n\t"
      "ds_read_b64_tr_b16 v[140:141], v180 offset:4224\n\t"
      "v_mfma_f32_16x16x32_bf16 v[48:51], v[76:79], v[92:95], 0\n\t"
      "ds_read_b64_tr_b16 v[142:143], v180 offset:5248\n\t"
      "v_mfma_f32_16x16x32_bf16 v[16:19], v[72:75], v[96:99], 0\n\t"
      "ds_read_b64_tr_b16 v[144:145], v181 offset:4224\n\t"
      "ds_read_b64_tr_b16 v[146:147], v181 offset:5248\n\t"
      "v_mfma_f32_16x16x32_bf16 v[164:167], v[172:175], v[96:99], 0\n\t"
      "ds_read_b64_tr_b16 v[148:149], v182 offset:4224\n\t"
      "v_mfma_f32_16x16x32_bf16 v[52:55], v[76:79], v[96:99], 0\n\t"
      "ds_read_b64_tr_b16 v[150:151], v182 offset:5248\n\t"
      "s_waitcnt lgkmcnt(14)\n\t"
      "v_mfma_f32_16x16x32_bf16 v[20:23], v[72:75], v[100:103], 0\n\t"
      "ds_read_b64_tr_b16 v[152:153], v183 offset:4224\n\t"
      "ds_read_b64_tr_b16 v[154:155], v183 offset:5248\n\t"
      "v_mfma_f32_16x16x32_bf16 v[56:59], v[76:79], v[100:103], 0\n\t"
      "ds_read_b64_tr_b16 v[156:157], v184 offset:1024\n\t"
      "v_mfma_f32_16x16x32_bf16 v[24:27], v[72:75], v[104:107], 0\n\t"
      "ds_read_b64_tr_b16 v[158:159], v184 offset:1280\n\t"
      "v_mfma_f32_16x16x32_bf16 v[60:63], v[76:79], v[104:107], 0\n\t"
      "v_mfma_f32_16x16x32_bf16 v[28:31], v[72:75], v[108:111], 0\n\t"
      "v_mfma_f32_16x16x32_bf16 v[64:67], v[76:79], v[108:111], 0\n\t"
      "v_mfma_f32_16x16x32_bf16 v[32:35], v[72:75], v[112:115], 0\n\t"
      "s_cmp_lg_u32 %[wm], 0\n\t"
      "s_cbranch_scc1 11f\n\t"
      "v_mfma_f32_16x16x32_bf16 v[168:171], v[172:175], v[112:115], v[168:171]\n\t"
      "11:\n\t"
      "v_mfma_f32_16x16x32_bf16 v[68:71], v[76:79], v[112:115], 0\n\t"
      "s_waitcnt lgkmcnt(0)\n\t"
      "s_barrier\n\t"
      "v_mfma_f32_16x16x32_bf16 v[0:3], v[116:119], v[124:127], v[0:3]\n\t"
      "ds_read_b128 v[72:75], v176 offset:36864\n\t"
      "ds_read_b64_tr_b16 v[80:81], v180 offset:36864\n\t"
      "v_mfma_f32_16x16x32_bf16 v[160:163], v[172:175], v[124:127], v[160:163]\n\t"
      "ds_read_b64_tr_b16 v[82:83], v180 offset:37888\n\t"
      "v_mfma_f32_16x16x32_bf16 v[36:39], v[120:123], v[124:127], v[36:39]\n\t"
      "ds_read_b128 v[76:79], v176 offset:38912\n\t"
      "v_mfma_f32_16x16x32_bf16 v[4:7], v[116:119], v[128:131], v[4:7]\n\t"
      "ds_read_b64_tr_b16 v[84:85], v181 offset:36864\n\t"
      "v_mfma_f32_16x16x32_bf16 v[40:43], v[120:123], v[128:131], v[40:43]\n\t"
      "ds_read_b64_tr_b16 v[86:87], v181 offset:37888\n\t"
      "v_mfma_f32_16x16x32_bf16 v[8:11], v[116:119], v[132:135], v[8:11]\n\t"
      "ds_read_b64_tr_b16 v[88:89], v182 offset:36864\n\t"
      "v_mfma_f32_16x16x32_bf16 v[44:47], v[120:123], v[132:135], v[44:47]\n\t"
      "ds_read_b64_tr_b16 v[90:91], v182 offset:37888\n\t"
      "v_mfma_f32_16x16x32_bf16 v[12:15], v[116:119], v[136:139], v[12:15]\n\t"
      "ds_read_b64_tr_b16 v[92:93], v183 offset:36864\n\t"
      "ds_read_b64_tr_b16 v[94:95], v183 offset:37888\n\t"
      "v_mfma_f32_16x16x32_bf16 v[48:51], v[120:123], v[136:139], v[48:51]\n\t"
      "ds_read_b64_tr_b16 v[96:97], v180 offset:36992\n\t"
      "v_mfma_f32_16x16x32_bf16 v[16:19], v[116:119], v[140:143], v[16:19]\n\t"
      "ds_read_b64_tr_b16 v[98:99], v180 offset:38016\n\t"
      "v_mfma_f32_16x16x32_bf16 v[164:167], v[172:175], v[140:143], v[164:167]\n\t"
      "ds_read_b64_tr_b16 v[100:101], v181 offset:36992\n\t"
      "v_mfma_f32_16x16x32_bf16 v[52:55], v[120:123], v[140:143], v[52:55]\n\t"
      "ds_read_b64_tr_b16 v[102:103], v181 offset:38016\n\t"
      "v_mfma_f32_16x16x32_bf16 v[20:23], v[116:119], v[144:147], v[20:23]\n\t"
      "ds_read_b64_tr_b16 v[104:105], v182 offset:36992\n\t"
      "v_mfma_f32_16x16x32_bf16 v[56:59], v[120:123], v[144:147], v[56:59]\n\t"
      "ds_read_b64_tr_b16 v[106:107], v182 offset:38016\n\t"
      "v_mfma_f32_16x16x32_bf16 v[24:27], v[116:119], v[148:151], v[24:27]\n\t"
      "ds_read_b64_tr_b16 v[108:109], v183 offset:36992\n\t"
      "ds_read_b64_tr_b16 v[110:111], v183 offset:38016\n\t"
      "v_mfma_f32_16x16x32_bf16 v[60:63], v[120:123], v[148:151], v[60:63]\n\t"
      "ds_read_b64_tr_b16 v[112:113], v184 offset:36864\n\t"
      "v_mfma_f32_16x16x32_bf16 v[28:31], v[116:119], v[152:155], v[28:31]\n\t"
      "ds_read_b64_tr_b16 v[114:115], v184 offset:37120\n\t"
      "v_mfma_f32_16x16x32_bf16 v[64:67], v[120:123], v[152:155], v[64:67]\n\t"
      "ds_read_b64_tr_b16 v[124:125], v180 offset:40960\n\t"
      "v_mfma_f32_16x16x32_bf16 v[32:35], v[116:119], v[156:159], v[32:35]\n\t"
      "ds_read_b64_tr_b16 v[126:127], v180 offset:41984\n\t"
      "s_cmp_lg_u32 %[wm], 0\n\t"
      "s_cbranch_scc1 12f\n\t"
      "v_mfma_f32_16x16x32_bf16 v[168:171], v[172:175], v[156:159], v[168:171]\n\t"
      "12:\n\t"
      "v_mfma_f32_16x16x32_bf16 v[68:71], v[120:123], v[156:159], v[68:71]\n\t"
      "ds_read_b64_tr_b16 v[128:129], v181 offset:40960\n\t"
      "s_waitcnt lgkmcnt(14)\n\t"
      "v_mfma_f32_16x16x32_bf16 v[0:3], v[72:75], v[80:83], v[0:3]\n\t"
      "ds_read_b64_tr_b16 v[130:131], v181 offset:41984\n\t"
      "v_mfma_f32_16x16x32_bf16 v[160:163], v[172:175], v[80:83], v[160:163]\n\t"
      "ds_read_b64_tr_b16 v[132:133], v182 offset:40960\n\t"
      "ds_read_b64_tr_b16 v[134:135], v182 offset:41984\n\t"
      "v_mfma_f32_16x16x32_bf16 v[36:39], v[76:79], v[80:83], v[36:39]\n\t"
      "ds_read_b64_tr_b16 v[136:137], v183 offset:40960\n\t"
      "v_mfma_f32_16x16x32_bf16 v[4:7], v[72:75], v[84:87], v[4:7]\n\t"
      "ds_read_b64_tr_b16 v[138:139], v183 offset:41984\n\t"
      "v_mfma_f32_16x16x32_bf16 v[40:43], v[76:79], v[84:87], v[40:43]\n\t"
      "ds_read_b64_tr_b16 v[140:141], v180 offset:41088\n\t"
      "v_mfma_f32_16x16x32_bf16 v[8:11], v[72:75], v[88:91], v[8:11]\n\t"
      "ds_read_b64_tr_b16 v[142:143], v180 offset:42112\n\t"
      "v_mfma_f32_16x16x32_bf16 v[44:47], v[76:79], v[88:91], v[44:47]\n\t"
      "ds_read_b64_tr_b16 v[144:145], v181 offset:41088\n\t"
      "s_waitcnt lgkmcnt(14)\n\t"
      "v_mfma_f32_16x16x32_bf16 v[12:15], v[72:75], v[92:95], v[12:15]\n\t"
      "ds_read_b64_tr_b16 v[146:147], v181 offset:42112\n\t"
      "v_mfma_f32_16x16x32_bf16 v[48:51], v[76:79], v[92:95], v[48:51]\n\t"
      "ds_read_b64_tr_b16 v[148:149], v182 offset:41088\n\t"
      "ds_read_b64_tr_b16 v[150:151], v182 offset:42112\n\t"
      "v_mfma_f32_16x16x32_bf16 v[16:19], v[72:75], v[96:99], v[16:19]\n\t"
      "ds_read_b64_tr_b16 v[152:153], v183 offset:41088\n\t"
      "v_mfma_f32_16x16x32_bf16 v[164:167], v[172:175], v[96:99], v[164:167]\n\t"
      "ds_read_b64_tr_b16 v[154:155], v183 offset:42112\n\t"
      "v_mfma_f32_16x16x32_bf16 v[52:55], v[76:79], v[96:99], v[52:55]\n\t"
      "ds_read_b128 v[116:119], v177 offset:36864\n\t"
      "v_mfma_f32_16x16x32_bf16 v[20:23], v[72:75], v[100:103], v[20:23]\n\t"
      "ds_read_b128 v[120:123], v177 offset:38912\n\t"
      "v_mfma_f32_16x16x32_bf16 v[56:59], v[76:79], v[100:103], v[56:59]\n\t"
      "ds_read_b64_tr_b16 v[156:157], v184 offset:37888\n\t"
      "v_mfma_f32_16x16x32_bf16 v[24:27], v[72:75], v[104:107], v[24:27]\n\t"
      "ds_read_b64_tr_b16 v[158:159], v184 offset:38144\n\t"
      "v_mfma_f32_16x16x32_bf16 v[60:63], v[76:79], v[104:107], v[60:63]\n\t"
      "s_waitcnt lgkmcnt(14)\n\t"
      "v_mfma_f32_16x16x32_bf16 v[28:31], v[72:75], v[108:111], v[28:31]\n\t"
      "v_mfma_f32_16x16x32_bf16 v[64:67], v[76:79], v[108:111], v[64:67]\n\t"
      "v_mfma_f32_16x16x32_bf16 v[32:35], v[72:75], v[112:115], v[32:35]\n\t"
      "s_cmp_lg_u32 %[wm], 1\n\t"
      "s_cbranch_scc1 13f\n\t"
      "v_mfma_f32_16x16x32_bf16 v[168:171], v[172:175], v[112:115], v[168:171]\n\t"
      "13:\n\t"
      "v_mfma_f32_16x16x32_bf16 v[68:71], v[76:79], v[112:115], v[68:71]\n\t"
      "s_waitcnt lgkmcnt(0)\n\t"
      "s_barrier\n\t"
      "v_mfma_f32_16x16x32_bf16 v[0:3], v[116:119], v[124:127], v[0:3]\n\t"
      "ds_read_b128 v[72:75], v178 offset:0\n\t"
      "ds_read_b64_tr_b16 v[80:81], v185 offset:0\n\t"
      "v_mfma_f32_16x16x32_bf16 v[160:163], v[172:175], v[124:127], v[160:163]\n\t"
      "ds_read_b64_tr_b16 v[82:83], v185 offset:1024\n\t"
      "v_mfma_f32_16x16x32_bf16 v[36:39], v[120:123], v[124:127], v[36:39]\n\t"
      "ds_read_b128 v[76:79], v178 offset:2048\n\t"
      "v_mfma_f32_16x16x32_bf16 v[4:7], v[116:119], v[128:131], v[4:7]\n\t"
      "ds_read_b64_tr_b16 v[84:85], v186 offset:0\n\t"
      "v_mfma_f32_16x16x32_bf16 v[40:43], v[120:123], v[128:131], v[40:43]\n\t"
      "ds_read_b64_tr_b16 v[86:87], v186 offset:1024\n\t"
      "v_mfma_f32_16x16x32_bf16 v[8:11], v[116:119], v[132:135], v[8:11]\n\t"
      "ds_read_b64_tr_b16 v[88:89], v187 offset:0\n\t"
      "v_mfma_f32_16x16x32_bf16 v[44:47], v[120:123], v[132:135], v[44:47]\n\t"
      "ds_read_b64_tr_b16 v[90:91], v187 offset:1024\n\t"
      "v_mfma_f32_16x16x32_bf16 v[12:15], v[116:119], v[136:139], v[12:15]\n\t"
      "ds_read_b64_tr_b16 v[92:93], v188 offset:0\n\t"
      "ds_read_b64_tr_b16 v[94:95], v188 offset:1024\n\t"
      "v_mfma_f32_16x16x32_bf16 v[48:51], v[120:123], v[136:139], v[48:51]\n\t"
      "ds_read_b64_tr_b16 v[96:97], v185 offset:128\n\t"
      "v_mfma_f32_16x16x32_bf16 v[16:19], v[116:119], v[140:143], v[16:19]\n\t"
      "ds_read_b64_tr_b16 v[98:99], v185 offset:1152\n\t"
      "v_mfma_f32_16x16x32_bf16 v[164:167], v[172:175], v[140:143], v[164:167]\n\t"
      "ds_read_b64_tr_b16 v[100:101], v186 offset:128\n\t"
      "v_mfma_f32_16x16x32_bf16 v[52:55], v[120:123], v[140:143], v[52:55]\n\t"
      "ds_read_b64_tr_b16 v[102:103], v186 offset:1152\n\t"
      "v_mfma_f32_16x16x32_bf16 v[20:23], v[116:119], v[144:147], v[20:23]\n\t"
      "ds_read_b64_tr_b16 v[104:105], v187 offset:128\n\t"
      "v_mfma_f32_16x16x32_bf16 v[56:59], v[120:123], v[144:147], v[56:59]\n\t"
      "ds_read_b64_tr_b16 v[106:107], v187 offset:1152\n\t"
      "v_mfma_f32_16x16x32_bf16 v[24:27], v[116:119], v[148:151], v[24:27]\n\t"
      "ds_read_b64_tr_b16 v[108:109], v188 offset:128\n\t"
      "ds_read_b64_tr_b16 v[110:111], v188 offset:1152\n\t"
      "v_mfma_f32_16x16x32_bf16 v[60:63], v[120:123], v[148:151], v[60:63]\n\t"
      "ds_read_b64_tr_b16 v[112:113], v189 offset:0\n\t"
      "v_mfma_f32_16x16x32_bf16 v[28:31], v[116:119], v[152:155], v[28:31]\n\t"
      "ds_read_b64_tr_b16 v[114:115], v189 offset:256\n\t"
      "v_mfma_f32_16x16x32_bf16 v[64:67], v[120:123], v[152:155], v[64:67]\n\t"
      "ds_read_b64_tr_b16 v[124:125], v185 offset:4096\n\t"
      "v_mfma_f32_16x16x32_bf16 v[32:35], v[116:119], v[156:159], v[32:35]\n\t"
      "ds_read_b64_tr_b16 v[126:127], v185 offset:5120\n\t"
      "s_cmp_lg_u32 %[wm], 1\n\t"
      "s_cbranch_scc1 14f\n\t"
      "v_mfma_f32_16x16x32_bf16 v[168:171], v[172:175], v[156:159], v[168:171]\n\t"
      "14:\n\t"
      "v_mfma_f32_16x16x32_bf16 v[68:71], v[120:123], v[156:159], v[68:71]\n\t"
      "ds_read_b64_tr_b16 v[128:129], v186 offset:4096\n\t"
      "s_waitcnt lgkmcnt(14)\n\t"
      "v_mfma_f32_16x16x32_bf16 v[0:3], v[72:75], v[80:83], v[0:3]\n\t"
      "ds_read_b64_tr_b16 v[130:131], v186 offset:5120\n\t"
      "v_mfma_f32_16x16x32_bf16 v[160:163], v[172:175], v[80:83], v[160:163]\n\t"
      "ds_read_b64_tr_b16 v[132:133], v187 offset:4096\n\t"
      "ds_read_b64_tr_b16 v[134:135], v187 offset:5120\n\t"
      "v_mfma_f32_16x16x32_bf16 v[36:39], v[76:79], v[80:83], v[36:39]\n\t"
      "ds_read_b64_tr_b16 v[136:137], v188 offset:4096\n\t"
      "v_mfma_f32_16x16x32_bf16 v[4:7], v[72:75], v[84:87], v[4:7]\n\t"
      "ds_read_b64_tr_b16 v[138:139], v188 offset:5120\n\t"
      "v_mfma_f32_16x16x32_bf16 v[40:43], v[76:79], v[84:87], v[40:43]\n\t"
      "ds_read_b64_tr_b16 v[140:141], v185 offset:4224\n\t"
      "v_mfma_f32_16x16x32_bf16 v[8:11], v[72:75], v[88:91], v[8:11]\n\t"
      "ds_read_b64_tr_b16 v[142:143], v185 offset:5248\n\t"
      "v_mfma_f32_16x16x32_bf16 v[44:47], v[76:79], v[88:91], v[44:47]\n\t"
      "ds_read_b64_tr_b16 v[144:145], v186 offset:4224\n\t"
      "s_waitcnt lgkmcnt(14)\n\t"
      "v_mfma_f32_16x16x32_bf16 v[12:15], v[72:75], v[92:95], v[12:15]\n\t"
      "ds_read_b64_tr_b16 v[146:147], v186 offset:5248\n\t"
      "v_mfma_f32_16x16x32_bf16 v[48:51], v[76:79], v[92:95], v[48:51]\n\t"
      "ds_read_b64_tr_b16 v[148:149], v187 offset:4224\n\t"
      "ds_read_b64_tr_b16 v[150:151], v187 offset:5248\n\t"
      "v_mfma_f32_16x16x32_bf16 v[16:19], v[72:75], v[96:99], v[16:19]\n\t"
      "ds_read_b64_tr_b16 v[152:153], v188 offset:4224\n\t"
      "v_mfma_f32_16x16x32_bf16 v[164:167], v[172:175], v[96:99], v[164:167]\n\t"
      "ds_read_b64_tr_b16 v[154:155], v188 offset:5248\n\t"
      "v_mfma_f32_16x16x32_bf16 v[52:55], v[76:79], v[96:99], v[52:55]\n\t"
      "ds_read_b128 v[116:119], v179 offset:0\n\t"
      "v_mfma_f32_16x16x32_bf16 v[20:23], v[72:75], v[100:103], v[20:23]\n\t"
      "ds_read_b128 v[120:123], v179 offset:2048\n\t"
      "v_mfma_f32_16x16x32_bf16 v[56:59], v[76:79], v[100:103], v[56:59]\n\t"
      "ds_read_b64_tr_b16 v[156:157], v189 offset:1024\n\t"
      "v_mfma_f32_16x16x32_bf16 v[24:27], v[72:75], v[104:107], v[24:27]\n\t"
      "ds_read_b64_tr_b16 v[158:159], v189 offset:1280\n\t"
      "v_mfma_f32_16x16x32_bf16 v[60:63], v[76:79], v[104:107], v[60:63]\n\t"
      "s_waitcnt lgkmcnt(14)\n\t"
      "v_mfma_f32_16x16x32_bf16 v[28:31], v[72:75], v[108:111], v[28:31]\n\t"
      "v_mfma_f32_16x16x32_bf16 v[64:67], v[76:79], v[108:111], v[64:67]\n\t"
      "v_mfma_f32_16x16x32_bf16 v[32:35], v[72:75], v[112:115], v[32:35]\n\t"
      "s_cmp_lg_u32 %[wm], 2\n\t"
      "s_cbranch_scc1 15f\n\t"
      "v_mfma_f32_16x16x32_bf16 v[168:171], v[172:175], v[112:115], v[168:171]\n\t"
      "15:\n\t"
      "v_mfma_f32_16x16x32_bf16 v[68:71], v[76:79], v[112:115], v[68:71]\n\t"
      "s_waitcnt lgkmcnt(0)\n\t"
      "s_barrier\n\t"
      "v_mfma_f32_16x16x32_bf16 v[0:3], v[116:119], v[124:127], v[0:3]\n\t"
      "ds_read_b128 v[72:75], v178 offset:36864\n\t"
      "ds_read_b64_tr_b16 v[80:81], v185 offset:36864\n\t"
      "v_mfma_f32_16x16x32_bf16 v[160:163], v[172:175], v[124:127], v[160:163]\n\t"
      "ds_read_b64_tr_b16 v[82:83], v185 offset:37888\n\t"
      "v_mfma_f32_16x16x32_bf16 v[36:39], v[120:123], v[124:127], v[36:39]\n\t"
      "ds_read_b128 v[76:79], v178 offset:38912\n\t"
      "v_mfma_f32_16x16x32_bf16 v[4:7], v[116:119], v[128:131], v[4:7]\n\t"
      "ds_read_b64_tr_b16 v[84:85], v186 offset:36864\n\t"
      "v_mfma_f32_16x16x32_bf16 v[40:43], v[120:123], v[128:131], v[40:43]\n\t"
      "ds_read_b64_tr_b16 v[86:87], v186 offset:37888\n\t"
      "v_mfma_f32_16x16x32_bf16 v[8:11], v[116:119], v[132:135], v[8:11]\n\t"
      "ds_read_b64_tr_b16 v[88:89], v187 offset:36864\n\t"
      "v_mfma_f32_16x16x32_bf16 v[44:47], v[120:123], v[132:135], v[44:47]\n\t"
      "ds_read_b64_tr_b16 v[90:91], v187 offset:37888\n\t"
      "v_mfma_f32_16x16x32_bf16 v[12:15], v[116:119], v[136:139], v[12:15]\n\t"
      "ds_read_b64_tr_b16 v[92:93], v188 offset:36864\n\t"
      "ds_read_b64_tr_b16 v[94:95], v188 offset:37888\n\t"
      "v_mfma_f32_16x16x32_bf16 v[48:51], v[120:123], v[136:139], v[48:51]\n\t"
      "ds_read_b64_tr_b16 v[96:97], v185 offset:36992\n\t"
      "v_mfma_f32_16x16x32_bf16 v[16:19], v[116:119], v[140:143], v[16:19]\n\t"
      "ds_read_b64_tr_b16 v[98:99], v185 offset:38016\n\t"
      "v_mfma_f32_16x16x32_bf16 v[164:167], v[172:175], v[140:143], v[164:167]\n\t"
      "ds_read_b64_tr_b16 v[100:101], v186 offset:36992\n\t"
      "v_mfma_f32_16x16x32_bf16 v[52:55], v[120:123], v[140:143], v[52:55]\n\t"
      "ds_read_b64_tr_b16 v[102:103], v186 offset:38016\n\t"
      "v_mfma_f32_16x16x32_bf16 v[20:23], v[116:119], v[144:147], v[20:23]\n\t"
      "ds_read_b64_tr_b16 v[104:105], v187 offset:36992\n\t"
      "v_mfma_f32_16x16x32_bf16 v[56:59], v[120:123], v[144:147], v[56:59]\n\t"
      "ds_read_b64_tr_b16 v[106:107], v187 offset:38016\n\t"
      "v_mfma_f32_16x16x32_bf16 v[24:27], v[116:119], v[148:151], v[24:27]\n\t"
      "ds_read_b64_tr_b16 v[108:109], v188 offset:36992\n\t"
      "ds_read_b64_tr_b16 v[110:111], v188 offset:38016\n\t"
      "v_mfma_f32_16x16x32_bf16 v[60:63], v[120:123], v[148:151], v[60:63]\n\t"
      "ds_read_b64_tr_b16 v[112:113], v189 offset:36864\n\t"
      "v_mfma_f32_16x16x32_bf16 v[28:31], v[116:119], v[152:155], v[28:31]\n\t"
      "ds_read_b64_tr_b16 v[114:115], v189 offset:37120\n\t"
      "v_mfma_f32_16x16x32_bf16 v[64:67], v[120:123], v[152:155], v[64:67]\n\t"
      "ds_read_b64_tr_b16 v[124:125], v185 offset:40960\n\t"
      "v_mfma_f32_16x16x32_bf16 v[32:35], v[116:119], v[156:159], v[32:35]\n\t"
      "ds_read_b64_tr_b16 v[126:127], v185 offset:41984\n\t"
      "s_cmp_lg_u32 %[wm], 2\n\t"
      "s_cbranch_scc1 16f\n\t"
      "v_mfma_f32_16x16x32_bf16 v[168:171], v[172:175], v[156:159], v[168:171]\n\t"
      "16:\n\t"
      "v_mfma_f32_16x16x32_bf16 v[68:71], v[120:123], v[156:159], v[68:71]\n\t"
      "ds_read_b64_tr_b16 v[128:129], v186 offset:40960\n\t"
      "s_waitcnt lgkmcnt(14)\n\t"
      "v_mfma_f32_16x16x32_bf16 v[0:3], v[72:75], v[80:83], v[0:3]\n\t"
      "ds_read_b64_tr_b16 v[130:131], v186 offset:41984\n\t"
      "v_mfma_f32_16x16x32_bf16 v[160:163], v[172:175], v[80:83], v[160:163]\n\t"
      "ds_read_b64_tr_b16 v[132:133], v187 offset:40960\n\t"
      "ds_read_b64_tr_b16 v[134:135], v187 offset:41984\n\t"
      "v_mfma_f32_16x16x32_bf16 v[36:39], v[76:79], v[80:83], v[36:39]\n\t"
      "ds_read_b64_tr_b16 v[136:137], v188 offset:40960\n\t"
      "v_mfma_f32_16x16x32_bf16 v[4:7], v[72:75], v[84:87], v[4:7]\n\t"
      "ds_read_b64_tr_b16 v[138:139], v188 offset:41984\n\t"
      "v_mfma_f32_16x16x32_bf16 v[40:43], v[76:79], v[84:87], v[40:43]\n\t"
      "ds_read_b64_tr_b16 v[140:141], v185 offset:41088\n\t"
      "v_mfma_f32_16x16x32_bf16 v[8:11], v[72:75], v[88:91], v[8:11]\n\t"
      "ds_read_b64_tr_b16 v[142:143], v185 offset:42112\n\t"
      "v_mfma_f32_16x16x32_bf16 v[44:47], v[76:79], v[88:91], v[44:47]\n\t"
      "ds_read_b64_tr_b16 v[144:145], v186 offset:41088\n\t"
      "s_waitcnt lgkmcnt(14)\n\t"
      "v_mfma_f32_16x16x32_bf16 v[12:15], v[72:75], v[92:95], v[12:15]\n\t"
      "ds_read_b64_tr_b16 v[146:147], v186 offset:42112\n\t"
      "v_mfma_f32_16x16x32_bf16 v[48:51], v[76:79], v[92:95], v[48:51]\n\t"
      "ds_read_b64_tr_b16 v[148:149], v187 offset:41088\n\t"
      "ds_read_b64_tr_b16 v[150:151], v187 offset:42112\n\t"
      "v_mfma_f32_16x16x32_bf16 v[16:19], v[72:75], v[96:99], v[16:19]\n\t"
      "ds_read_b64_tr_b16 v[152:153], v188 offset:41088\n\t"
      "v_mfma_f32_16x16x32_bf16 v[164:167], v[172:175], v[96:99], v[164:167]\n\t"
      "ds_read_b64_tr_b16 v[154:155], v188 offset:42112\n\t"
      "v_mfma_f32_16x16x32_bf16 v[52:55], v[76:79], v[96:99], v[52:55]\n\t"
      "ds_read_b128 v[116:119], v179 offset:36864\n\t"
      "v_mfma_f32_16x16x32_bf16 v[20:23], v[72:75], v[100:103], v[20:23]\n\t"
      "ds_read_b128 v[120:123], v179 offset:38912\n\t"
      "v_mfma_f32_16x16x32_bf16 v[56:59], v[76:79], v[100:103], v[56:59]\n\t"
      "ds_read_b64_tr_b16 v[156:157], v189 offset:37888\n\t"
      "v_mfma_f32_16x16x32_bf16 v[24:27], v[72:75], v[104:107], v[24:27]\n\t"
      "ds_read_b64_tr_b16 v[158:159], v189 offset:38144\n\t"
      "v_mfma_f32_16x16x32_bf16 v[60:63], v[76:79], v[104:107], v[60:63]\n\t"
      "s_waitcnt lgkmcnt(14)\n\t"
      "v_mfma_f32_16x16x32_bf16 v[28:31], v[72:75], v[108:111], v[28:31]\n\t"
      "v_mfma_f32_16x16x32_bf16 v[64:67], v[76:79], v[108:111], v[64:67]\n\t"
      "v_mfma_f32_16x16x32_bf16 v[32:35], v[72:75], v[112:115], v[32:35]\n\t"
      "s_cmp_lg_u32 %[wm], 3\n\t"
      "s_cbranch_scc1 17f\n\t"
      "v_mfma_f32_16x16x32_bf16 v[168:171], v[172:175], v[112:115], v[168:171]\n\t"
      "17:\n\t"
      "v_mfma_f32_16x16x32_bf16 v[68:71], v[76:79], v[112:115], v[68:71]\n\t"
      "s_waitcnt lgkmcnt(0)\n\t"
      "s_barrier\n\t"
      "v_mfma_f32_16x16x32_bf16 v[0:3], v[116:119], v[124:127], v[0:3]\n\t"
      "ds_read_b128 v[72:75], v176 offset:0\n\t"
      "ds_read_b64_tr_b16 v[80:81], v180 offset:0\n\t"
      "v_mfma_f32_16x16x32_bf16 v[160:163], v[172:175], v[124:127], v[160:163]\n\t"
      "ds_read_b64_tr_b16 v[82:83], v180 offset:1024\n\t"
      "v_mfma_f32_16x16x32_bf16 v[36:39], v[120:123], v[124:127], v[36:39]\n\t"
      "ds_read_b128 v[76:79], v176 offset:2048\n\t"
      "v_mfma_f32_16x16x32_bf16 v[4:7], v[116:119], v[128:131], v[4:7]\n\t"
      "ds_read_b64_tr_b16 v[84:85], v181 offset:0\n\t"
      "v_mfma_f32_16x16x32_bf16 v[40:43], v[120:123], v[128:131], v[40:43]\n\t"
      "ds_read_b64_tr_b16 v[86:87], v181 offset:1024\n\t"
      "v_mfma_f32_16x16x32_bf16 v[8:11], v[116:119], v[132:135], v[8:11]\n\t"
      "ds_read_b64_tr_b16 v[88:89], v182 offset:0\n\t"
      "v_mfma_f32_16x16x32_bf16 v[44:47], v[120:123], v[132:135], v[44:47]\n\t"
      "ds_read_b64_tr_b16 v[90:91], v182 offset:1024\n\t"
      "v_mfma_f32_16x16x32_bf16 v[12:15], v[116:119], v[136:139], v[12:15]\n\t"
      "ds_read_b64_tr_b16 v[92:93], v183 offset:0\n\t"
      "ds_read_b64_tr_b16 v[94:95], v183 offset:1024\n\t"
      "v_mfma_f32_16x16x32_bf16 v[48:51], v[120:123], v[136:139], v[48:51]\n\t"
      "ds_read_b64_tr_b16 v[96:97], v180 offset:128\n\t"
      "v_mfma_f32_16x16x32_bf16 v[16:19], v[116:119], v[140:143], v[16:19]\n\t"
      "ds_read_b64_tr_b16 v[98:99], v180 offset:1152\n\t"
      "v_mfma_f32_16x16x32_bf16 v[164:167], v[172:175], v[140:143], v[164:167]\n\t"
      "ds_read_b64_tr_b16 v[100:101], v181 offset:128\n\t"
      "v_mfma_f32_16x16x32_bf16 v[52:55], v[120:123], v[140:143], v[52:55]\n\t"
      "ds_read_b64_tr_b16 v[102:103], v181 offset:1152\n\t"
      "v_mfma_f32_16x16x32_bf16 v[20:23], v[116:119], v[144:147], v[20:23]\n\t"
      "ds_read_b64_tr_b16 v[104:105], v182 offset:128\n\t"
      "v_mfma_f32_16x16x32_bf16 v[56:59], v[120:123], v[144:147], v[56:59]\n\t"
      "ds_read_b64_tr_b16 v[106:107], v182 offset:1152\n\t"
      "v_mfma_f32_16x16x32_bf16 v[24:27], v[116:119], v[148:151], v[24:27]\n\t"
      "ds_read_b64_tr_b16 v[108:109], v183 offset:128\n\t"
      "ds_read_b64_tr_b16 v[110:111], v183 offset:1152\n\t"
      "v_mfma_f32_16x16x32_bf16 v[60:63], v[120:123], v[148:151], v[60:63]\n\t"
      "ds_read_b64_tr_b16 v[112:113], v184 offset:0\n\t"
      "v_mfma_f32_16x16x32_bf16 v[28:31], v[116:119], v[152:155], v[28:31]\n\t"
      "ds_read_b64_tr_b16 v[114:115], v184 offset:256\n\t"
      "v_mfma_f32_16x16x32_bf16 v[64:67], v[120:123], v[152:155], v[64:67]\n\t"
      "ds_read_b64_tr_b16 v[124:125], v180 offset:4096\n\t"
      "v_mfma_f32_16x16x32_bf16 v[32:35], v[116:119], v[156:159], v[32:35]\n\t"
      "ds_read_b64_tr_b16 v[126:127], v180 offset:5120\n\t"
      "s_cmp_lg_u32 %[wm], 3\n\t"
      "s_cbranch_scc1 18f\n\t"
      "v_mfma_f32_16x16x32_bf16 v[168:171], v[172:175], v[156:159], v[168:171]\n\t"
      "18:\n\t"
      "v_mfma_f32_16x16x32_bf16 v[68:71], v[120:123], v[156:159], v[68:71]\n\t"
      "ds_read_b64_tr_b16 v[128:129], v181 offset:4096\n\t"
      "s_waitcnt lgkmcnt(14)\n\t"
      "v_mfma_f32_16x16x32_bf16 v[0:3], v[72:75], v[80:83], v[0:3]\n\t"
      "ds_read_b64_tr_b16 v[130:131], v181 offset:5120\n\t"
      "v_mfma_f32_16x16x32_bf16 v[160:163], v[172:175], v[80:83], v[160:163]\n\t"
      "ds_read_b64_tr_b16 v[132:133], v182 offset:4096\n\t"
      "ds_read_b64_tr_b16 v[134:135], v182 offset:5120\n\t"
      "v_mfma_f32_16x16x32_bf16 v[36:39], v[76:79], v[80:83], v[36:39]\n\t"
      "ds_read_b64_tr_b16 v[136:137], v183 offset:4096\n\t"
      "v_mfma_f32_16x16x32_bf16 v[4:7], v[72:75], v[84:87], v[4:7]\n\t"
      "ds_read_b64_tr_b16 v[138:139], v183 offset:5120\n\t"
      "v_mfma_f32_16x16x32_bf16 v[40:43], v[76:79], v[84:87], v[40:43]\n\t"
      "ds_read_b64_tr_b16 v[140:141], v180 offset:4224\n\t"
      "v_mfma_f32_16x16x32_bf16 v[8:11], v[72:75], v[88:91], v[8:11]\n\t"
      "ds_read_b64_tr_b16 v[142:143], v180 offset:5248\n\t"
      "v_mfma_f32_16x16x32_bf16 v[44:47], v[76:79], v[88:91], v[44:47]\n\t"
      "ds_read_b64_tr_b16 v[144:145], v181 offset:4224\n\t"
      "s_waitcnt lgkmcnt(14)\n\t"
      "v_mfma_f32_16x16x32_bf16 v[12:15], v[72:75], v[92:95], v[12:15]\n\t"
      "ds_read_b64_tr_b16 v[146:147], v181 offset:5248\n\t"
      "v_mfma_f32_16x16x32_bf16 v[48:51], v[76:79], v[92:95], v[48:51]\n\t"
      "ds_read_b64_tr_b16 v[148:149], v182 offset:4224\n\t"
      "ds_read_b64_tr_b16 v[150:151], v182 offset:5248\n\t"
      "v_mfma_f32_16x16x32_bf16 v[16:19], v[72:75], v[96:99], v[16:19]\n\t"
      "ds_read_b64_tr_b16 v[152:153], v183 offset:4224\n\t"
      "v_mfma_f32_16x16x32_bf16 v[164:167], v[172:175], v[96:99], v[164:167]\n\t"
      "ds_read_b64_tr_b16 v[154:155], v183 offset:5248\n\t"
      "v_mfma_f32_16x16x32_bf16 v[52:55], v[76:79], v[96:99], v[52:55]\n\t"
      "ds_read_b128 v[116:119], v177 offset:0\n\t"
      "v_mfma_f32_16x16x32_bf16 v[20:23], v[72:75], v[100:103], v[20:23]\n\t"
      "ds_read_b128 v[120:123], v177 offset:2048\n\t"
      "v_mfma_f32_16x16x32_bf16 v[56:59], v[76:79], v[100:103], v[56:59]\n\t"
      "ds_read_b64_tr_b16 v[156:157], v184 offset:1024\n\t"
      "v_mfma_f32_16x16x32_bf16 v[24:27], v[72:75], v[104:107], v[24:27]\n\t"
      "ds_read_b64_tr_b16 v[158:159], v184 offset:1280\n\t"
      "v_mfma_f32_16x16x32_bf16 v[60:63], v[76:79], v[104:107], v[60:63]\n\t"
      "s_waitcnt lgkmcnt(14)\n\t"
      "v_mfma_f32_16x16x32_bf16 v[28:31], v[72:75], v[108:111], v[28:31]\n\t"
      "v_mfma_f32_16x16x32_bf16 v[64:67], v[76:79], v[108:111], v[64:67]\n\t"
      "v_mfma_f32_16x16x32_bf16 v[32:35], v[72:75], v[112:115], v[32:35]\n\t"
      "s_cmp_lg_u32 %[wm], 0\n\t"
      "s_cbranch_scc1 19f\n\t"
      "v_mfma_f32_16x16x32_bf16 v[168:171], v[172:175], v[112:115], v[168:171]\n\t"
      "19:\n\t"
      "v_mfma_f32_16x16x32_bf16 v[68:71], v[76:79], v[112:115], v[68:71]\n\t"
      "s_waitcnt lgkmcnt(0)\n\t"
      "s_barrier\n\t"
      "v_mfma_f32_16x16x32_bf16 v[0:3], v[116:119], v[124:127], v[0:3]\n\t"
      "ds_read_b128 v[72:75], v176 offset:36864\n\t"
      "ds_read_b64_tr_b16 v[80:81], v180 offset:36864\n\t"
      "v_mfma_f32_16x16x32_bf16 v[160:163], v[172:175], v[124:127], v[160:163]\n\t"
      "ds_read_b64_tr_b16 v[82:83], v180 offset:37888\n\t"
      "v_mfma_f32_16x16x32_bf16 v[36:39], v[120:123], v[124:127], v[36:39]\n\t"
      "ds_read_b128 v[76:79], v176 offset:38912\n\t"
      "v_mfma_f32_16x16x32_bf16 v[4:7], v[116:119], v[128:131], v[4:7]\n\t"
      "ds_read_b64_tr_b16 v[84:85], v181 offset:36864\n\t"
      "v_mfma_f32_16x16x32_bf16 v[40:43], v[120:123], v[128:131], v[40:43]\n\t"
      "ds_read_b64_tr_b16 v[86:87], v181 offset:37888\n\t"
      "v_mfma_f32_16x16x32_bf16 v[8:11], v[116:119], v[132:135], v[8:11]\n\t"
      "ds_read_b64_tr_b16 v[88:89], v182 offset:36864\n\t"
      "v_mfma_f32_16x16x32_bf16 v[44:47], v[120:123], v[132:135], v[44:47]\n\t"
      "ds_read_b64_tr_b16 v[90:91], v182 offset:37888\n\t"
      "v_mfma_f32_16x16x32_bf16 v[12:15], v[116:119], v[136:139], v[12:15]\n\t"
      "ds_read_b64_tr_b16 v[92:93], v183 offset:36864\n\t"
      "ds_read_b64_tr_b16 v[94:95], v183 offset:37888\n\t"
      "v_mfma_f32_16x16x32_bf16 v[48:51], v[120:123], v[136:139], v[48:51]\n\t"
      "ds_read_b64_tr_b16 v[96:97], v180 offset:36992\n\t"
      "v_mfma_f32_16x16x32_bf16 v[16:19], v[116:119], v[140:143], v[16:19]\n\t"
      "ds_read_b64_tr_b16 v[98:99], v180 offset:38016\n\t"
      "v_mfma_f32_16x16x32_bf16 v[164:167], v[172:175], v[140:143], v[164:167]\n\t"
      "ds_read_b64_tr_b16 v[100:101], v181 offset:36992\n\t"
      "v_mfma_f32_16x16x32_bf16 v[52:55], v[120:123], v[140:143], v[52:55]\n\t"
      "ds_read_b64_tr_b16 v[102:103], v181 offset:38016\n\t"
      "v_mfma_f32_16x16x32_bf16 v[20:23], v[116:119], v[144:147], v[20:23]\n\t"
      "ds_read_b64_tr_b16 v[104:105], v182 offset:36992\n\t"
      "v_mfma_f32_16x16x32_bf16 v[56:59], v[120:123], v[144:147], v[56:59]\n\t"
      "ds_read_b64_tr_b16 v[106:107], v182 offset:38016\n\t"
      "v_mfma_f32_16x16x32_bf16 v[24:27], v[116:119], v[148:151], v[24:27]\n\t"
      "ds_read_b64_tr_b16 v[108:109], v183 offset:36992\n\t"
      "ds_read_b64_tr_b16 v[110:111], v183 offset:38016\n\t"
      "v_mfma_f32_16x16x32_bf16 v[60:63], v[120:123], v[148:151], v[60:63]\n\t"
      "ds_read_b64_tr_b16 v[112:113], v184 offset:36864\n\t"
      "v_mfma_f32_16x16x32_bf16 v[28:31], v[116:119], v[152:155], v[28:31]\n\t"
      "ds_read_b64_tr_b16 v[114:115], v184 offset:37120\n\t"
      "v_mfma_f32_16x16x32_bf16 v[64:67], v[120:123], v[152:155], v[64:67]\n\t"
      "ds_read_b64_tr_b16 v[124:125], v180 offset:40960\n\t"
      "v_mfma_f32_16x16x32_bf16 v[32:35], v[116:119], v[156:159], v[32:35]\n\t"
      "ds_read_b64_tr_b16 v[126:127], v180 offset:41984\n\t"
      "s_cmp_lg_u32 %[wm], 0\n\t"
      "s_cbranch_scc1 20f\n\t"
      "v_mfma_f32_16x16x32_bf16 v[168:171], v[172:175], v[156:159], v[168:171]\n\t"
      "20:\n\t"
      "v_mfma_f32_16x16x32_bf16 v[68:71], v[120:123], v[156:159], v[68:71]\n\t"
      "ds_read_b64_tr_b16 v[128:129], v181 offset:40960\n\t"
      "s_waitcnt lgkmcnt(14)\n\t"
      "v_mfma_f32_16x16x32_bf16 v[0:3], v[72:75], v[80:83], v[0:3]\n\t"
      "ds_read_b64_tr_b16 v[130:131], v181 offset:41984\n\t"
      "v_mfma_f32_16x16x32_bf16 v[160:163], v[172:175], v[80:83], v[160:163]\n\t"
      "ds_read_b64_tr_b16 v[132:133], v182 offset:40960\n\t"
      "ds_read_b64_tr_b16 v[134:135], v182 offset:41984\n\t"
      "v_mfma_f32_16x16x32_bf16 v[36:39], v[76:79], v[80:83], v[36:39]\n\t"
      "ds_read_b64_tr_b16 v[136:137], v183 offset:40960\n\t"
      "v_mfma_f32_16x16x32_bf16 v[4:7], v[72:75], v[84:87], v[4:7]\n\t"
      "ds_read_b64_tr_b16 v[138:139], v183 offset:41984\n\t"
      "v_mfma_f32_16x16x32_bf16 v[40:43], v[76:79], v[84:87], v[40:43]\n\t"
      "ds_read_b64_tr_b16 v[140:141], v180 offset:41088\n\t"
      "v_mfma_f32_16x16x32_bf16 v[8:11], v[72:75], v[88:91], v[8:11]\n\t"
      "ds_read_b64_tr_b16 v[142:143], v180 offset:42112\n\t"
      "v_mfma_f32_16x16x32_bf16 v[44:47], v[76:79], v[88:91], v[44:47]\n\t"
      "ds_read_b64_tr_b16 v[144:145], v181 offset:41088\n\t"
      "s_waitcnt lgkmcnt(14)\n\t"
      "v_mfma_f32_16x16x32_bf16 v[12:15], v[72:75], v[92:95], v[12:15]\n\t"
      "ds_read_b64_tr_b16 v[146:147], v181 offset:42112\n\t"
      "v_mfma_f32_16x16x32_bf16 v[48:51], v[76:79], v[92:95], v[48:51]\n\t"
      "ds_read_b64_tr_b16 v[148:149], v182 offset:41088\n\t"
      "ds_read_b64_tr_b16 v[150:151], v182 offset:42112\n\t"
      "v_mfma_f32_16x16x32_bf16 v[16:19], v[72:75], v[96:99], v[16:19]\n\t"
      "ds_read_b64_tr_b16 v[152:153], v183 offset:41088\n\t"
      "v_mfma_f32_16x16x32_bf16 v[164:167], v[172:175], v[96:99], v[164:167]\n\t"
      "ds_read_b64_tr_b16 v[154:155], v183 offset:42112\n\t"
      "v_mfma_f32_16x16x32_bf16 v[52:55], v[76:79], v[96:99], v[52:55]\n\t"
      "ds_read_b128 v[116:119], v177 offset:36864\n\t"
      "v_mfma_f32_16x16x32_bf16 v[20:23], v[72:75], v[100:103], v[20:23]\n\t"
      "ds_read_b128 v[120:123], v177 offset:38912\n\t"
      "v_mfma_f32_16x16x32_bf16 v[56:59], v[76:79], v[100:103], v[56:59]\n\t"
      "ds_read_b64_tr_b16 v[156:157], v184 offset:37888\n\t"
      "v_mfma_f32_16x16x32_bf16 v[24:27], v[72:75], v[104:107], v[24:27]\n\t"
      "ds_read_b64_tr_b16 v[158:159], v184 offset:38144\n\t"
      "v_mfma_f32_16x16x32_bf16 v[60:63], v[76:79], v[104:107], v[60:63]\n\t"
      "s_waitcnt lgkmcnt(14)\n\t"
      "v_mfma_f32_16x16x32_bf16 v[28:31], v[72:75], v[108:111], v[28:31]\n\t"
      "v_mfma_f32_16x16x32_bf16 v[64:67], v[76:79], v[108:111], v[64:67]\n\t"
      "v_mfma_f32_16x16x32_bf16 v[32:35], v[72:75], v[112:115], v[32:35]\n\t"
      "s_cmp_lg_u32 %[wm], 1\n\t"
      "s_cbranch_scc1 21f\n\t"
      "v_mfma_f32_16x16x32_bf16 v[168:171], v[172:175], v[112:115], v[168:171]\n\t"
      "21:\n\t"
      "v_mfma_f32_16x16x32_bf16 v[68:71], v[76:79], v[112:115], v[68:71]\n\t"
      "s_waitcnt lgkmcnt(0)\n\t"
      "s_barrier\n\t"
      "v_mfma_f32_16x16x32_bf16 v[0:3], v[116:119], v[124:127], v[0:3]\n\t"
      "ds_read_b128 v[72:75], v178 offset:0\n\t"
      "ds_read_b64_tr_b16 v[80:81], v185 offset:0\n\t"
      "v_mfma_f32_16x16x32_bf16 v[160:163], v[172:175], v[124:127], v[160:163]\n\t"
      "ds_read_b64_tr_b16 v[82:83], v185 offset:1024\n\t"
      "v_mfma_f32_16x16x32_bf16 v[36:39], v[120:123], v[124:127], v[36:39]\n\t"
      "ds_read_b128 v[76:79], v178 offset:2048\n\t"
      "v_mfma_f32_16x16x32_bf16 v[4:7], v[116:119], v[128:131], v[4:7]\n\t"
      "ds_read_b64_tr_b16 v[84:85], v186 offset:0\n\t"
      "v_mfma_f32_16x16x32_bf16 v[40:43], v[120:123], v[128:131], v[40:43]\n\t"
      "ds_read_b64_tr_b16 v[86:87], v186 offset:1024\n\t"
      "v_mfma_f32_16x16x32_bf16 v[8:11], v[116:119], v[132:135], v[8:11]\n\t"
      "ds_read_b64_tr_b16 v[88:89], v187 offset:0\n\t"
      "v_mfma_f32_16x16x32_bf16 v[44:47], v[120:123], v[132:135], v[44:47]\n\t"
      "ds_read_b64_tr_b16 v[90:91], v187 offset:1024\n\t"
      "v_mfma_f32_16x16x32_bf16 v[12:15], v[116:119], v[136:139], v[12:15]\n\t"
      "ds_read_b64_tr_b16 v[92:93], v188 offset:0\n\t"
      "ds_read_b64_tr_b16 v[94:95], v188 offset:1024\n\t"
      "v_mfma_f32_16x16x32_bf16 v[48:51], v[120:123], v[136:139], v[48:51]\n\t"
      "ds_read_b64_tr_b16 v[96:97], v185 offset:128\n\t"
      "v_mfma_f32_16x16x32_bf16 v[16:19], v[116:119], v[140:143], v[16:19]\n\t"
      "ds_read_b64_tr_b16 v[98:99], v185 offset:1152\n\t"
      "v_mfma_f32_16x16x32_bf16 v[164:167], v[172:175], v[140:143], v[164:167]\n\t"
      "ds_read_b64_tr_b16 v[100:101], v186 offset:128\n\t"
      "v_mfma_f32_16x16x32_bf16 v[52:55], v[120:123], v[140:143], v[52:55]\n\t"
      "ds_read_b64_tr_b16 v[102:103], v186 offset:1152\n\t"
      "v_mfma_f32_16x16x32_bf16 v[20:23], v[116:119], v[144:147], v[20:23]\n\t"
      "ds_read_b64_tr_b16 v[104:105], v187 offset:128\n\t"
      "v_mfma_f32_16x16x32_bf16 v[56:59], v[120:123], v[144:147], v[56:59]\n\t"
      "ds_read_b64_tr_b16 v[106:107], v187 offset:1152\n\t"
      "v_mfma_f32_16x16x32_bf16 v[24:27], v[116:119], v[148:151], v[24:27]\n\t"
      "ds_read_b64_tr_b16 v[108:109], v188 offset:128\n\t"
      "ds_read_b64_tr_b16 v[110:111], v188 offset:1152\n\t"
      "v_mfma_f32_16x16x32_bf16 v[60:63], v[120:123], v[148:151], v[60:63]\n\t"
      "ds_read_b64_tr_b16 v[112:113], v189 offset:0\n\t"
      "v_mfma_f32_16x16x32_bf16 v[28:31], v[116:119], v[152:155], v[28:31]\n\t"
      "ds_read_b64_tr_b16 v[114:115], v189 offset:256\n\t"
      "v_mfma_f32_16x16x32_bf16 v[64:67], v[120:123], v[152:155], v[64:67]\n\t"
      "ds_read_b64_tr_b16 v[124:125], v185 offset:4096\n\t"
      "v_mfma_f32_16x16x32_bf16 v[32:35], v[116:119], v[156:159], v[32:35]\n\t"
      "ds_read_b64_tr_b16 v[126:127], v185 offset:5120\n\t"
      "s_cmp_lg_u32 %[wm], 1\n\t"
      "s_cbranch_scc1 22f\n\t"
      "v_mfma_f32_16x16x32_bf16 v[168:171], v[172:175], v[156:159], v[168:171]\n\t"
      "22:\n\t"
      "v_mfma_f32_16x16x32_bf16 v[68:71], v[120:123], v[156:159], v[68:71]\n\t"
      "ds_read_b64_tr_b16 v[128:129], v186 offset:4096\n\t"
      "s_waitcnt lgkmcnt(14)\n\t"
      "v_mfma_f32_16x16x32_bf16 v[0:3], v[72:75], v[80:83], v[0:3]\n\t"
      "ds_read_b64_tr_b16 v[130:131], v186 offset:5120\n\t"
      "v_mfma_f32_16x16x32_bf16 v[160:163], v[172:175], v[80:83], v[160:163]\n\t"
      "ds_read_b64_tr_b16 v[132:133], v187 offset:4096\n\t"
      "ds_read_b64_tr_b16 v[134:135], v187 offset:5120\n\t"
      "v_mfma_f32_16x16x32_bf16 v[36:39], v[76:79], v[80:83], v[36:39]\n\t"
      "ds_read_b64_tr_b16 v[136:137], v188 offset:4096\n\t"
      "v_mfma_f32_16x16x32_bf16 v[4:7], v[72:75], v[84:87], v[4:7]\n\t"
      "ds_read_b64_tr_b16 v[138:139], v188 offset:5120\n\t"
      "v_mfma_f32_16x16x32_bf16 v[40:43], v[76:79], v[84:87], v[40:43]\n\t"
      "ds_read_b64_tr_b16 v[140:141], v185 offset:4224\n\t"
      "v_mfma_f32_16x16x32_bf16 v[8:11], v[72:75], v[88:91], v[8:11]\n\t"
      "ds_read_b64_tr_b16 v[142:143], v185 offset:5248\n\t"
      "v_mfma_f32_16x16x32_bf16 v[44:47], v[76:79], v[88:91], v[44:47]\n\t"
      "ds_read_b64_tr_b16 v[144:145], v186 offset:4224\n\t"
      "s_waitcnt lgkmcnt(14)\n\t"
      "v_mfma_f32_16x16x32_bf16 v[12:15], v[72:75], v[92:95], v[12:15]\n\t"
      "ds_read_b64_tr_b16 v[146:147], v186 offset:5248\n\t"
      "v_mfma_f32_16x16x32_bf16 v[48:51], v[76:79], v[92:95], v[48:51]\n\t"
      "ds_read_b64_tr_b16 v[148:149], v187 offset:4224\n\t"
      "ds_read_b64_tr_b16 v[150:151], v187 offset:5248\n\t"
      "v_mfma_f32_16x16x32_bf16 v[16:19], v[72:75], v[96:99], v[16:19]\n\t"
      "ds_read_b64_tr_b16 v[152:153], v188 offset:4224\n\t"
      "v_mfma_f32_16x16x32_bf16 v[164:167], v[172:175], v[96:99], v[164:167]\n\t"
      "ds_read_b64_tr_b16 v[154:155], v188 offset:5248\n\t"
      "v_mfma_f32_16x16x32_bf16 v[52:55], v[76:79], v[96:99], v[52:55]\n\t"
      "ds_read_b128 v[116:119], v179 offset:0\n\t"
      "v_mfma_f32_16x16x32_bf16 v[20:23], v[72:75], v[100:103], v[20:23]\n\t"
      "ds_read_b128 v[120:123], v179 offset:2048\n\t"
      "v_mfma_f32_16x16x32_bf16 v[56:59], v[76:79], v[100:103], v[56:59]\n\t"
      "ds_read_b64_tr_b16 v[156:157], v189 offset:1024\n\t"
      "v_mfma_f32_16x16x32_bf16 v[24:27], v[72:75], v[104:107], v[24:27]\n\t"
      "ds_read_b64_tr_b16 v[158:159], v189 offset:1280\n\t"
      "v_mfma_f32_16x16x32_bf16 v[60:63], v[76:79], v[104:107], v[60:63]\n\t"
      "s_waitcnt lgkmcnt(14)\n\t"
      "v_mfma_f32_16x16x32_bf16 v[28:31], v[72:75], v[108:111], v[28:31]\n\t"
      "v_mfma_f32_16x16x32_bf16 v[64:67], v[76:79], v[108:111], v[64:67]\n\t"
      "v_mfma_f32_16x16x32_bf16 v[32:35], v[72:75], v[112:115], v[32:35]\n\t"
      "s_cmp_lg_u32 %[wm], 2\n\t"
      "s_cbranch_scc1 23f\n\t"
      "v_mfma_f32_16x16x32_bf16 v[168:171], v[172:175], v[112:115], v[168:171]\n\t"
      "23:\n\t"
      "v_mfma_f32_16x16x32_bf16 v[68:71], v[76:79], v[112:115], v[68:71]\n\t"
      "s_waitcnt lgkmcnt(0)\n\t"
      "s_barrier\n\t"
      "v_mfma_f32_16x16x32_bf16 v[0:3], v[116:119], v[124:127], v[0:3]\n\t"
      "ds_read_b128 v[72:75], v178 offset:36864\n\t"
      "ds_read_b64_tr_b16 v[80:81], v185 offset:36864\n\t"
      "v_mfma_f32_16x16x32_bf16 v[160:163], v[172:175], v[124:127], v[160:163]\n\t"
      "ds_read_b64_tr_b16 v[82:83], v185 offset:37888\n\t"
      "v_mfma_f32_16x16x32_bf16 v[36:39], v[120:123], v[124:127], v[36:39]\n\t"
      "ds_read_b128 v[76:79], v178 offset:38912\n\t"
      "v_mfma_f32_16x16x32_bf16 v[4:7], v[116:119], v[128:131], v[4:7]\n\t"
      "ds_read_b64_tr_b16 v[84:85], v186 offset:36864\n\t"
      "v_mfma_f32_16x16x32_bf16 v[40:43], v[120:123], v[128:131], v[40:43]\n\t"
      "ds_read_b64_tr_b16 v[86:87], v186 offset:37888\n\t"
      "v_mfma_f32_16x16x32_bf16 v[8:11], v[116:119], v[132:135], v[8:11]\n\t"
      "ds_read_b64_tr_b16 v[88:89], v187 offset:36864\n\t"
      "v_mfma_f32_16x16x32_bf16 v[44:47], v[120:123], v[132:135], v[44:47]\n\t"
      "ds_read_b64_tr_b16 v[90:91], v187 offset:37888\n\t"
      "v_mfma_f32_16x16x32_bf16 v[12:15], v[116:119], v[136:139], v[12:15]\n\t"
      "ds_read_b64_tr_b16 v[92:93], v188 offset:36864\n\t"
      "ds_read_b64_tr_b16 v[94:95], v188 offset:37888\n\t"
      "v_mfma_f32_16x16x32_bf16 v[48:51], v[120:123], v[136:139], v[48:51]\n\t"
      "ds_read_b64_tr_b16 v[96:97], v185 offset:36992\n\t"
      "v_mfma_f32_16x16x32_bf16 v[16:19], v[116:119], v[140:143], v[16:19]\n\t"
      "ds_read_b64_tr_b16 v[98:99], v185 offset:38016\n\t"
      "v_mfma_f32_16x16x32_bf16 v[164:167], v[172:175], v[140:143], v[164:167]\n\t"
      "ds_read_b64_tr_b16 v[100:101], v186 offset:36992\n\t"
      "v_mfma_f32_16x16x32_bf16 v[52:55], v[120:123], v[140:143], v[52:55]\n\t"
      "ds_read_b64_tr_b16 v[102:103], v186 offset:38016\n\t"
      "v_mfma_f32_16x16x32_bf16 v[20:23], v[116:119], v[144:147], v[20:23]\n\t"
      "ds_read_b64_tr_b16 v[104:105], v187 offset:36992\n\t"
      "v_mfma_f32_16x16x32_bf16 v[56:59], v[120:123], v[144:147], v[56:59]\n\t"
      "ds_read_b64_tr_b16 v[106:107], v187 offset:38016\n\t"
      "v_mfma_f32_16x16x32_bf16 v[24:27], v[116:119], v[148:151], v[24:27]\n\t"
      "ds_read_b64_tr_b16 v[108:109], v188 offset:36992\n\t"
      "ds_read_b64_tr_b16 v[110:111], v188 offset:38016\n\t"
      "v_mfma_f32_16x16x32_bf16 v[60:63], v[120:123], v[148:151], v[60:63]\n\t"
      "ds_read_b64_tr_b16 v[112:113], v189 offset:36864\n\t"
      "v_mfma_f32_16x16x32_bf16 v[28:31], v[116:119], v[152:155], v[28:31]\n\t"
      "ds_read_b64_tr_b16 v[114:115], v189 offset:37120\n\t"
      "v_mfma_f32_16x16x32_bf16 v[64:67], v[120:123], v[152:155], v[64:67]\n\t"
      "ds_read_b64_tr_b16 v[124:125], v185 offset:40960\n\t"
      "v_mfma_f32_16x16x32_bf16 v[32:35], v[116:119], v[156:159], v[32:35]\n\t"
      "ds_read_b64_tr_b16 v[126:127], v185 offset:41984\n\t"
      "s_cmp_lg_u32 %[wm], 2\n\t"
      "s_cbranch_scc1 24f\n\t"
      "v_mfma_f32_16x16x32_bf16 v[168:171], v[172:175], v[156:159], v[168:171]\n\t"
      "24:\n\t"
      "v_mfma_f32_16x16x32_bf16 v[68:71], v[120:123], v[156:159], v[68:71]\n\t"
      "ds_read_b64_tr_b16 v[128:129], v186 offset:40960\n\t"
      "s_waitcnt lgkmcnt(14)\n\t"
      "v_mfma_f32_16x16x32_bf16 v[0:3], v[72:75], v[80:83], v[0:3]\n\t"
      "ds_read_b64_tr_b16 v[130:131], v186 offset:41984\n\t"
      "v_mfma_f32_16x16x32_bf16 v[160:163], v[172:175], v[80:83], v[160:163]\n\t"
      "ds_read_b64_tr_b16 v[132:133], v187 offset:40960\n\t"
      "ds_read_b64_tr_b16 v[134:135], v187 offset:41984\n\t"
      "v_mfma_f32_16x16x32_bf16 v[36:39], v[76:79], v[80:83], v[36:39]\n\t"
      "ds_read_b64_tr_b16 v[136:137], v188 offset:40960\n\t"
      "v_mfma_f32_16x16x32_bf16 v[4:7], v[72:75], v[84:87], v[4:7]\n\t"
      "ds_read_b64_tr_b16 v[138:139], v188 offset:41984\n\t"
      "v_mfma_f32_16x16x32_bf16 v[40:43], v[76:79], v[84:87], v[40:43]\n\t"
      "ds_read_b64_tr_b16 v[140:141], v185 offset:41088\n\t"
      "v_mfma_f32_16x16x32_bf16 v[8:11], v[72:75], v[88:91], v[8:11]\n\t"
      "ds_read_b64_tr_b16 v[142:143], v185 offset:42112\n\t"
      "v_mfma_f32_16x16x32_bf16 v[44:47], v[76:79], v[88:91], v[44:47]\n\t"
      "ds_read_b64_tr_b16 v[144:145], v186 offset:41088\n\t"
      "s_waitcnt lgkmcnt(14)\n\t"
      "v_mfma_f32_16x16x32_bf16 v[12:15], v[72:75], v[92:95], v[12:15]\n\t"
      "ds_read_b64_tr_b16 v[146:147], v186 offset:42112\n\t"
      "v_mfma_f32_16x16x32_bf16 v[48:51], v[76:79], v[92:95], v[48:51]\n\t"
      "ds_read_b64_tr_b16 v[148:149], v187 offset:41088\n\t"
      "ds_read_b64_tr_b16 v[150:151], v187 offset:42112\n\t"
      "v_mfma_f32_16x16x32_bf16 v[16:19], v[72:75], v[96:99], v[16:19]\n\t"
      "ds_read_b64_tr_b16 v[152:153], v188 offset:41088\n\t"
      "v_mfma_f32_16x16x32_bf16 v[164:167], v[172:175], v[96:99], v[164:167]\n\t"
      "ds_read_b64_tr_b16 v[154:155], v188 offset:42112\n\t"
      "v_mfma_f32_16x16x32_bf16 v[52:55], v[76:79], v[96:99], v[52:55]\n\t"
      "ds_read_b128 v[116:119], v179 offset:36864\n\t"
      "v_mfma_f32_16x16x32_bf16 v[20:23], v[72:75], v[100:103], v[20:23]\n\t"
      "ds_read_b128 v[120:123], v179 offset:38912\n\t"
      "v_mfma_f32_16x16x32_bf16 v[56:59], v[76:79], v[100:103], v[56:59]\n\t"
      "ds_read_b64_tr_b16 v[156:157], v189 offset:37888\n\t"
      "v_mfma_f32_16x16x32_bf16 v[24:27], v[72:75], v[104:107], v[24:27]\n\t"
      "ds_read_b64_tr_b16 v[158:159], v189 offset:38144\n\t"
      "v_mov_b32 v237, 8\n\t"
      "ds_write_b32 v236, v237\n\t"
      "v_mfma_f32_16x16x32_bf16 v[60:63], v[76:79], v[104:107], v[60:63]\n\t"
      "s_waitcnt lgkmcnt(14)\n\t"
      "v_mfma_f32_16x16x32_bf16 v[28:31], v[72:75], v[108:111], v[28:31]\n\t"
      "v_mfma_f32_16x16x32_bf16 v[64:67], v[76:79], v[108:111], v[64:67]\n\t"
      "v_mfma_f32_16x16x32_bf16 v[32:35], v[72:75], v[112:115], v[32:35]\n\t"
      "s_cmp_lg_u32 %[wm], 3\n\t"
      "s_cbranch_scc1 25f\n\t"
      "v_mfma_f32_16x16x32_bf16 v[168:171], v[172:175], v[112:115], v[168:171]\n\t"
      "25:\n\t"
      "v_mfma_f32_16x16x32_bf16 v[68:71], v[76:79], v[112:115], v[68:71]\n\t"
      "s_waitcnt lgkmcnt(1)\n\t"
      "v_mfma_f32_16x16x32_bf16 v[0:3], v[116:119], v[124:127], v[0:3]\n\t"
      "ds_read_b128 v[72:75], v176 offset:0\n\t"
      "ds_read_b64_tr_b16 v[80:81], v180 offset:0\n\t"
      "v_mfma_f32_16x16x32_bf16 v[160:163], v[172:175], v[124:127], v[160:163]\n\t"
      "ds_read_b64_tr_b16 v[82:83], v180 offset:1024\n\t"
      "v_mfma_f32_16x16x32_bf16 v[36:39], v[120:123], v[124:127], v[36:39]\n\t"
      "ds_read_b128 v[76:79], v176 offset:2048\n\t"
      "v_mfma_f32_16x16x32_bf16 v[4:7], v[116:119], v[128:131], v[4:7]\n\t"
      "ds_read_b64_tr_b16 v[84:85], v181 offset:0\n\t"
      "ds_read_b64_tr_b16 v[86:87], v181 offset:1024\n\t"
      "v_mfma_f32_16x16x32_bf16 v[40:43], v[120:123], v[128:131], v[40:43]\n\t"
      "ds_read_b64_tr_b16 v[88:89], v182 offset:0\n\t"
      "v_mfma_f32_16x16x32_bf16 v[8:11], v[116:119], v[132:135], v[8:11]\n\t"
      "ds_read_b64_tr_b16 v[90:91], v182 offset:1024\n\t"
      "v_mfma_f32_16x16x32_bf16 v[44:47], v[120:123], v[132:135], v[44:47]\n\t"
      "ds_read_b64_tr_b16 v[92:93], v183 offset:0\n\t"
      "ds_read_b64_tr_b16 v[94:95], v183 offset:1024\n\t"
      "v_mfma_f32_16x16x32_bf16 v[12:15], v[116:119], v[136:139], v[12:15]\n\t"
      "ds_read_b64_tr_b16 v[96:97], v180 offset:128\n\t"
      "v_mfma_f32_16x16x32_bf16 v[48:51], v[120:123], v[136:139], v[48:51]\n\t"
      "ds_read_b64_tr_b16 v[98:99], v180 offset:1152\n\t"
      "v_mfma_f32_16x16x32_bf16 v[16:19], v[116:119], v[140:143], v[16:19]\n\t"
      "ds_read_b64_tr_b16 v[100:101], v181 offset:128\n\t"
      "ds_read_b64_tr_b16 v[102:103], v181 offset:1152\n\t"
      "v_mfma_f32_16x16x32_bf16 v[164:167], v[172:175], v[140:143], v[164:167]\n\t"
      "ds_read_b64_tr_b16 v[104:105], v182 offset:128\n\t"
      "v_mfma_f32_16x16x32_bf16 v[52:55], v[120:123], v[140:143], v[52:55]\n\t"
      "ds_read_b64_tr_b16 v[106:107], v182 offset:1152\n\t"
      "v_mfma_f32_16x16x32_bf16 v[20:23], v[116:119], v[144:147], v[20:23]\n\t"
      "ds_read_b64_tr_b16 v[108:109], v183 offset:128\n\t"
      "ds_read_b64_tr_b16 v[110:111], v183 offset:1152\n\t"
      "v_mfma_f32_16x16x32_bf16 v[56:59], v[120:123], v[144:147], v[56:59]\n\t"
      "ds_read_b64_tr_b16 v[112:113], v184 offset:0\n\t"
      "v_mfma_f32_16x16x32_bf16 v[24:27], v[116:119], v[148:151], v[24:27]\n\t"
      "ds_read_b64_tr_b16 v[114:115], v184 offset:256\n\t"
      "v_mfma_f32_16x16x32_bf16 v[60:63], v[120:123], v[148:151], v[60:63]\n\t"
      "ds_read_b64_tr_b16 v[124:125], v180 offset:4096\n\t"
      "ds_read_b64_tr_b16 v[126:127], v180 offset:5120\n\t"
      "v_mfma_f32_16x16x32_bf16 v[28:31], v[116:119], v[152:155], v[28:31]\n\t"
      "ds_read_b64_tr_b16 v[128:129], v181 offset:4096\n\t"
      "v_mfma_f32_16x16x32_bf16 v[64:67], v[120:123], v[152:155], v[64:67]\n\t"
      "ds_read_b64_tr_b16 v[130:131], v181 offset:5120\n\t"
      "v_mfma_f32_16x16x32_bf16 v[32:35], v[116:119], v[156:159], v[32:35]\n\t"
      "ds_read_b64_tr_b16 v[132:133], v182 offset:4096\n\t"
      "ds_read_b64_tr_b16 v[134:135], v182 offset:5120\n\t"
      "s_cmp_lg_u32 %[wm], 3\n\t"
      "s_cbranch_scc1 26f\n\t"
      "v_mfma_f32_16x16x32_bf16 v[168:171], v[172:175], v[156:159], v[168:171]\n\t"
      "26:\n\t"
      "v_mfma_f32_16x16x32_bf16 v[68:71], v[120:123], v[156:159], v[68:71]\n\t"
      "ds_read_b64_tr_b16 v[136:137], v183 offset:4096\n\t"
      "s_waitcnt lgkmcnt(14)\n\t"
      "v_mfma_f32_16x16x32_bf16 v[0:3], v[72:75], v[80:83], v[0:3]\n\t"
      "ds_read_b64_tr_b16 v[138:139], v183 offset:5120\n\t"
      "v_mfma_f32_16x16x32_bf16 v[160:163], v[172:175], v[80:83], v[160:163]\n\t"
      "ds_read_b64_tr_b16 v[140:141], v180 offset:4224\n\t"
      "ds_read_b64_tr_b16 v[142:143], v180 offset:5248\n\t"
      "v_mfma_f32_16x16x32_bf16 v[36:39], v[76:79], v[80:83], v[36:39]\n\t"
      "ds_read_b64_tr_b16 v[144:145], v181 offset:4224\n\t"
      "v_mfma_f32_16x16x32_bf16 v[4:7], v[72:75], v[84:87], v[4:7]\n\t"
      "ds_read_b64_tr_b16 v[146:147], v181 offset:5248\n\t"
      "v_mfma_f32_16x16x32_bf16 v[40:43], v[76:79], v[84:87], v[40:43]\n\t"
      "ds_read_b64_tr_b16 v[148:149], v182 offset:4224\n\t"
      "ds_read_b64_tr_b16 v[150:151], v182 offset:5248\n\t"
      "v_mfma_f32_16x16x32_bf16 v[8:11], v[72:75], v[88:91], v[8:11]\n\t"
      "ds_read_b64_tr_b16 v[152:153], v183 offset:4224\n\t"
      "v_mfma_f32_16x16x32_bf16 v[44:47], v[76:79], v[88:91], v[44:47]\n\t"
      "ds_read_b64_tr_b16 v[154:155], v183 offset:5248\n\t"
      "v_mfma_f32_16x16x32_bf16 v[12:15], v[72:75], v[92:95], v[12:15]\n\t"
      "ds_read_b128 v[116:119], v177 offset:0\n\t"
      "ds_read_b128 v[120:123], v177 offset:2048\n\t"
      "v_mfma_f32_16x16x32_bf16 v[48:51], v[76:79], v[92:95], v[48:51]\n\t"
      "ds_read_b64_tr_b16 v[156:157], v184 offset:1024\n\t"
      "v_mfma_f32_16x16x32_bf16 v[16:19], v[72:75], v[96:99], v[16:19]\n\t"
      "ds_read_b64_tr_b16 v[158:159], v184 offset:1280\n\t"
      "v_mfma_f32_16x16x32_bf16 v[164:167], v[172:175], v[96:99], v[164:167]\n\t"
      "v_mfma_f32_16x16x32_bf16 v[52:55], v[76:79], v[96:99], v[52:55]\n\t"
      "s_waitcnt lgkmcnt(14)\n\t"
      "v_mfma_f32_16x16x32_bf16 v[20:23], v[72:75], v[100:103], v[20:23]\n\t"
      "v_mfma_f32_16x16x32_bf16 v[56:59], v[76:79], v[100:103], v[56:59]\n\t"
      "v_mfma_f32_16x16x32_bf16 v[24:27], v[72:75], v[104:107], v[24:27]\n\t"
      "s_waitcnt lgkmcnt(0)\n\t"
      "v_mfma_f32_16x16x32_bf16 v[160:163], v[172:175], v[124:127], v[160:163]\n\t"
      "v_mfma_f32_16x16x32_bf16 v[164:167], v[172:175], v[140:143], v[164:167]\n\t"
      "v_mfma_f32_16x16x32_bf16 v[60:63], v[76:79], v[104:107], v[60:63]\n\t"
      "s_cmp_lg_u32 %[wm], 0\n\t"
      "s_cbranch_scc1 28f\n\t"
      "v_mfma_f32_16x16x32_bf16 v[168:171], v[172:175], v[112:115], v[168:171]\n\t"
      "v_mfma_f32_16x16x32_bf16 v[168:171], v[172:175], v[156:159], v[168:171]\n\t"
      "28:\n\t"
      "v_mfma_f32_16x16x32_bf16 v[28:31], v[72:75], v[108:111], v[28:31]\n\t"
      "v_mfma_f32_16x16x32_bf16 v[64:67], v[76:79], v[108:111], v[64:67]\n\t"
      "v_mfma_f32_16x16x32_bf16 v[32:35], v[72:75], v[112:115], v[32:35]\n\t"
      "ds_write_b32 v190, v160 offset:0\n\t"
      "ds_write_b32 v190, v164 offset:256\n\t"
      "v_mfma_f32_16x16x32_bf16 v[68:71], v[76:79], v[112:115], v[68:71]\n\t"
      "s_nop 1\n\t"
      "ds_write_b32 v190, v168 offset:512\n\t"
      "s_waitcnt lgkmcnt(0)\n\t"
      "s_mov_b64 exec, 1\n\t"
      "v_mov_b32 v172, 1\n\t"
      "ds_add_u32 v191, v172 offset:0\n\t"
      "s_mov_b64 exec, -1\n\t"
      "v_mfma_f32_16x16x32_bf16 v[0:3], v[116:119], v[124:127], v[0:3]\n\t"
      "v_mfma_f32_16x16x32_bf16 v[36:39], v[120:123], v[124:127], v[36:39]\n\t"
      "v_mfma_f32_16x16x32_bf16 v[4:7], v[116:119], v[128:131], v[4:7]\n\t"
      "v_mfma_f32_16x16x32_bf16 v[40:43], v[120:123], v[128:131], v[40:43]\n\t"
      "ds_read_b32 v224, v191\n\t"
      "v_mfma_f32_16x16x32_bf16 v[8:11], v[116:119], v[132:135], v[8:11]\n\t"
      "v_mfma_f32_16x16x32_bf16 v[44:47], v[120:123], v[132:135], v[44:47]\n\t"
      "v_mfma_f32_16x16x32_bf16 v[12:15], v[116:119], v[136:139], v[12:15]\n\t"
      "v_mfma_f32_16x16x32_bf16 v[48:51], v[120:123], v[136:139], v[48:51]\n\t"
      "s_branch 3f\n\t"
      "2:\n\t"
      "ds_read_b32 v224, v191\n\t"
      "3:\n\t"
      "s_waitcnt lgkmcnt(0)\n\t"
      "v_readfirstlane_b32 %[t], v224\n\t"
      "s_cmp_lt_u32 %[t], 8\n\t"
      "s_cbranch_scc1 2b\n\t"
      "ds_read_b32 v224, v250 offset:0\n\t"
      "ds_read_b32 v225, v251 offset:0\n\t"
      "v_mfma_f32_16x16x32_bf16 v[16:19], v[116:119], v[140:143], v[16:19]\n\t"
      "ds_read_b32 v226, v252 offset:0\n\t"
      "ds_read_b32 v227, v253 offset:0\n\t"
      "v_mfma_f32_16x16x32_bf16 v[52:55], v[120:123], v[140:143], v[52:55]\n\t"
      "ds_read_b32 v228, v250 offset:256\n\t"
      "ds_read_b32 v229, v251 offset:256\n\t"
      "v_mfma_f32_16x16x32_bf16 v[20:23], v[116:119], v[144:147], v[20:23]\n\t"
      "ds_read_b32 v230, v252 offset:256\n\t"
      "ds_read_b32 v231, v253 offset:256\n\t"
      "v_mfma_f32_16x16x32_bf16 v[56:59], v[120:123], v[144:147], v[56:59]\n\t"
      "ds_read_b32 v232, v250 offset:512\n\t"
      "ds_read_b32 v233, v251 offset:512\n\t"
      "v_mfma_f32_16x16x32_bf16 v[24:27], v[116:119], v[148:151], v[24:27]\n\t"
      "ds_read_b32 v234, v252 offset:512\n\t"
      "ds_read_b32 v235, v253 offset:512\n\t"
      "v_mfma_f32_16x16x32_bf16 v[60:63], v[120:123], v[148:151], v[60:63]\n\t"
      "ds_read_b128 v[192:195], v248 offset:0\n\t"
      "ds_read_b128 v[196:199], v248 offset:16\n\t"
      "v_mfma_f32_16x16x32_bf16 v[28:31], v[116:119], v[152:155], v[28:31]\n\t"
      "ds_read_b128 v[200:203], v248 offset:512\n\t"
      "ds_read_b128 v[204:207], v248 offset:528\n\t"
      "v_mfma_f32_16x16x32_bf16 v[64:67], v[120:123], v[152:155], v[64:67]\n\t"
      "ds_read_b128 v[208:211], v248 offset:1024\n\t"
      "ds_read_b128 v[212:215], v248 offset:1040\n\t"
      "v_mfma_f32_16x16x32_bf16 v[32:35], v[116:119], v[156:159], v[32:35]\n\t"
      "ds_read_b128 v[216:219], v248 offset:1536\n\t"
      "ds_read_b128 v[220:223], v248 offset:1552\n\t"
      "v_mfma_f32_16x16x32_bf16 v[68:71], v[120:123], v[156:159], v[68:71]\n\t"
      "s_nop 7\n\t"
      "s_waitcnt lgkmcnt(0)\n\t"
      "s_nop 3\n\t"
      : "={v[0:3]}"(acc[0][0]), "={v[4:7]}"(acc[0][1]), "={v[8:11]}"(acc[0][2]), "={v[12:15]}"(acc[0][3]), "={v[16:19]}"(acc[0][4]), "={v[20:23]}"(acc[0][5]), "={v[24:27]}"(acc[0][6]), "={v[28:31]}"(acc[0][7]), "={v[32:35]}"(acc[0][8]), "={v[36:39]}"(acc[1][0]), "={v[40:43]}"(acc[1][1]), "={v[44:47]}"(acc[1][2]), "={v[48:51]}"(acc[1][3]), "={v[52:55]}"(acc[1][4]), "={v[56:59]}"(acc[1][5]), "={v[60:63]}"(acc[1][6]), "={v[64:67]}"(acc[1][7]), "={v[68:71]}"(acc[1][8]), "={v[192:199]}"(rc[0]), "={v[200:207]}"(rc[1]), "={v[208:215]}"(rc[2]), "={v[216:223]}"(rc[3]), "={v[224:227]}"(cs[0]), "={v[228:231]}"(cs[1]), "={v[232:235]}"(cs[2]), [t] "=&s"(tmp)
      : "{v[176:179]}"(ad0), "{v[180:187]}"(ad1), "{v[188:189]}"(ad2), "{v190}"(cw), "{v191}"(lb + FL_C9A), [wm] "s"(wm), "{v248}"(rs), "{v[250:253]}"(ca), "{v236}"(lb + FL_CP + wm * 16 + (lane & 3) * 4) : "memory", "scc", "vcc", "v237", "v238", "v72", "v73", "v74", "v75", "v76", "v77", "v78", "v79", "v80", "v81", "v82", "v83", "v84", "v85", "v86", "v87", "v88", "v89", "v90", "v91", "v92", "v93", "v94", "v95", "v96", "v97", "v98", "v99", "v100", "v101", "v102", "v103", "v104", "v105", "v106", "v107", "v108", "v109", "v110", "v111", "v112", "v113", "v114", "v115", "v116", "v117", "v118", "v119", "v120", "v121", "v122", "v123", "v124", "v125", "v126", "v127", "v128", "v129", "v130", "v131", "v132", "v133", "v134", "v135", "v136", "v137", "v138", "v139", "v140", "v141", "v142", "v143", "v144", "v145", "v146", "v147", "v148", "v149", "v150", "v151", "v152", "v153", "v154", "v155", "v156", "v157", "v158", "v159", "v160", "v161", "v162", "v163", "v164", "v165", "v166", "v167", "v168", "v169", "v170", "v171", "v172", "v173", "v174", "v175");
    TS(2);
    epi_a(lb, ws, acc, rc, cs, z2p, z28, tm, tn, wm, ep TS_ARG);
  }
  TS(3);
  f32x4 ac[18], c[3];
  u32x8 ad;
  {
    const int lane = lane_id(), r = lane & 15, g = lane >> 4;
    const unsigned cw = lb + T_CW + wm * 64 + g * 16;
    const unsigned a0 = lb + RING + r * 128 + ((g ^ ((r >> 1) & 7)) * 16);
#pragma unroll
    for (int p = 0; p < 4; ++p) { ad[2 * p] = a0 + (p ^ wm) * 4096; ad[2 * p + 1] = (a0 ^ 64) + (p ^ wm) * 4096; }
    const unsigned b0 = lb + RING + 16384 + (g & 1) * 8320 + (g >> 1) * 2048 + (r >> 2) * 256 + (lane & 3) * 8 + (wm >> 1) * 128, x = ((wm & 1) * 2) ^ (r >> 2);
    u32x3 bd = {b0 + x * 32, b0 + (x ^ 1) * 32, lb + RING + 32896 + (g >> 1) * 512 + (g & 1) * 128 + (r >> 2) * 32 + (lane & 3) * 8};
    unsigned tmp;
    asm volatile(
      "v_mov_b32 v172, 0x3f803f80\n\t"
      "v_mov_b32 v173, 0x3f803f80\n\t"
      "v_mov_b32 v174, 0x3f803f80\n\t"
      "v_mov_b32 v175, 0x3f803f80\n\t"
      "v_mov_b32 v168, 0\n\t"
      "v_mov_b32 v169, 0\n\t"
      "v_mov_b32 v170, 0\n\t"
      "v_mov_b32 v171, 0\n\t"
      "11:\n\t"
      "ds_read_b32 v189, v187 offset:64\n\t"
      "s_waitcnt lgkmcnt(0)\n\t"
      "v_cmp_gt_u32 vcc, 10, v189\n\t"
      "s_cbranch_vccnz 11b\n\t"
      "ds_read_b64_tr_b16 v[104:105], v184 offset:36864\n\t"
      "ds_read_b64_tr_b16 v[106:107], v184 offset:37888\n\t"
      "ds_read_b128 v[80:83], v178 offset:36864\n\t"
      "ds_read_b64_tr_b16 v[108:109], v185 offset:36864\n\t"
      "ds_read_b64_tr_b16 v[110:111], v185 offset:37888\n\t"
      "ds_read_b128 v[84:87], v178 offset:38912\n\tv_add_u32 v178, 73728, v178\n\t"
      "ds_read_b128 v[88:91], v180 offset:36864\n\t"
      "ds_read_b128 v[92:95], v180 offset:38912\n\tv_add_u32 v180, 73728, v180\n\t"
      "ds_read_b128 v[96:99], v182 offset:36864\n\t"
      "ds_read_b128 v[100:103], v182 offset:38912\n\tv_add_u32 v182, 73728, v182\n\t"
      "ds_read_b128 v[72:75], v176 offset:36864\n\t"
      "ds_read_b64_tr_b16 v[112:113], v186 offset:36864\n\t"
      "ds_read_b64_tr_b16 v[114:115], v186 offset:37120\n\t"
      "ds_read_b128 v[76:79], v176 offset:38912\n\tv_add_u32 v176, 73728, v176\n\t"
      "s_waitcnt lgkmcnt(11)\n\t"
      "v_mfma_f32_16x16x32_bf16 v[16:19], v[104:107], v[80:83], 0\n\t"
      "ds_read_b64_tr_b16 v[148:149], v184 offset:40960\n\t"
      "s_waitcnt lgkmcnt(10)\n\t"
      "v_mfma_f32_16x16x32_bf16 v[20:23], v[108:111], v[80:83], 0\n\t"
      "ds_read_b64_tr_b16 v[150:151], v184 offset:41984\n\tv_add_u32 v184, 73728, v184\n\t"
      "s_waitcnt lgkmcnt(10)\n\t"
      "v_mfma_f32_16x16x32_bf16 v[24:27], v[104:107], v[84:87], 0\n\t"
      "ds_read_b128 v[124:127], v179 offset:36864\n\t"
      "v_mfma_f32_16x16x32_bf16 v[28:31], v[108:111], v[84:87], 0\n\t"
      "ds_read_b64_tr_b16 v[152:153], v185 offset:40960\n\t"
      "v_mfma_f32_16x16x32_bf16 v[160:163], v[104:107], v[172:175], 0\n\t"
      "ds_read_b64_tr_b16 v[154:155], v185 offset:41984\n\tv_add_u32 v185, 73728, v185\n\t"
      "s_waitcnt lgkmcnt(12)\n\t"
      "v_mfma_f32_16x16x32_bf16 v[32:35], v[104:107], v[88:91], 0\n\t"
      "ds_read_b128 v[128:131], v179 offset:38912\n\tv_add_u32 v179, 73728, v179\n\t"
      "v_mfma_f32_16x16x32_bf16 v[36:39], v[108:111], v[88:91], 0\n\t"
      "ds_read_b128 v[132:135], v181 offset:36864\n\t"
      "s_waitcnt lgkmcnt(13)\n\t"
      "v_mfma_f32_16x16x32_bf16 v[40:43], v[104:107], v[92:95], 0\n\t"
      "ds_read_b128 v[136:139], v181 offset:38912\n\tv_add_u32 v181, 73728, v181\n\t"
      "v_mfma_f32_16x16x32_bf16 v[44:47], v[108:111], v[92:95], 0\n\t"
      "ds_read_b128 v[140:143], v183 offset:36864\n\t"
      "v_mfma_f32_16x16x32_bf16 v[164:167], v[108:111], v[172:175], 0\n\t"
      "ds_read_b128 v[144:147], v183 offset:38912\n\tv_add_u32 v183, 73728, v183\n\t"
      "ds_read_b32 v189, v187 offset:64\n\t"
      "s_waitcnt lgkmcnt(14)\n\t"
      "v_mfma_f32_16x16x32_bf16 v[48:51], v[104:107], v[96:99], 0\n\t"
      "ds_read_b128 v[116:119], v177 offset:36864\n\t"
      "v_mfma_f32_16x16x32_bf16 v[52:55], v[108:111], v[96:99], 0\n\t"
      "ds_read_b64_tr_b16 v[156:157], v186 offset:37888\n\t"
      "v_mfma_f32_16x16x32_bf16 v[56:59], v[104:107], v[100:103], 0\n\t"
      "ds_read_b64_tr_b16 v[158:159], v186 offset:38144\n\tv_add_u32 v186, 73728, v186\n\t"
      "v_mfma_f32_16x16x32_bf16 v[60:63], v[108:111], v[100:103], 0\n\t"
      "ds_read_b128 v[120:123], v177 offset:38912\n\tv_add_u32 v177, 73728, v177\n\t"
      "v_mov_b32 v188, 10\n\t"
      "ds_write_b32 v187, v188\n\t"
      "s_waitcnt lgkmcnt(14)\n\t"
      "v_mfma_f32_16x16x32_bf16 v[0:3], v[104:107], v[72:75], 0\n\t"
      "v_mfma_f32_16x16x32_bf16 v[4:7], v[108:111], v[72:75], 0\n\t"
      "v_mfma_f32_16x16x32_bf16 v[64:67], v[112:115], v[72:75], 0\n\t"
      "s_cmp_lg_u32 %[wm], 0\n\t"
      "s_cbranch_scc1 12f\n\t"
      "v_mfma_f32_16x16x32_bf16 v[168:171], v[112:115], v[172:175], v[168:171]\n\t"
      "12:\n\t"
      "v_mfma_f32_16x16x32_bf16 v[8:11], v[104:107], v[76:79], 0\n\t"
      "v_mfma_f32_16x16x32_bf16 v[12:15], v[108:111], v[76:79], 0\n\t"
      "v_mfma_f32_16x16x32_bf16 v[68:71], v[112:115], v[76:79], 0\n\t"
      "s_waitcnt lgkmcnt(1)\n\t"
      "v_cmp_gt_u32 vcc, 11, v189\n\t"
      "v_mfma_f32_16x16x32_bf16 v[16:19], v[148:151], v[124:127], v[16:19]\n\t"
      "s_cbranch_vccnz 13f\n\t"
      "14:\n\t"
      "ds_read_b64_tr_b16 v[104:105], v184 offset:0\n\t"
      "v_mfma_f32_16x16x32_bf16 v[20:23], v[152:155], v[124:127], v[20:23]\n\t"
      "ds_read_b64_tr_b16 v[106:107], v184 offset:1024\n\t"
      "v_mfma_f32_16x16x32_bf16 v[24:27], v[148:151], v[128:131], v[24:27]\n\t"
      "ds_read_b128 v[80:83], v178 offset:0\n\t"
      "v_mfma_f32_16x16x32_bf16 v[28:31], v[152:155], v[128:131], v[28:31]\n\t"
      "ds_read_b64_tr_b16 v[108:109], v185 offset:0\n\t"
      "v_mfma_f32_16x16x32_bf16 v[160:163], v[148:151], v[172:175], v[160:163]\n\t"
      "v_mfma_f32_16x16x32_bf16 v[32:35], v[148:151], v[132:135], v[32:35]\n\t"
      "ds_read_b64_tr_b16 v[110:111], v185 offset:1024\n\t"
      "v_mfma_f32_16x16x32_bf16 v[36:39], v[152:155], v[132:135], v[36:39]\n\t"
      "ds_read_b128 v[84:87], v178 offset:2048\n\t"
      "v_mfma_f32_16x16x32_bf16 v[40:43], v[148:151], v[136:139], v[40:43]\n\t"
      "ds_read_b128 v[88:91], v180 offset:0\n\t"
      "v_mfma_f32_16x16x32_bf16 v[44:47], v[152:155], v[136:139], v[44:47]\n\t"
      "ds_read_b128 v[92:95], v180 offset:2048\n\t"
      "v_mfma_f32_16x16x32_bf16 v[164:167], v[152:155], v[172:175], v[164:167]\n\t"
      "v_mfma_f32_16x16x32_bf16 v[48:51], v[148:151], v[140:143], v[48:51]\n\t"
      "ds_read_b128 v[96:99], v182 offset:0\n\t"
      "v_mfma_f32_16x16x32_bf16 v[52:55], v[152:155], v[140:143], v[52:55]\n\t"
      "ds_read_b128 v[100:103], v182 offset:2048\n\t"
      "v_mfma_f32_16x16x32_bf16 v[56:59], v[148:151], v[144:147], v[56:59]\n\t"
      "ds_read_b128 v[72:75], v176 offset:0\n\t"
      "v_mfma_f32_16x16x32_bf16 v[60:63], v[152:155], v[144:147], v[60:63]\n\t"
      "ds_read_b64_tr_b16 v[112:113], v186 offset:0\n\t"
      "v_mfma_f32_16x16x32_bf16 v[0:3], v[148:151], v[116:119], v[0:3]\n\t"
      "v_mfma_f32_16x16x32_bf16 v[4:7], v[152:155], v[116:119], v[4:7]\n\t"
      "ds_read_b64_tr_b16 v[114:115], v186 offset:256\n\t"
      "v_mfma_f32_16x16x32_bf16 v[64:67], v[156:159], v[116:119], v[64:67]\n\t"
      "s_cmp_lg_u32 %[wm], 0\n\t"
      "s_cbranch_scc1 15f\n\t"
      "v_mfma_f32_16x16x32_bf16 v[168:171], v[156:159], v[172:175], v[168:171]\n\t"
      "15:\n\t"
      "ds_read_b128 v[76:79], v176 offset:2048\n\t"
      "v_mfma_f32_16x16x32_bf16 v[8:11], v[148:151], v[120:123], v[8:11]\n\t"
      "ds_read_b128 v[124:127], v179 offset:0\n\t"
      "v_mfma_f32_16x16x32_bf16 v[12:15], v[152:155], v[120:123], v[12:15]\n\t"
      "ds_read_b128 v[128:131], v179 offset:2048\n\t"
      "v_mfma_f32_16x16x32_bf16 v[68:71], v[156:159], v[120:123], v[68:71]\n\t"
      "s_waitcnt lgkmcnt(13)\n\t"
      "v_mfma_f32_16x16x32_bf16 v[16:19], v[104:107], v[80:83], v[16:19]\n\t"
      "ds_read_b128 v[132:135], v181 offset:0\n\t"
      "s_waitcnt lgkmcnt(12)\n\t"
      "v_mfma_f32_16x16x32_bf16 v[20:23], v[108:111], v[80:83], v[20:23]\n\t"
      "ds_read_b128 v[136:139], v181 offset:2048\n\t"
      "s_waitcnt lgkmcnt(12)\n\t"
      "v_mfma_f32_16x16x32_bf16 v[24:27], v[104:107], v[84:87], v[24:27]\n\t"
      "ds_read_b128 v[140:143], v183 offset:0\n\t"
      "v_mfma_f32_16x16x32_bf16 v[28:31], v[108:111], v[84:87], v[28:31]\n\t"
      "ds_read_b128 v[144:147], v183 offset:2048\n\t"
      "v_mfma_f32_16x16x32_bf16 v[160:163], v[104:107], v[172:175], v[160:163]\n\t"
      "s_waitcnt lgkmcnt(13)\n\t"
      "v_mfma_f32_16x16x32_bf16 v[32:35], v[104:107], v[88:91], v[32:35]\n\t"
      "ds_read_b128 v[116:119], v177 offset:0\n\t"
      "v_mfma_f32_16x16x32_bf16 v[36:39], v[108:111], v[88:91], v[36:39]\n\t"
      "ds_read_b64_tr_b16 v[148:149], v184 offset:4096\n\t"
      "s_waitcnt lgkmcnt(14)\n\t"
      "v_mfma_f32_16x16x32_bf16 v[40:43], v[104:107], v[92:95], v[40:43]\n\t"
      "ds_read_b64_tr_b16 v[150:151], v184 offset:5120\n\t"
      "v_mfma_f32_16x16x32_bf16 v[44:47], v[108:111], v[92:95], v[44:47]\n\t"
      "ds_read_b64_tr_b16 v[152:153], v185 offset:4096\n\t"
      "ds_read_b32 v189, v187 offset:64\n\t"
      "v_mfma_f32_16x16x32_bf16 v[164:167], v[108:111], v[172:175], v[164:167]\n\t"
      "s_waitcnt lgkmcnt(14)\n\t"
      "v_mfma_f32_16x16x32_bf16 v[48:51], v[104:107], v[96:99], v[48:51]\n\t"
      "ds_read_b64_tr_b16 v[154:155], v185 offset:5120\n\t"
      "v_mfma_f32_16x16x32_bf16 v[52:55], v[108:111], v[96:99], v[52:55]\n\t"
      "ds_read_b64_tr_b16 v[156:157], v186 offset:1024\n\t"
      "v_mfma_f32_16x16x32_bf16 v[56:59], v[104:107], v[100:103], v[56:59]\n\t"
      "ds_read_b64_tr_b16 v[158:159], v186 offset:1280\n\t"
      "v_mfma_f32_16x16x32_bf16 v[60:63], v[108:111], v[100:103], v[60:63]\n\t"
      "ds_read_b128 v[120:123], v177 offset:2048\n\t"
      "v_mov_b32 v188, 11\n\t"
      "ds_write_b32 v187, v188\n\t"
      "s_waitcnt lgkmcnt(14)\n\t"
      "v_mfma_f32_16x16x32_bf16 v[0:3], v[104:107], v[72:75], v[0:3]\n\t"
      "v_mfma_f32_16x16x32_bf16 v[4:7], v[108:111], v[72:75], v[4:7]\n\t"
      "v_mfma_f32_16x16x32_bf16 v[64:67], v[112:115], v[72:75], v[64:67]\n\t"
      "s_cmp_lg_u32 %[wm], 1\n\t"
      "s_cbranch_scc1 16f\n\t"
      "v_mfma_f32_16x16x32_bf16 v[168:171], v[112:115], v[172:175], v[168:171]\n\t"
      "16:\n\t"
      "v_mfma_f32_16x16x32_bf16 v[8:11], v[104:107], v[76:79], v[8:11]\n\t"
      "v_mfma_f32_16x16x32_bf16 v[12:15], v[108:111], v[76:79], v[12:15]\n\t"
      "v_mfma_f32_16x16x32_bf16 v[68:71], v[112:115], v[76:79], v[68:71]\n\t"
      "s_waitcnt lgkmcnt(1)\n\t"
      "v_cmp_gt_u32 vcc, 12, v189\n\t"
      "v_mfma_f32_16x16x32_bf16 v[16:19], v[148:151], v[124:127], v[16:19]\n\t"
      "s_cbranch_vccnz 17f\n\t"
      "18:\n\t"
      "ds_read_b64_tr_b16 v[104:105], v184 offset:36864\n\t"
      "v_mfma_f32_16x16x32_bf16 v[20:23], v[152:155], v[124:127], v[20:23]\n\t"
      "ds_read_b64_tr_b16 v[106:107], v184 offset:37888\n\t"
      "v_mfma_f32_16x16x32_bf16 v[24:27], v[148:151], v[128:131], v[24:27]\n\t"
      "ds_read_b128 v[80:83], v178 offset:36864\n\t"
      "v_mfma_f32_16x16x32_bf16 v[28:31], v[152:155], v[128:131], v[28:31]\n\t"
      "ds_read_b64_tr_b16 v[108:109], v185 offset:36864\n\t"
      "v_mfma_f32_16x16x32_bf16 v[160:163], v[148:151], v[172:175], v[160:163]\n\t"
      "v_mfma_f32_16x16x32_bf16 v[32:35], v[148:151], v[132:135], v[32:35]\n\t"
      "ds_read_b64_tr_b16 v[110:111], v185 offset:37888\n\t"
      "v_mfma_f32_16x16x32_bf16 v[36:39], v[152:155], v[132:135], v[36:39]\n\t"
      "ds_read_b128 v[84:87], v178 offset:38912\n\tv_subrev_u32 v178, 73728, v178\n\t"
      "v_mfma_f32_16x16x32_bf16 v[40:43], v[148:151], v[136:139], v[40:43]\n\t"
      "ds_read_b128 v[88:91], v180 offset:36864\n\t"
      "v_mfma_f32_16x16x32_bf16 v[44:47], v[152:155], v[136:139], v[44:47]\n\t"
      "ds_read_b128 v[92:95], v180 offset:38912\n\tv_subrev_u32 v180, 73728, v180\n\t"
      "v_mfma_f32_16x16x32_bf16 v[164:167], v[152:155], v[172:175], v[164:167]\n\t"
      "v_mfma_f32_16x16x32_bf16 v[48:51], v[148:151], v[140:143], v[48:51]\n\t"
      "ds_read_b128 v[96:99], v182 offset:36864\n\t"
      "v_mfma_f32_16x16x32_bf16 v[52:55], v[152:155], v[140:143], v[52:55]\n\t"
      "ds_read_b128 v[100:103], v182 offset:38912\n\tv_subrev_u32 v182, 73728, v182\n\t"
      "v_mfma_f32_16x16x32_bf16 v[56:59], v[148:151], v[144:147], v[56:59]\n\t"
      "ds_read_b128 v[72:75], v176 offset:36864\n\t"
      "v_mfma_f32_16x16x32_bf16 v[60:63], v[152:155], v[144:147], v[60:63]\n\t"
      "ds_read_b64_tr_b16 v[112:113], v186 offset:36864\n\t"
      "v_mfma_f32_16x16x32_bf16 v[0:3], v[148:151], v[116:119], v[0:3]\n\t"
      "v_mfma_f32_16x16x32_bf16 v[4:7], v[152:155], v[116:119], v[4:7]\n\t"
      "ds_read_b64_tr_b16 v[114:115], v186 offset:37120\n\t"
      "v_mfma_f32_16x16x32_bf16 v[64:67], v[156:159], v[116:119], v[64:67]\n\t"
      "s_cmp_lg_u32 %[wm], 1\n\t"
      "s_cbranch_scc1 19f\n\t"
      "v_mfma_f32_16x16x32_bf16 v[168:171], v[156:159], v[172:175], v[168:171]\n\t"
      "19:\n\t"
      "ds_read_b128 v[76:79], v176 offset:38912\n\tv_subrev_u32 v176, 73728, v176\n\t"
      "v_mfma_f32_16x16x32_bf16 v[8:11], v[148:151], v[120:123], v[8:11]\n\t"
      "ds_read_b128 v[124:127], v179 offset:36864\n\t"
      "v_mfma_f32_16x16x32_bf16 v[12:15], v[152:155], v[120:123], v[12:15]\n\t"
      "ds_read_b128 v[128:131], v179 offset:38912\n\tv_subrev_u32 v179, 73728, v179\n\t"
      "v_mfma_f32_16x16x32_bf16 v[68:71], v[156:159], v[120:123], v[68:71]\n\t"
      "s_waitcnt lgkmcnt(13)\n\t"
      "v_mfma_f32_16x16x32_bf16 v[16:19], v[104:107], v[80:83], v[16:19]\n\t"
      "ds_read_b128 v[132:135], v181 offset:36864\n\t"
      "s_waitcnt lgkmcnt(12)\n\t"
      "v_mfma_f32_16x16x32_bf16 v[20:23], v[108:111], v[80:83], v[20:23]\n\t"
      "ds_read_b128 v[136:139], v181 offset:38912\n\tv_subrev_u32 v181, 73728, v181\n\t"
      "s_waitcnt lgkmcnt(12)\n\t"
      "v_mfma_f32_16x16x32_bf16 v[24:27], v[104:107], v[84:87], v[24:27]\n\t"
      "ds_read_b128 v[140:143], v183 offset:36864\n\t"
      "v_mfma_f32_16x16x32_bf16 v[28:31], v[108:111], v[84:87], v[28:31]\n\t"
      "ds_read_b128 v[144:147], v183 offset:38912\n\tv_subrev_u32 v183, 73728, v183\n\t"
      "v_mfma_f32_16x16x32_bf16 v[160:163], v[104:107], v[172:175], v[160:163]\n\t"
      "s_waitcnt lgkmcnt(13)\n\t"
      "v_mfma_f32_16x16x32_bf16 v[32:35], v[104:107], v[88:91], v[32:35]\n\t"
      "ds_read_b128 v[116:119], v177 offset:36864\n\t"
      "v_mfma_f32_16x16x32_bf16 v[36:39], v[108:111], v[88:91], v[36:39]\n\t"
      "ds_read_b64_tr_b16 v[148:149], v184 offset:40960\n\t"
      "s_waitcnt lgkmcnt(14)\n\t"
      "v_mfma_f32_16x16x32_bf16 v[40:43], v[104:107], v[92:95], v[40:43]\n\t"
      "ds_read_b64_tr_b16 v[150:151], v184 offset:41984\n\tv_subrev_u32 v184, 73728, v184\n\t"
      "v_mfma_f32_16x16x32_bf16 v[44:47], v[108:111], v[92:95], v[44:47]\n\t"
      "ds_read_b64_tr_b16 v[152:153], v185 offset:40960\n\t"
      "ds_read_b32 v189, v187 offset:64\n\t"
      "v_mfma_f32_16x16x32_bf16 v[164:167], v[108:111], v[172:175], v[164:167]\n\t"
      "s_waitcnt lgkmcnt(14)\n\t"
      "v_mfma_f32_16x16x32_bf16 v[48:51], v[104:107], v[96:99], v[48:51]\n\t"
      "ds_read_b64_tr_b16 v[154:155], v185 offset:41984\n\tv_subrev_u32 v185, 73728, v185\n\t"
      "v_mfma_f32_16x16x32_bf16 v[52:55], v[108:111], v[96:99], v[52:55]\n\t"
      "ds_read_b64_tr_b16 v[156:157], v186 offset:37888\n\t"
      "v_mfma_f32_16x16x32_bf16 v[56:59], v[104:107], v[100:103], v[56:59]\n\t"
      "ds_read_b64_tr_b16 v[158:159], v186 offset:38144\n\tv_subrev_u32 v186, 73728, v186\n\t"
      "v_mfma_f32_16x16x32_bf16 v[60:63], v[108:111], v[100:103], v[60:63]\n\t"
      "ds_read_b128 v[120:123], v177 offset:38912\n\tv_subrev_u32 v177, 73728, v177\n\t"
      "v_mov_b32 v188, 12\n\t"
      "ds_write_b32 v187, v188\n\t"
      "s_waitcnt lgkmcnt(14)\n\t"
      "v_mfma_f32_16x16x32_bf16 v[0:3], v[104:107], v[72:75], v[0:3]\n\t"
      "v_mfma_f32_16x16x32_bf16 v[4:7], v[108:111], v[72:75], v[4:7]\n\t"
      "v_mfma_f32_16x16x32_bf16 v[64:67], v[112:115], v[72:75], v[64:67]\n\t"
      "s_cmp_lg_u32 %[wm], 2\n\t"
      "s_cbranch_scc1 20f\n\t"
      "v_mfma_f32_16x16x32_bf16 v[168:171], v[112:115], v[172:175], v[168:171]\n\t"
      "20:\n\t"
      "v_mfma_f32_16x16x32_bf16 v[8:11], v[104:107], v[76:79], v[8:11]\n\t"
      "v_mfma_f32_16x16x32_bf16 v[12:15], v[108:111], v[76:79], v[12:15]\n\t"
      "v_mfma_f32_16x16x32_bf16 v[68:71], v[112:115], v[76:79], v[68:71]\n\t"
      "s_waitcnt lgkmcnt(1)\n\t"
      "v_cmp_gt_u32 vcc, 13, v189\n\t"
      "v_mfma_f32_16x16x32_bf16 v[16:19], v[148:151], v[124:127], v[16:19]\n\t"
      "s_cbranch_vccnz 21f\n\t"
      "22:\n\t"
      "ds_read_b64_tr_b16 v[104:105], v184 offset:0\n\t"
      "v_mfma_f32_16x16x32_bf16 v[20:23], v[152:155], v[124:127], v[20:23]\n\t"
      "ds_read_b64_tr_b16 v[106:107], v184 offset:1024\n\t"
      "v_mfma_f32_16x16x32_bf16 v[24:27], v[148:151], v[128:131], v[24:27]\n\t"
      "ds_read_b128 v[80:83], v178 offset:0\n\t"
      "v_mfma_f32_16x16x32_bf16 v[28:31], v[152:155], v[128:131], v[28:31]\n\t"
      "ds_read_b64_tr_b16 v[108:109], v185 offset:0\n\t"
      "v_mfma_f32_16x16x32_bf16 v[160:163], v[148:151], v[172:175], v[160:163]\n\t"
      "v_mfma_f32_16x16x32_bf16 v[32:35], v[148:151], v[132:135], v[32:35]\n\t"
      "ds_read_b64_tr_b16 v[110:111], v185 offset:1024\n\t"
      "v_mfma_f32_16x16x32_bf16 v[36:39], v[152:155], v[132:135], v[36:39]\n\t"
      "ds_read_b128 v[84:87], v178 offset:2048\n\t"
      "v_mfma_f32_16x16x32_bf16 v[40:43], v[148:151], v[136:139], v[40:43]\n\t"
      "ds_read_b128 v[88:91], v180 offset:0\n\t"
      "v_mfma_f32_16x16x32_bf16 v[44:47], v[152:155], v[136:139], v[44:47]\n\t"
      "ds_read_b128 v[92:95], v180 offset:2048\n\t"
      "v_mfma_f32_16x16x32_bf16 v[164:167], v[152:155], v[172:175], v[164:167]\n\t"
      "v_mfma_f32_16x16x32_bf16 v[48:51], v[148:151], v[140:143], v[48:51]\n\t"
      "ds_read_b128 v[96:99], v182 offset:0\n\t"
      "v_mfma_f32_16x16x32_bf16 v[52:55], v[152:155], v[140:143], v[52:55]\n\t"
      "ds_read_b128 v[100:103], v182 offset:2048\n\t"
      "v_mfma_f32_16x16x32_bf16 v[56:59], v[148:151], v[144:147], v[56:59]\n\t"
      "ds_read_b128 v[72:75], v176 offset:0\n\t"
      "v_mfma_f32_16x16x32_bf16 v[60:63], v[152:155], v[144:147], v[60:63]\n\t"
      "ds_read_b64_tr_b16 v[112:113], v186 offset:0\n\t"
      "v_mfma_f32_16x16x32_bf16 v[0:3], v[148:151], v[116:119], v[0:3]\n\t"
      "v_mfma_f32_16x16x32_bf16 v[4:7], v[152:155], v[116:119], v[4:7]\n\t"
      "ds_read_b64_tr_b16 v[114:115], v186 offset:256\n\t"
      "v_mfma_f32_16x16x32_bf16 v[64:67], v[156:159], v[116:119], v[64:67]\n\t"
      "s_cmp_lg_u32 %[wm], 2\n\t"
      "s_cbranch_scc1 23f\n\t"
      "v_mfma_f32_16x16x32_bf16 v[168:171], v[156:159], v[172:175], v[168:171]\n\t"
      "23:\n\t"
      "ds_read_b128 v[76:79], v176 offset:2048\n\t"
      "v_mfma_f32_16x16x32_bf16 v[8:11], v[148:151], v[120:123], v[8:11]\n\t"
      "ds_read_b128 v[124:127], v179 offset:0\n\t"
      "v_mfma_f32_16x16x32_bf16 v[12:15], v[152:155], v[120:123], v[12:15]\n\t"
      "ds_read_b128 v[128:131], v179 offset:2048\n\t"
      "v_mfma_f32_16x16x32_bf16 v[68:71], v[156:159], v[120:123], v[68:71]\n\t"
      "s_waitcnt lgkmcnt(13)\n\t"
      "v_mfma_f32_16x16x32_bf16 v[16:19], v[104:107], v[80:83], v[16:19]\n\t"
      "ds_read_b128 v[132:135], v181 offset:0\n\t"
      "s_waitcnt lgkmcnt(12)\n\t"
      "v_mfma_f32_16x16x32_bf16 v[20:23], v[108:111], v[80:83], v[20:23]\n\t"
      "ds_read_b128 v[136:139], v181 offset:2048\n\t"
      "s_waitcnt lgkmcnt(12)\n\t"
      "v_mfma_f32_16x16x32_bf16 v[24:27], v[104:107], v[84:87], v[24:27]\n\t"
      "ds_read_b128 v[140:143], v183 offset:0\n\t"
      "v_mfma_f32_16x16x32_bf16 v[28:31], v[108:111], v[84:87], v[28:31]\n\t"
      "ds_read_b128 v[144:147], v183 offset:2048\n\t"
      "v_mfma_f32_16x16x32_bf16 v[160:163], v[104:107], v[172:175], v[160:163]\n\t"
      "s_waitcnt lgkmcnt(13)\n\t"
      "v_mfma_f32_16x16x32_bf16 v[32:35], v[104:107], v[88:91], v[32:35]\n\t"
      "ds_read_b128 v[116:119], v177 offset:0\n\t"
      "v_mfma_f32_16x16x32_bf16 v[36:39], v[108:111], v[88:91], v[36:39]\n\t"
      "ds_read_b64_tr_b16 v[148:149], v184 offset:4096\n\t"
      "s_waitcnt lgkmcnt(14)\n\t"
      "v_mfma_f32_16x16x32_bf16 v[40:43], v[104:107], v[92:95], v[40:43]\n\t"
      "ds_read_b64_tr_b16 v[150:151], v184 offset:5120\n\t"
      "v_mfma_f32_16x16x32_bf16 v[44:47], v[108:111], v[92:95], v[44:47]\n\t"
      "ds_read_b64_tr_b16 v[152:153], v185 offset:4096\n\t"
      "v_mfma_f32_16x16x32_bf16 v[164:167], v[108:111], v[172:175], v[164:167]\n\t"
      "s_waitcnt lgkmcnt(14)\n\t"
      "v_mfma_f32_16x16x32_bf16 v[48:51], v[104:107], v[96:99], v[48:51]\n\t"
      "ds_read_b64_tr_b16 v[154:155], v185 offset:5120\n\t"
      "v_mfma_f32_16x16x32_bf16 v[52:55], v[108:111], v[96:99], v[52:55]\n\t"
      "ds_read_b64_tr_b16 v[156:157], v186 offset:1024\n\t"
      "v_mfma_f32_16x16x32_bf16 v[56:59], v[104:107], v[100:103], v[56:59]\n\t"
      "ds_read_b64_tr_b16 v[158:159], v186 offset:1280\n\t"
      "v_mfma_f32_16x16x32_bf16 v[60:63], v[108:111], v[100:103], v[60:63]\n\t"
      "ds_read_b128 v[120:123], v177 offset:2048\n\t"
      "s_waitcnt lgkmcnt(14)\n\t"
      "v_mfma_f32_16x16x32_bf16 v[0:3], v[104:107], v[72:75], v[0:3]\n\t"
      "v_mfma_f32_16x16x32_bf16 v[4:7], v[108:111], v[72:75], v[4:7]\n\t"
      "v_mfma_f32_16x16x32_bf16 v[64:67], v[112:115], v[72:75], v[64:67]\n\t"
      "s_cmp_lg_u32 %[wm], 3\n\t"
      "s_cbranch_scc1 24f\n\t"
      "v_mfma_f32_16x16x32_bf16 v[168:171], v[112:115], v[172:175], v[168:171]\n\t"
      "24:\n\t"
      "v_mfma_f32_16x16x32_bf16 v[8:11], v[104:107], v[76:79], v[8:11]\n\t"
      "v_mfma_f32_16x16x32_bf16 v[12:15], v[108:111], v[76:79], v[12:15]\n\t"
      "v_mfma_f32_16x16x32_bf16 v[68:71], v[112:115], v[76:79], v[68:71]\n\t"
      "s_waitcnt lgkmcnt(0)\n\t"
      "s_barrier\n\t"
      "v_mfma_f32_16x16x32_bf16 v[16:19], v[148:151], v[124:127], v[16:19]\n\t"
      "ds_read_b64_tr_b16 v[104:105], v184 offset:36864\n\t"
      "v_mfma_f32_16x16x32_bf16 v[20:23], v[152:155], v[124:127], v[20:23]\n\t"
      "ds_read_b64_tr_b16 v[106:107], v184 offset:37888\n\t"
      "v_mfma_f32_16x16x32_bf16 v[24:27], v[148:151], v[128:131], v[24:27]\n\t"
      "ds_read_b128 v[80:83], v178 offset:36864\n\t"
      "v_mfma_f32_16x16x32_bf16 v[28:31], v[152:155], v[128:131], v[28:31]\n\t"
      "ds_read_b64_tr_b16 v[108:109], v185 offset:36864\n\t"
      "v_mfma_f32_16x16x32_bf16 v[160:163], v[148:151], v[172:175], v[160:163]\n\t"
      "v_mfma_f32_16x16x32_bf16 v[32:35], v[148:151], v[132:135], v[32:35]\n\t"
      "ds_read_b64_tr_b16 v[110:111], v185 offset:37888\n\t"
      "v_mfma_f32_16x16x32_bf16 v[36:39], v[152:155], v[132:135], v[36:39]\n\t"
      "ds_read_b128 v[84:87], v178 offset:38912\n\tv_add_u32 v178, 73728, v178\n\t"
      "v_mfma_f32_16x16x32_bf16 v[40:43], v[148:151], v[136:139], v[40:43]\n\t"
      "ds_read_b128 v[88:91], v180 offset:36864\n\t"
      "v_mfma_f32_16x16x32_bf16 v[44:47], v[152:155], v[136:139], v[44:47]\n\t"
      "ds_read_b128 v[92:95], v180 offset:38912\n\tv_add_u32 v180, 73728, v180\n\t"
      "v_mfma_f32_16x16x32_bf16 v[164:167], v[152:155], v[172:175], v[164:167]\n\t"
      "v_mfma_f32_16x16x32_bf16 v[48:51], v[148:151], v[140:143], v[48:51]\n\t"
      "ds_read_b128 v[96:99], v182 offset:36864\n\t"
      "v_mfma_f32_16x16x32_bf16 v[52:55], v[152:155], v[140:143], v[52:55]\n\t"
      "ds_read_b128 v[100:103], v182 offset:38912\n\tv_add_u32 v182, 73728, v182\n\t"
      "v_mfma_f32_16x16x32_bf16 v[56:59], v[148:151], v[144:147], v[56:59]\n\t"
      "ds_read_b128 v[72:75], v176 offset:36864\n\t"
      "v_mfma_f32_16x16x32_bf16 v[60:63], v[152:155], v[144:147], v[60:63]\n\t"
      "ds_read_b64_tr_b16 v[112:113], v186 offset:36864\n\t"
      "v_mfma_f32_16x16x32_bf16 v[0:3], v[148:151], v[116:119], v[0:3]\n\t"
      "v_mfma_f32_16x16x32_bf16 v[4:7], v[152:155], v[116:119], v[4:7]\n\t"
      "ds_read_b64_tr_b16 v[114:115], v186 offset:37120\n\t"
      "v_mfma_f32_16x16x32_bf16 v[64:67], v[156:159], v[116:119], v[64:67]\n\t"
      "s_cmp_lg_u32 %[wm], 3\n\t"
      "s_cbranch_scc1 25f\n\t"
      "v_mfma_f32_16x16x32_bf16 v[168:171], v[156:159], v[172:175], v[168:171]\n\t"
      "25:\n\t"
      "ds_read_b128 v[76:79], v176 offset:38912\n\tv_add_u32 v176, 73728, v176\n\t"
      "v_mfma_f32_16x16x32_bf16 v[8:11], v[148:151], v[120:123], v[8:11]\n\t"
      "ds_read_b128 v[124:127], v179 offset:36864\n\t"
      "v_mfma_f32_16x16x32_bf16 v[12:15], v[152:155], v[120:123], v[12:15]\n\t"
      "ds_read_b128 v[128:131], v179 offset:38912\n\tv_add_u32 v179, 73728, v179\n\t"
      "v_mfma_f32_16x16x32_bf16 v[68:71], v[156:159], v[120:123], v[68:71]\n\t"
      "s_waitcnt lgkmcnt(13)\n\t"
      "v_mfma_f32_16x16x32_bf16 v[16:19], v[104:107], v[80:83], v[16:19]\n\t"
      "ds_read_b128 v[132:135], v181 offset:36864\n\t"
      "s_waitcnt lgkmcnt(12)\n\t"
      "v_mfma_f32_16x16x32_bf16 v[20:23], v[108:111], v[80:83], v[20:23]\n\t"
      "ds_read_b128 v[136:139], v181 offset:38912\n\tv_add_u32 v181, 73728, v181\n\t"
      "s_waitcnt lgkmcnt(12)\n\t"
      "v_mfma_f32_16x16x32_bf16 v[24:27], v[104:107], v[84:87], v[24:27]\n\t"
      "ds_read_b128 v[140:143], v183 offset:36864\n\t"
      "v_mfma_f32_16x16x32_bf16 v[28:31], v[108:111], v[84:87], v[28:31]\n\t"
      "ds_read_b128 v[144:147], v183 offset:38912\n\tv_add_u32 v183, 73728, v183\n\t"
      "v_mfma_f32_16x16x32_bf16 v[160:163], v[104:107], v[172:175], v[160:163]\n\t"
      "s_waitcnt lgkmcnt(13)\n\t"
      "v_mfma_f32_16x16x32_bf16 v[32:35], v[104:107], v[88:91], v[32:35]\n\t"
      "ds_read_b128 v[116:119], v177 offset:36864\n\t"
      "v_mfma_f32_16x16x32_bf16 v[36:39], v[108:111], v[88:91], v[36:39]\n\t"
      "ds_read_b64_tr_b16 v[148:149], v184 offset:40960\n\t"
      "s_waitcnt lgkmcnt(14)\n\t"
      "v_mfma_f32_16x16x32_bf16 v[40:43], v[104:107], v[92:95], v[40:43]\n\t"
      "ds_read_b64_tr_b16 v[150:151], v184 offset:41984\n\tv_add_u32 v184, 73728, v184\n\t"
      "v_mfma_f32_16x16x32_bf16 v[44:47], v[108:111], v[92:95], v[44:47]\n\t"
      "ds_read_b64_tr_b16 v[152:153], v185 offset:40960\n\t"
      "v_mfma_f32_16x16x32_bf16 v[164:167], v[108:111], v[172:175], v[164:167]\n\t"
      "s_waitcnt lgkmcnt(14)\n\t"
      "v_mfma_f32_16x16x32_bf16 v[48:51], v[104:107], v[96:99], v[48:51]\n\t"
      "ds_read_b64_tr_b16 v[154:155], v185 offset:41984\n\tv_add_u32 v185, 73728, v185\n\t"
      "v_mfma_f32_16x16x32_bf16 v[52:55], v[108:111], v[96:99], v[52:55]\n\t"
      "ds_read_b64_tr_b16 v[156:157], v186 offset:37888\n\t"
      "v_mfma_f32_16x16x32_bf16 v[56:59], v[104:107], v[100:103], v[56:59]\n\t"
      "ds_read_b64_tr_b16 v[158:159], v186 offset:38144\n\tv_add_u32 v186, 73728, v186\n\t"
      "v_mfma_f32_16x16x32_bf16 v[60:63], v[108:111], v[100:103], v[60:63]\n\t"
      "ds_read_b128 v[120:123], v177 offset:38912\n\tv_add_u32 v177, 73728, v177\n\t"
      "s_waitcnt lgkmcnt(14)\n\t"
      "v_mfma_f32_16x16x32_bf16 v[0:3], v[104:107], v[72:75], v[0:3]\n\t"
      "v_mfma_f32_16x16x32_bf16 v[4:7], v[108:111], v[72:75], v[4:7]\n\t"
      "v_mfma_f32_16x16x32_bf16 v[64:67], v[112:115], v[72:75], v[64:67]\n\t"
      "s_cmp_lg_u32 %[wm], 0\n\t"
      "s_cbranch_scc1 26f\n\t"
      "v_mfma_f32_16x16x32_bf16 v[168:171], v[112:115], v[172:175], v[168:171]\n\t"
      "26:\n\t"
      "v_mfma_f32_16x16x32_bf16 v[8:11], v[104:107], v[76:79], v[8:11]\n\t"
      "v_mfma_f32_16x16x32_bf16 v[12:15], v[108:111], v[76:79], v[12:15]\n\t"
      "v_mfma_f32_16x16x32_bf16 v[68:71], v[112:115], v[76:79], v[68:71]\n\t"
      "s_waitcnt lgkmcnt(0)\n\t"
      "s_barrier\n\t"
      "v_mfma_f32_16x16x32_bf16 v[16:19], v[148:151], v[124:127], v[16:19]\n\t"
      "ds_read_b64_tr_b16 v[104:105], v184 offset:0\n\t"
      "v_mfma_f32_16x16x32_bf16 v[20:23], v[152:155], v[124:127], v[20:23]\n\t"
      "ds_read_b64_tr_b16 v[106:107], v184 offset:1024\n\t"
      "v_mfma_f32_16x16x32_bf16 v[24:27], v[148:151], v[128:131], v[24:27]\n\t"
      "ds_read_b128 v[80:83], v178 offset:0\n\t"
      "v_mfma_f32_16x16x32_bf16 v[28:31], v[152:155], v[128:131], v[28:31]\n\t"
      "ds_read_b64_tr_b16 v[108:109], v185 offset:0\n\t"
      "v_mfma_f32_16x16x32_bf16 v[160:163], v[148:151], v[172:175], v[160:163]\n\t"
      "v_mfma_f32_16x16x32_bf16 v[32:35], v[148:151], v[132:135], v[32:35]\n\t"
      "ds_read_b64_tr_b16 v[110:111], v185 offset:1024\n\t"
      "v_mfma_f32_16x16x32_bf16 v[36:39], v[152:155], v[132:135], v[36:39]\n\t"
      "ds_read_b128 v[84:87], v178 offset:2048\n\t"
      "v_mfma_f32_16x16x32_bf16 v[40:43], v[148:151], v[136:139], v[40:43]\n\t"
      "ds_read_b128 v[88:91], v180 offset:0\n\t"
      "v_mfma_f32_16x16x32_bf16 v[44:47], v[152:155], v[136:139], v[44:47]\n\t"
      "ds_read_b128 v[92:95], v180 offset:2048\n\t"
      "v_mfma_f32_16x16x32_bf16 v[164:167], v[152:155], v[172:175], v[164:167]\n\t"
      "v_mfma_f32_16x16x32_bf16 v[48:51], v[148:151], v[140:143], v[48:51]\n\t"
      "ds_read_b128 v[96:99], v182 offset:0\n\t"
      "v_mfma_f32_16x16x32_bf16 v[52:55], v[152:155], v[140:143], v[52:55]\n\t"
      "ds_read_b128 v[100:103], v182 offset:2048\n\t"
      "v_mfma_f32_16x16x32_bf16 v[56:59], v[148:151], v[144:147], v[56:59]\n\t"
      "ds_read_b128 v[72:75], v176 offset:0\n\t"
      "v_mfma_f32_16x16x32_bf16 v[60:63], v[152:155], v[144:147], v[60:63]\n\t"
      "ds_read_b64_tr_b16 v[112:113], v186 offset:0\n\t"
      "v_mfma_f32_16x16x32_bf16 v[0:3], v[148:151], v[116:119], v[0:3]\n\t"
      "v_mfma_f32_16x16x32_bf16 v[4:7], v[152:155], v[116:119], v[4:7]\n\t"
      "ds_read_b64_tr_b16 v[114:115], v186 offset:256\n\t"
      "v_mfma_f32_16x16x32_bf16 v[64:67], v[156:159], v[116:119], v[64:67]\n\t"
      "s_cmp_lg_u32 %[wm], 0\n\t"
      "s_cbranch_scc1 27f\n\t"
      "v_mfma_f32_16x16x32_bf16 v[168:171], v[156:159], v[172:175], v[168:171]\n\t"
      "27:\n\t"
      "ds_read_b128 v[76:79], v176 offset:2048\n\t"
      "v_mfma_f32_16x16x32_bf16 v[8:11], v[148:151], v[120:123], v[8:11]\n\t"
      "ds_read_b128 v[124:127], v179 offset:0\n\t"
      "v_mfma_f32_16x16x32_bf16 v[12:15], v[152:155], v[120:123], v[12:15]\n\t"
      "ds_read_b128 v[128:131], v179 offset:2048\n\t"
      "v_mfma_f32_16x16x32_bf16 v[68:71], v[156:159], v[120:123], v[68:71]\n\t"
      "s_waitcnt lgkmcnt(13)\n\t"
      "v_mfma_f32_16x16x32_bf16 v[16:19], v[104:107], v[80:83], v[16:19]\n\t"
      "ds_read_b128 v[132:135], v181 offset:0\n\t"
      "s_waitcnt lgkmcnt(12)\n\t"
      "v_mfma_f32_16x16x32_bf16 v[20:23], v[108:111], v[80:83], v[20:23]\n\t"
      "ds_read_b128 v[136:139], v181 offset:2048\n\t"
      "s_waitcnt lgkmcnt(12)\n\t"
      "v_mfma_f32_16x16x32_bf16 v[24:27], v[104:107], v[84:87], v[24:27]\n\t"
      "ds_read_b128 v[140:143], v183 offset:0\n\t"
      "v_mfma_f32_16x16x32_bf16 v[28:31], v[108:111], v[84:87], v[28:31]\n\t"
      "ds_read_b128 v[144:147], v183 offset:2048\n\t"
      "v_mfma_f32_16x16x32_bf16 v[160:163], v[104:107], v[172:175], v[160:163]\n\t"
      "s_waitcnt lgkmcnt(13)\n\t"
      "v_mfma_f32_16x16x32_bf16 v[32:35], v[104:107], v[88:91], v[32:35]\n\t"
      "ds_read_b128 v[116:119], v177 offset:0\n\t"
      "v_mfma_f32_16x16x32_bf16 v[36:39], v[108:111], v[88:91], v[36:39]\n\t"
      "ds_read_b64_tr_b16 v[148:149], v184 offset:4096\n\t"
      "s_waitcnt lgkmcnt(14)\n\t"
      "v_mfma_f32_16x16x32_bf16 v[40:43], v[104:107], v[92:95], v[40:43]\n\t"
      "ds_read_b64_tr_b16 v[150:151], v184 offset:5120\n\t"
      "v_mfma_f32_16x16x32_bf16 v[44:47], v[108:111], v[92:95], v[44:47]\n\t"
      "ds_read_b64_tr_b16 v[152:153], v185 offset:4096\n\t"
      "v_mfma_f32_16x16x32_bf16 v[164:167], v[108:111], v[172:175], v[164:167]\n\t"
      "s_waitcnt lgkmcnt(14)\n\t"
      "v_mfma_f32_16x16x32_bf16 v[48:51], v[104:107], v[96:99], v[48:51]\n\t"
      "ds_read_b64_tr_b16 v[154:155], v185 offset:5120\n\t"
      "v_mfma_f32_16x16x32_bf16 v[52:55], v[108:111], v[96:99], v[52:55]\n\t"
      "ds_read_b64_tr_b16 v[156:157], v186 offset:1024\n\t"
      "v_mfma_f32_16x16x32_bf16 v[56:59], v[104:107], v[100:103], v[56:59]\n\t"
      "ds_read_b64_tr_b16 v[158:159], v186 offset:1280\n\t"
      "v_mfma_f32_16x16x32_bf16 v[60:63], v[108:111], v[100:103], v[60:63]\n\t"
      "ds_read_b128 v[120:123], v177 offset:2048\n\t"
      "s_waitcnt lgkmcnt(14)\n\t"
      "v_mfma_f32_16x16x32_bf16 v[0:3], v[104:107], v[72:75], v[0:3]\n\t"
      "v_mfma_f32_16x16x32_bf16 v[4:7], v[108:111], v[72:75], v[4:7]\n\t"
      "v_mfma_f32_16x16x32_bf16 v[64:67], v[112:115], v[72:75], v[64:67]\n\t"
      "s_cmp_lg_u32 %[wm], 1\n\t"
      "s_cbranch_scc1 28f\n\t"
      "v_mfma_f32_16x16x32_bf16 v[168:171], v[112:115], v[172:175], v[168:171]\n\t"
      "28:\n\t"
      "v_mfma_f32_16x16x32_bf16 v[8:11], v[104:107], v[76:79], v[8:11]\n\t"
      "v_mfma_f32_16x16x32_bf16 v[12:15], v[108:111], v[76:79], v[12:15]\n\t"
      "v_mfma_f32_16x16x32_bf16 v[68:71], v[112:115], v[76:79], v[68:71]\n\t"
      "s_waitcnt lgkmcnt(0)\n\t"
      "v_mfma_f32_16x16x32_bf16 v[16:19], v[148:151], v[124:127], v[16:19]\n\t"
      "ds_read_b64_tr_b16 v[104:105], v184 offset:36864\n\t"
      "v_mfma_f32_16x16x32_bf16 v[20:23], v[152:155], v[124:127], v[20:23]\n\t"
      "ds_read_b64_tr_b16 v[106:107], v184 offset:37888\n\t"
      "v_mfma_f32_16x16x32_bf16 v[24:27], v[148:151], v[128:131], v[24:27]\n\t"
      "ds_read_b128 v[80:83], v178 offset:36864\n\t"
      "v_mfma_f32_16x16x32_bf16 v[28:31], v[152:155], v[128:131], v[28:31]\n\t"
      "ds_read_b64_tr_b16 v[108:109], v185 offset:36864\n\t"
      "v_mfma_f32_16x16x32_bf16 v[160:163], v[148:151], v[172:175], v[160:163]\n\t"
      "v_mfma_f32_16x16x32_bf16 v[32:35], v[148:151], v[132:135], v[32:35]\n\t"
      "ds_read_b64_tr_b16 v[110:111], v185 offset:37888\n\t"
      "v_mfma_f32_16x16x32_bf16 v[36:39], v[152:155], v[132:135], v[36:39]\n\t"
      "ds_read_b128 v[84:87], v178 offset:38912\n\tv_subrev_u32 v178, 73728, v178\n\t"
      "v_mfma_f32_16x16x32_bf16 v[40:43], v[148:151], v[136:139], v[40:43]\n\t"
      "ds_read_b128 v[88:91], v180 offset:36864\n\t"
      "v_mfma_f32_16x16x32_bf16 v[44:47], v[152:155], v[136:139], v[44:47]\n\t"
      "v_mfma_f32_16x16x32_bf16 v[164:167], v[152:155], v[172:175], v[164:167]\n\t"
      "ds_read_b128 v[92:95], v180 offset:38912\n\tv_subrev_u32 v180, 73728, v180\n\t"
      "v_mfma_f32_16x16x32_bf16 v[48:51], v[148:151], v[140:143], v[48:51]\n\t"
      "ds_read_b128 v[96:99], v182 offset:36864\n\t"
      "v_mfma_f32_16x16x32_bf16 v[52:55], v[152:155], v[140:143], v[52:55]\n\t"
      "ds_read_b128 v[100:103], v182 offset:38912\n\tv_subrev_u32 v182, 73728, v182\n\t"
      "v_mfma_f32_16x16x32_bf16 v[56:59], v[148:151], v[144:147], v[56:59]\n\t"
      "ds_read_b128 v[72:75], v176 offset:36864\n\t"
      "v_mfma_f32_16x16x32_bf16 v[60:63], v[152:155], v[144:147], v[60:63]\n\t"
      "v_mfma_f32_16x16x32_bf16 v[0:3], v[148:151], v[116:119], v[0:3]\n\t"
      "ds_read_b64_tr_b16 v[112:113], v186 offset:36864\n\t"
      "v_mfma_f32_16x16x32_bf16 v[4:7], v[152:155], v[116:119], v[4:7]\n\t"
      "ds_read_b64_tr_b16 v[114:115], v186 offset:37120\n\t"
      "v_mfma_f32_16x16x32_bf16 v[64:67], v[156:159], v[116:119], v[64:67]\n\t"
      "s_cmp_lg_u32 %[wm], 1\n\t"
      "s_cbranch_scc1 29f\n\t"
      "v_mfma_f32_16x16x32_bf16 v[168:171], v[156:159], v[172:175], v[168:171]\n\t"
      "29:\n\t"
      "ds_read_b128 v[76:79], v176 offset:38912\n\tv_subrev_u32 v176, 73728, v176\n\t"
      "v_mfma_f32_16x16x32_bf16 v[8:11], v[148:151], v[120:123], v[8:11]\n\t"
      "v_mfma_f32_16x16x32_bf16 v[12:15], v[152:155], v[120:123], v[12:15]\n\t"
      "ds_read_b128 v[124:127], v179 offset:36864\n\t"
      "v_mfma_f32_16x16x32_bf16 v[68:71], v[156:159], v[120:123], v[68:71]\n\t"
      "ds_read_b128 v[128:131], v179 offset:38912\n\tv_subrev_u32 v179, 73728, v179\n\t"
      "s_waitcnt lgkmcnt(13)\n\t"
      "v_mfma_f32_16x16x32_bf16 v[16:19], v[104:107], v[80:83], v[16:19]\n\t"
      "ds_read_b128 v[132:135], v181 offset:36864\n\t"
      "s_waitcnt lgkmcnt(12)\n\t"
      "v_mfma_f32_16x16x32_bf16 v[20:23], v[108:111], v[80:83], v[20:23]\n\t"
      "ds_read_b128 v[136:139], v181 offset:38912\n\tv_subrev_u32 v181, 73728, v181\n\t"
      "s_waitcnt lgkmcnt(12)\n\t"
      "v_mfma_f32_16x16x32_bf16 v[24:27], v[104:107], v[84:87], v[24:27]\n\t"
      "v_mfma_f32_16x16x32_bf16 v[28:31], v[108:111], v[84:87], v[28:31]\n\t"
      "ds_read_b128 v[140:143], v183 offset:36864\n\t"
      "v_mfma_f32_16x16x32_bf16 v[160:163], v[104:107], v[172:175], v[160:163]\n\t"
      "ds_read_b128 v[144:147], v183 offset:38912\n\tv_subrev_u32 v183, 73728, v183\n\t"
      "s_waitcnt lgkmcnt(13)\n\t"
      "v_mfma_f32_16x16x32_bf16 v[32:35], v[104:107], v[88:91], v[32:35]\n\t"
      "ds_read_b128 v[116:119], v177 offset:36864\n\t"
      "v_mfma_f32_16x16x32_bf16 v[36:39], v[108:111], v[88:91], v[36:39]\n\t"
      "s_waitcnt lgkmcnt(13)\n\t"
      "v_mfma_f32_16x16x32_bf16 v[40:43], v[104:107], v[92:95], v[40:43]\n\t"
      "ds_read_b64_tr_b16 v[148:149], v184 offset:40960\n\t"
      "v_mfma_f32_16x16x32_bf16 v[44:47], v[108:111], v[92:95], v[44:47]\n\t"
      "ds_read_b64_tr_b16 v[150:151], v184 offset:41984\n\tv_subrev_u32 v184, 73728, v184\n\t"
      "v_mfma_f32_16x16x32_bf16 v[164:167], v[108:111], v[172:175], v[164:167]\n\t"
      "ds_read_b64_tr_b16 v[152:153], v185 offset:40960\n\t"
      "ds_read_b32 v189, v187 offset:64\n\t"
      "s_waitcnt lgkmcnt(14)\n\t"
      "v_mfma_f32_16x16x32_bf16 v[48:51], v[104:107], v[96:99], v[48:51]\n\t"
      "ds_read_b64_tr_b16 v[154:155], v185 offset:41984\n\tv_subrev_u32 v185, 73728, v185\n\t"
      "v_mfma_f32_16x16x32_bf16 v[52:55], v[108:111], v[96:99], v[52:55]\n\t"
      "v_mfma_f32_16x16x32_bf16 v[56:59], v[104:107], v[100:103], v[56:59]\n\t"
      "ds_read_b64_tr_b16 v[156:157], v186 offset:37888\n\t"
      "v_mfma_f32_16x16x32_bf16 v[60:63], v[108:111], v[100:103], v[60:63]\n\t"
      "ds_read_b64_tr_b16 v[158:159], v186 offset:38144\n\tv_subrev_u32 v186, 73728, v186\n\t"
      "s_waitcnt lgkmcnt(14)\n\t"
      "v_mfma_f32_16x16x32_bf16 v[0:3], v[104:107], v[72:75], v[0:3]\n\t"
      "ds_read_b128 v[120:123], v177 offset:38912\n\tv_subrev_u32 v177, 73728, v177\n\t"
      "v_mov_b32 v188, 16\n\t"
      "ds_write_b32 v187, v188\n\t"
      "v_mfma_f32_16x16x32_bf16 v[4:7], v[108:111], v[72:75], v[4:7]\n\t"
      "v_mfma_f32_16x16x32_bf16 v[64:67], v[112:115], v[72:75], v[64:67]\n\t"
      "v_mfma_f32_16x16x32_bf16 v[252:255], v[112:115], v[172:175], 0\n\t"
      "s_waitcnt lgkmcnt(14)\n\t"
      "v_mfma_f32_16x16x32_bf16 v[8:11], v[104:107], v[76:79], v[8:11]\n\t"
      "v_mfma_f32_16x16x32_bf16 v[12:15], v[108:111], v[76:79], v[12:15]\n\t"
      "ds_write_b128 v190, v[168:171]\n\t"
      "v_mov_b32 v188, 1\n\t"
      "s_mov_b64 exec, 1\n\t"
      "ds_add_u32 v191, v188 offset:4\n\t"
      "s_mov_b64 exec, -1\n\t"
      "v_mfma_f32_16x16x32_bf16 v[68:71], v[112:115], v[76:79], v[68:71]\n\t"
      "s_waitcnt lgkmcnt(0)\n\t"
      "v_cmp_gt_u32 vcc, 17, v189\n\t"
      "v_mfma_f32_16x16x32_bf16 v[16:19], v[148:151], v[124:127], v[16:19]\n\t"
      "s_cbranch_vccnz 30f\n\t"
      "31:\n\t"
      "ds_read_b64_tr_b16 v[104:105], v184 offset:0\n\t"
      "v_mfma_f32_16x16x32_bf16 v[20:23], v[152:155], v[124:127], v[20:23]\n\t"
      "ds_read_b64_tr_b16 v[106:107], v184 offset:1024\n\t"
      "v_mfma_f32_16x16x32_bf16 v[24:27], v[148:151], v[128:131], v[24:27]\n\t"
      "ds_read_b128 v[80:83], v178 offset:0\n\t"
      "v_mfma_f32_16x16x32_bf16 v[28:31], v[152:155], v[128:131], v[28:31]\n\t"
      "ds_read_b64_tr_b16 v[108:109], v185 offset:0\n\t"
      "v_mfma_f32_16x16x32_bf16 v[160:163], v[148:151], v[172:175], v[160:163]\n\t"
      "v_mfma_f32_16x16x32_bf16 v[32:35], v[148:151], v[132:135], v[32:35]\n\t"
      "ds_read_b64_tr_b16 v[110:111], v185 offset:1024\n\t"
      "v_mfma_f32_16x16x32_bf16 v[36:39], v[152:155], v[132:135], v[36:39]\n\t"
      "ds_read_b128 v[84:87], v178 offset:2048\n\t"
      "v_mfma_f32_16x16x32_bf16 v[40:43], v[148:151], v[136:139], v[40:43]\n\t"
      "ds_read_b128 v[88:91], v180 offset:0\n\t"
      "v_mfma_f32_16x16x32_bf16 v[44:47], v[152:155], v[136:139], v[44:47]\n\t"
      "v_mfma_f32_16x16x32_bf16 v[164:167], v[152:155], v[172:175], v[164:167]\n\t"
      "ds_read_b128 v[92:95], v180 offset:2048\n\t"
      "v_mfma_f32_16x16x32_bf16 v[48:51], v[148:151], v[140:143], v[48:51]\n\t"
      "ds_read_b128 v[96:99], v182 offset:0\n\t"
      "v_mfma_f32_16x16x32_bf16 v[52:55], v[152:155], v[140:143], v[52:55]\n\t"
      "ds_read_b128 v[100:103], v182 offset:2048\n\t"
      "v_mfma_f32_16x16x32_bf16 v[56:59], v[148:151], v[144:147], v[56:59]\n\t"
      "v_mfma_f32_16x16x32_bf16 v[60:63], v[152:155], v[144:147], v[60:63]\n\t"
      "ds_read_b128 v[72:75], v176 offset:0\n\t"
      "v_mfma_f32_16x16x32_bf16 v[0:3], v[148:151], v[116:119], v[0:3]\n\t"
      "ds_read_b64_tr_b16 v[112:113], v186 offset:0\n\t"
      "v_mfma_f32_16x16x32_bf16 v[4:7], v[152:155], v[116:119], v[4:7]\n\t"
      "ds_read_b64_tr_b16 v[114:115], v186 offset:256\n\t"
      "v_mfma_f32_16x16x32_bf16 v[64:67], v[156:159], v[116:119], v[64:67]\n\t"
      "v_mfma_f32_16x16x32_bf16 v[252:255], v[156:159], v[172:175], v[252:255]\n\t"
      "ds_read_b128 v[76:79], v176 offset:2048\n\t"
      "v_mfma_f32_16x16x32_bf16 v[8:11], v[148:151], v[120:123], v[8:11]\n\t"
      "ds_read_b128 v[124:127], v179 offset:0\n\t"
      "v_mfma_f32_16x16x32_bf16 v[12:15], v[152:155], v[120:123], v[12:15]\n\t"
      "ds_read_b128 v[128:131], v179 offset:2048\n\t"
      "v_mfma_f32_16x16x32_bf16 v[68:71], v[156:159], v[120:123], v[68:71]\n\t"
      "s_waitcnt lgkmcnt(13)\n\t"
      "v_mfma_f32_16x16x32_bf16 v[16:19], v[104:107], v[80:83], v[16:19]\n\t"
      "ds_read_b128 v[132:135], v181 offset:0\n\t"
      "s_waitcnt lgkmcnt(12)\n\t"
      "v_mfma_f32_16x16x32_bf16 v[20:23], v[108:111], v[80:83], v[20:23]\n\t"
      "ds_read_b128 v[136:139], v181 offset:2048\n\t"
      "s_waitcnt lgkmcnt(12)\n\t"
      "v_mfma_f32_16x16x32_bf16 v[24:27], v[104:107], v[84:87], v[24:27]\n\t"
      "ds_read_b128 v[140:143], v183 offset:0\n\t"
      "v_mfma_f32_16x16x32_bf16 v[28:31], v[108:111], v[84:87], v[28:31]\n\t"
      "v_mfma_f32_16x16x32_bf16 v[160:163], v[104:107], v[172:175], v[160:163]\n\t"
      "ds_read_b128 v[144:147], v183 offset:2048\n\t"
      "s_waitcnt lgkmcnt(13)\n\t"
      "v_mfma_f32_16x16x32_bf16 v[32:35], v[104:107], v[88:91], v[32:35]\n\t"
      "ds_read_b128 v[116:119], v177 offset:0\n\t"
      "v_mfma_f32_16x16x32_bf16 v[36:39], v[108:111], v[88:91], v[36:39]\n\t"
      "ds_read_b64_tr_b16 v[148:149], v184 offset:4096\n\t"
      "s_waitcnt lgkmcnt(14)\n\t"
      "v_mfma_f32_16x16x32_bf16 v[40:43], v[104:107], v[92:95], v[40:43]\n\t"
      "v_mfma_f32_16x16x32_bf16 v[44:47], v[108:111], v[92:95], v[44:47]\n\t"
      "ds_read_b64_tr_b16 v[150:151], v184 offset:5120\n\t"
      "v_mfma_f32_16x16x32_bf16 v[164:167], v[108:111], v[172:175], v[164:167]\n\t"
      "ds_read_b64_tr_b16 v[152:153], v185 offset:4096\n\t"
      "ds_read_b32 v189, v187 offset:64\n\t"
      "s_waitcnt lgkmcnt(14)\n\t"
      "v_mfma_f32_16x16x32_bf16 v[48:51], v[104:107], v[96:99], v[48:51]\n\t"
      "ds_read_b64_tr_b16 v[154:155], v185 offset:5120\n\t"
      "v_mfma_f32_16x16x32_bf16 v[52:55], v[108:111], v[96:99], v[52:55]\n\t"
      "v_mfma_f32_16x16x32_bf16 v[56:59], v[104:107], v[100:103], v[56:59]\n\t"
      "ds_read_b64_tr_b16 v[156:157], v186 offset:1024\n\t"
      "v_mfma_f32_16x16x32_bf16 v[60:63], v[108:111], v[100:103], v[60:63]\n\t"
      "ds_read_b64_tr_b16 v[158:159], v186 offset:1280\n\t"
      "s_waitcnt lgkmcnt(14)\n\t"
      "v_mfma_f32_16x16x32_bf16 v[0:3], v[104:107], v[72:75], v[0:3]\n\t"
      "ds_read_b128 v[120:123], v177 offset:2048\n\t"
      "v_mfma_f32_16x16x32_bf16 v[4:7], v[108:111], v[72:75], v[4:7]\n\t"
      "v_mfma_f32_16x16x32_bf16 v[64:67], v[112:115], v[72:75], v[64:67]\n\t"
      "v_mfma_f32_16x16x32_bf16 v[252:255], v[112:115], v[172:175], v[252:255]\n\t"
      "s_waitcnt lgkmcnt(14)\n\t"
      "v_mfma_f32_16x16x32_bf16 v[8:11], v[104:107], v[76:79], v[8:11]\n\t"
      "v_mfma_f32_16x16x32_bf16 v[12:15], v[108:111], v[76:79], v[12:15]\n\t"
      "v_mfma_f32_16x16x32_bf16 v[68:71], v[112:115], v[76:79], v[68:71]\n\t"
      "s_waitcnt lgkmcnt(0)\n\t"
      "v_cmp_gt_u32 vcc, 18, v189\n\t"
      "v_mfma_f32_16x16x32_bf16 v[16:19], v[148:151], v[124:127], v[16:19]\n\t"
      "s_cbranch_vccnz 32f\n\t"
      "33:\n\t"
      "ds_read_b64_tr_b16 v[104:105], v184 offset:36864\n\t"
      "v_mfma_f32_16x16x32_bf16 v[20:23], v[152:155], v[124:127], v[20:23]\n\t"
      "ds_read_b64_tr_b16 v[106:107], v184 offset:37888\n\t"
      "v_mfma_f32_16x16x32_bf16 v[24:27], v[148:151], v[128:131], v[24:27]\n\t"
      "ds_read_b128 v[80:83], v178 offset:36864\n\t"
      "v_mfma_f32_16x16x32_bf16 v[28:31], v[152:155], v[128:131], v[28:31]\n\t"
      "ds_read_b64_tr_b16 v[108:109], v185 offset:36864\n\t"
      "v_mfma_f32_16x16x32_bf16 v[160:163], v[148:151], v[172:175], v[160:163]\n\t"
      "v_mfma_f32_16x16x32_bf16 v[32:35], v[148:151], v[132:135], v[32:35]\n\t"
      "ds_read_b64_tr_b16 v[110:111], v185 offset:37888\n\t"
      "v_mfma_f32_16x16x32_bf16 v[36:39], v[152:155], v[132:135], v[36:39]\n\t"
      "ds_read_b128 v[84:87], v178 offset:38912\n\tv_add_u32 v178, 73728, v178\n\t"
      "v_mfma_f32_16x16x32_bf16 v[40:43], v[148:151], v[136:139], v[40:43]\n\t"
      "ds_read_b128 v[88:91], v180 offset:36864\n\t"
      "v_mfma_f32_16x16x32_bf16 v[44:47], v[152:155], v[136:139], v[44:47]\n\t"
      "v_mfma_f32_16x16x32_bf16 v[164:167], v[152:155], v[172:175], v[164:167]\n\t"
      "ds_read_b128 v[92:95], v180 offset:38912\n\tv_add_u32 v180, 73728, v180\n\t"
      "v_mfma_f32_16x16x32_bf16 v[48:51], v[148:151], v[140:143], v[48:51]\n\t"
      "ds_read_b128 v[96:99], v182 offset:36864\n\t"
      "v_mfma_f32_16x16x32_bf16 v[52:55], v[152:155], v[140:143], v[52:55]\n\t"
      "ds_read_b128 v[100:103], v182 offset:38912\n\tv_add_u32 v182, 73728, v182\n\t"
      "v_mfma_f32_16x16x32_bf16 v[56:59], v[148:151], v[144:147], v[56:59]\n\t"
      "v_mfma_f32_16x16x32_bf16 v[60:63], v[152:155], v[144:147], v[60:63]\n\t"
      "ds_read_b128 v[72:75], v176 offset:36864\n\t"
      "v_mfma_f32_16x16x32_bf16 v[0:3], v[148:151], v[116:119], v[0:3]\n\t"
      "ds_read_b64_tr_b16 v[112:113], v186 offset:36864\n\t"
      "v_mfma_f32_16x16x32_bf16 v[4:7], v[152:155], v[116:119], v[4:7]\n\t"
      "ds_read_b64_tr_b16 v[114:115], v186 offset:37120\n\t"
      "v_mfma_f32_16x16x32_bf16 v[64:67], v[156:159], v[116:119], v[64:67]\n\t"
      "v_mfma_f32_16x16x32_bf16 v[252:255], v[156:159], v[172:175], v[252:255]\n\t"
      "ds_read_b128 v[76:79], v176 offset:38912\n\tv_add_u32 v176, 73728, v176\n\t"
      "v_mfma_f32_16x16x32_bf16 v[8:11], v[148:151], v[120:123], v[8:11]\n\t"
      "ds_read_b128 v[124:127], v179 offset:36864\n\t"
      "v_mfma_f32_16x16x32_bf16 v[12:15], v[152:155], v[120:123], v[12:15]\n\t"
      "ds_read_b128 v[128:131], v179 offset:38912\n\tv_add_u32 v179, 73728, v179\n\t"
      "v_mfma_f32_16x16x32_bf16 v[68:71], v[156:159], v[120:123], v[68:71]\n\t"
      "s_waitcnt lgkmcnt(13)\n\t"
      "v_mfma_f32_16x16x32_bf16 v[16:19], v[104:107], v[80:83], v[16:19]\n\t"
      "ds_read_b128 v[132:135], v181 offset:36864\n\t"
      "s_waitcnt lgkmcnt(12)\n\t"
      "v_mfma_f32_16x16x32_bf16 v[20:23], v[108:111], v[80:83], v[20:23]\n\t"
      "ds_read_b128 v[136:139], v181 offset:38912\n\tv_add_u32 v181, 73728, v181\n\t"
      "s_waitcnt lgkmcnt(12)\n\t"
      "v_mfma_f32_16x16x32_bf16 v[24:27], v[104:107], v[84:87], v[24:27]\n\t"
      "ds_read_b128 v[140:143], v183 offset:36864\n\t"
      "v_mfma_f32_16x16x32_bf16 v[28:31], v[108:111], v[84:87], v[28:31]\n\t"
      "v_mfma_f32_16x16x32_bf16 v[160:163], v[104:107], v[172:175], v[160:163]\n\t"
      "ds_read_b128 v[144:147], v183 offset:38912\n\tv_add_u32 v183, 73728, v183\n\t"
      "s_waitcnt lgkmcnt(13)\n\t"
      "v_mfma_f32_16x16x32_bf16 v[32:35], v[104:107], v[88:91], v[32:35]\n\t"
      "ds_read_b128 v[116:119], v177 offset:36864\n\t"
      "v_mfma_f32_16x16x32_bf16 v[36:39], v[108:111], v[88:91], v[36:39]\n\t"
      "ds_read_b64_tr_b16 v[148:149], v184 offset:40960\n\t"
      "s_waitcnt lgkmcnt(14)\n\t"
      "v_mfma_f32_16x16x32_bf16 v[40:43], v[104:107], v[92:95], v[40:43]\n\t"
      "v_mfma_f32_16x16x32_bf16 v[44:47], v[108:111], v[92:95], v[44:47]\n\t"
      "ds_read_b64_tr_b16 v[150:151], v184 offset:41984\n\t"
      "v_mfma_f32_16x16x32_bf16 v[164:167], v[108:111], v[172:175], v[164:167]\n\t"
      "ds_read_b64_tr_b16 v[152:153], v185 offset:40960\n\t"
      "ds_read_b32 v189, v191 offset:4\n\t"
      "s_waitcnt lgkmcnt(14)\n\t"
      "v_mfma_f32_16x16x32_bf16 v[48:51], v[104:107], v[96:99], v[48:51]\n\t"
      "ds_read_b64_tr_b16 v[154:155], v185 offset:41984\n\t"
      "v_mfma_f32_16x16x32_bf16 v[52:55], v[108:111], v[96:99], v[52:55]\n\t"
      "v_mfma_f32_16x16x32_bf16 v[56:59], v[104:107], v[100:103], v[56:59]\n\t"
      "ds_read_b64_tr_b16 v[156:157], v186 offset:37888\n\t"
      "v_mfma_f32_16x16x32_bf16 v[60:63], v[108:111], v[100:103], v[60:63]\n\t"
      "ds_read_b64_tr_b16 v[158:159], v186 offset:38144\n\t"
      "s_waitcnt lgkmcnt(14)\n\t"
      "v_mfma_f32_16x16x32_bf16 v[0:3], v[104:107], v[72:75], v[0:3]\n\t"
      "ds_read_b128 v[120:123], v177 offset:38912\n\tv_add_u32 v177, 73728, v177\n\t"
      "v_mov_b32 v188, 18\n\t"
      "ds_write_b32 v187, v188\n\t"
      "v_mfma_f32_16x16x32_bf16 v[4:7], v[108:111], v[72:75], v[4:7]\n\t"
      "v_mfma_f32_16x16x32_bf16 v[64:67], v[112:115], v[72:75], v[64:67]\n\t"
      "v_mfma_f32_16x16x32_bf16 v[252:255], v[112:115], v[172:175], v[252:255]\n\t"
      "s_waitcnt lgkmcnt(14)\n\t"
      "v_mfma_f32_16x16x32_bf16 v[8:11], v[104:107], v[76:79], v[8:11]\n\t"
      "v_mfma_f32_16x16x32_bf16 v[12:15], v[108:111], v[76:79], v[12:15]\n\t"
      "v_mfma_f32_16x16x32_bf16 v[68:71], v[112:115], v[76:79], v[68:71]\n\t"
      "s_branch 3f\n\t"
      "2:\n\t"
      "ds_read_b32 v189, v191 offset:4\n\t"
      "3:\n\t"
      "s_waitcnt lgkmcnt(0)\n\t"
      "v_cmp_gt_u32 vcc, 4, v189\n\t"
      "s_cbranch_vccnz 2b\n\t"
      "v_mfma_f32_16x16x32_bf16 v[16:19], v[148:151], v[124:127], v[16:19]\n\t"
      "v_mfma_f32_16x16x32_bf16 v[20:23], v[152:155], v[124:127], v[20:23]\n\t"
      "v_mfma_f32_16x16x32_bf16 v[24:27], v[148:151], v[128:131], v[24:27]\n\t"
      "v_mfma_f32_16x16x32_bf16 v[28:31], v[152:155], v[128:131], v[28:31]\n\t"
      "v_mfma_f32_16x16x32_bf16 v[160:163], v[148:151], v[172:175], v[160:163]\n\t"
      "v_mfma_f32_16x16x32_bf16 v[32:35], v[148:151], v[132:135], v[32:35]\n\t"
      "v_mfma_f32_16x16x32_bf16 v[36:39], v[152:155], v[132:135], v[36:39]\n\t"
      "v_mfma_f32_16x16x32_bf16 v[40:43], v[148:151], v[136:139], v[40:43]\n\t"
      "v_mfma_f32_16x16x32_bf16 v[44:47], v[152:155], v[136:139], v[44:47]\n\t"
      "v_mfma_f32_16x16x32_bf16 v[164:167], v[152:155], v[172:175], v[164:167]\n\t"
      "v_mfma_f32_16x16x32_bf16 v[48:51], v[148:151], v[140:143], v[48:51]\n\t"
      "v_mfma_f32_16x16x32_bf16 v[52:55], v[152:155], v[140:143], v[52:55]\n\t"
      "v_mfma_f32_16x16x32_bf16 v[56:59], v[148:151], v[144:147], v[56:59]\n\t"
      "v_mfma_f32_16x16x32_bf16 v[60:63], v[152:155], v[144:147], v[60:63]\n\t"
      "v_mfma_f32_16x16x32_bf16 v[0:3], v[148:151], v[116:119], v[0:3]\n\t"
      "v_mfma_f32_16x16x32_bf16 v[4:7], v[152:155], v[116:119], v[4:7]\n\t"
      "v_mfma_f32_16x16x32_bf16 v[64:67], v[156:159], v[116:119], v[64:67]\n\t"
      "v_mfma_f32_16x16x32_bf16 v[252:255], v[156:159], v[172:175], v[252:255]\n\t"
      "v_mfma_f32_16x16x32_bf16 v[8:11], v[148:151], v[120:123], v[8:11]\n\t"
      "v_mfma_f32_16x16x32_bf16 v[12:15], v[152:155], v[120:123], v[12:15]\n\t"
      "v_mfma_f32_16x16x32_bf16 v[68:71], v[156:159], v[120:123], v[68:71]\n\t"
      "s_branch 9f\n\t"
      "13:\n\t"
      "ds_read_b32 v189, v187 offset:64\n\t"
      "s_waitcnt lgkmcnt(0)\n\t"
      "v_cmp_gt_u32 vcc, 11, v189\n\t"
      "s_cbranch_vccnz 13b\n\t"
      "s_branch 14b\n\t"
      "17:\n\t"
      "ds_read_b32 v189, v187 offset:64\n\t"
      "s_waitcnt lgkmcnt(0)\n\t"
      "v_cmp_gt_u32 vcc, 12, v189\n\t"
      "s_cbranch_vccnz 17b\n\t"
      "s_branch 18b\n\t"
      "21:\n\t"
      "ds_read_b32 v189, v187 offset:64\n\t"
      "s_waitcnt lgkmcnt(0)\n\t"
      "v_cmp_gt_u32 vcc, 13, v189\n\t"
      "s_cbranch_vccnz 21b\n\t"
      "s_branch 22b\n\t"
      "30:\n\t"
      "ds_read_b32 v189, v187 offset:64\n\t"
      "s_waitcnt lgkmcnt(0)\n\t"
      "v_cmp_gt_u32 vcc, 17, v189\n\t"
      "s_cbranch_vccnz 30b\n\t"
      "s_branch 31b\n\t"
      "32:\n\t"
      "ds_read_b32 v189, v187 offset:64\n\t"
      "s_waitcnt lgkmcnt(0)\n\t"
      "v_cmp_gt_u32 vcc, 18, v189\n\t"
      "s_cbranch_vccnz 32b\n\t"
      "s_branch 33b\n\t"
      "9:\n\t"
      "s_nop 7\n\t"
      "s_nop 3\n\t"
      : "={v[0:3]}"(ac[0]), "={v[4:7]}"(ac[1]), "={v[8:11]}"(ac[2]), "={v[12:15]}"(ac[3]), "={v[16:19]}"(ac[4]), "={v[20:23]}"(ac[5]), "={v[24:27]}"(ac[6]), "={v[28:31]}"(ac[7]), "={v[32:35]}"(ac[8]), "={v[36:39]}"(ac[9]), "={v[40:43]}"(ac[10]), "={v[44:47]}"(ac[11]), "={v[48:51]}"(ac[12]), "={v[52:55]}"(ac[13]), "={v[56:59]}"(ac[14]), "={v[60:63]}"(ac[15]), "={v[64:67]}"(ac[16]), "={v[68:71]}"(ac[17]), [t] "=&s"(tmp), "+{v[176:183]}"(ad), "+{v[184:186]}"(bd), "={v[160:163]}"(c[0]), "={v[164:167]}"(c[1]), "={v[252:255]}"(c[2])
      : "{v190}"(cw), "{v191}"(lb + FL_C9A), [wm] "s"(wm),
        "{v187}"(lb + FL_CP + wm * 16 + (lane & 3) * 4) : "memory", "scc", "vcc", "v188", "v189", "v72", "v73", "v74", "v75", "v76", "v77", "v78", "v79", "v80", "v81", "v82", "v83", "v84", "v85", "v86", "v87", "v88", "v89", "v90", "v91", "v92", "v93", "v94", "v95", "v96", "v97", "v98", "v99", "v100", "v101", "v102", "v103", "v104", "v105", "v106", "v107", "v108", "v109", "v110", "v111", "v112", "v113", "v114", "v115", "v116", "v117", "v118", "v119", "v120", "v121", "v122", "v123", "v124", "v125", "v126", "v127", "v128", "v129", "v130", "v131", "v132", "v133", "v134", "v135", "v136", "v137", "v138", "v139", "v140", "v141", "v142", "v143", "v144", "v145", "v146", "v147", "v148", "v149", "v150", "v151", "v152", "v153", "v154", "v155", "v156", "v157", "v158", "v159", "v168", "v169", "v170", "v171", "v172", "v173", "v174", "v175");
  }
  TS(4);
  u32x2 z1[18];
  {
    const int lane = lane_id(), r = lane & 15, g = lane >> 4, j = ROTC;
    f32x4 rc[8];
#pragma unroll
    for (int k = 0; k < 8; ++k) { const int m = (k + 2) & 7; rc[m] = *(const LAS f32x4*)(smem + T_ROW + ((m ^ (2 * wm)) * 16 + r) * 16); }
    const char* cb = smem + T_CW + g * 16;
    c[2] += (*(const LAS f32x4*)cb + *(const LAS f32x4*)(cb + 64)) + (*(const LAS f32x4*)(cb + 128) + *(const LAS f32x4*)(cb + 192));
    char* f3p = ws + WS_F3 + (tn * 16 + tm) * 256 + lane * 4;
    epi_b<0, EB1>(ac, rc, c, z1);
    epi_b<EB1, EB2>(ac, rc, c, z1);
    FENCE();
    TS(19);
    lds_wait1(lb + FL_F3, 1u);
    TS(13);
    auto zr = RSRC(ws + WS_ZN + (size_t)tn * 32 * CSL, 32 * CSL);
    unsigned so0 = j * 4 * CSL, so1 = so0 + 9216, cnt, d0, dz;
    u32x2 bd = {lb + RING + 4 * CSL + 16384 + lane * 16, (unsigned)(lane * 16 + wm * 2048)};
    u32x16 zf[3];
    unsigned cs0, cs1;
    asm volatile(
      "buffer_load_dwordx4 v[108:111], v185, %[zr], %[so0] offen offset:0 sc1\n\t"
      "buffer_load_dwordx4 v[112:115], v185, %[zr], %[so0] offen offset:1024 sc1\n\ts_add_u32 %[so0], %[so0], 18432\n\t"
      "buffer_load_dwordx4 v[116:119], v185, %[zr], %[so1] offen offset:0 sc1\n\t"
      "buffer_load_dwordx4 v[120:123], v185, %[zr], %[so1] offen offset:1024 sc1\n\ts_add_u32 %[so1], %[so1], 18432\n\t"
      "buffer_load_dwordx4 v[124:127], v185, %[zr], %[so0] offen offset:0 sc1\n\t"
      "buffer_load_dwordx4 v[128:131], v185, %[zr], %[so0] offen offset:1024 sc1\n\ts_add_u32 %[so0], %[so0], 18432\n\t"
      "buffer_load_dwordx4 v[132:135], v185, %[zr], %[so1] offen offset:0 sc1\n\t"
      "buffer_load_dwordx4 v[136:139], v185, %[zr], %[so1] offen offset:1024 sc1\n\ts_add_u32 %[so1], %[so1], 18432\n\t"
      "buffer_load_dwordx4 v[140:143], v185, %[zr], %[so0] offen offset:0 sc1\n\t"
      "buffer_load_dwordx4 v[144:147], v185, %[zr], %[so0] offen offset:1024 sc1\n\ts_add_u32 %[so0], %[so0], 18432\n\t"
      "buffer_load_dwordx4 v[148:151], v185, %[zr], %[so1] offen offset:0 sc1\n\t"
      "buffer_load_dwordx4 v[152:155], v185, %[zr], %[so1] offen offset:1024 sc1\n\ts_add_u32 %[so1], %[so1], 18432\n\t"
      : "={v[108:123]}"(zf[0]), "={v[124:139]}"(zf[1]), "={v[140:155]}"(zf[2]), [so0] "+s"(so0), [so1] "+s"(so1) : "{v[184:185]}"(bd), [zr] "s"(zr) : "memory", "scc");
    asm volatile("" : "+v"(c[0]), "+v"(c[1]), "+v"(c[2]));
    epi_b<EB2, 18>(ac, rc, c, z1);
    const unsigned t4 = lb + T_B4 + r * 16;
    u32x4 ba = {t4 + wm * 512, t4 + (wm ^ 1) * 512, t4 + (wm ^ 2) * 512, t4 + (wm ^ 3) * 512};
    auto ur = RSRC(ws + WS_U + (size_t)(tm * 8 * US + (tn >> 1) * 9216) + (tn & 1) * 4096 - wm * 1024, 8 * US);
    TS(5);
    asm volatile(
      "ds_read_b128 v[16:19], v187 offset:0\n\t"
      "ds_read_b128 v[20:23], v187 offset:0\n\t"
      "ds_read_b128 v[24:27], v187 offset:256\n\t"
      "ds_read_b128 v[28:31], v187 offset:256\n\t"
      "ds_read_b128 v[32:35], v188 offset:0\n\t"
      "ds_read_b128 v[36:39], v188 offset:0\n\t"
      "ds_read_b128 v[40:43], v188 offset:256\n\t"
      "ds_read_b128 v[44:47], v188 offset:256\n\t"
      "ds_read_b128 v[0:3], v186 offset:0\n\t"
      "ds_read_b128 v[4:7], v186 offset:0\n\t"
      "ds_read_b128 v[64:67], v186 offset:0\n\t"
      "ds_read_b128 v[8:11], v186 offset:256\n\t"
      "ds_read_b128 v[12:15], v186 offset:256\n\t"
      "ds_read_b128 v[68:71], v186 offset:256\n\t"
      "ds_read_b128 v[48:51], v189 offset:0\n\t"
      "ds_read_b128 v[52:55], v189 offset:0\n\t"
      "ds_read_b128 v[56:59], v189 offset:256\n\t"
      "ds_read_b128 v[60:63], v189 offset:256\n\t"
      "s_cmp_eq_u32 %[j], 7\n\t"
      "s_cselect_b32 %[dz], %[dw], 18432\n\t"
      "s_mov_b32 %[d0], 4294856704\n\t"
      "s_barrier\n\t"
      "ds_read_b128 v[72:75], v178 offset:0\n\t"
      "ds_read_b128 v[76:79], v178 offset:2048\n\t"
      "ds_read_b128 v[80:83], v180 offset:0\n\t"
      "ds_read_b128 v[84:87], v180 offset:2048\n\t"
      "ds_read_b128 v[88:91], v184 offset:0\n\t"
      "ds_read_b128 v[92:95], v176 offset:0\n\t"
      "ds_read_b128 v[96:99], v176 offset:2048\n\t"
      "ds_read_b128 v[100:103], v182 offset:0\n\t"
      "s_waitcnt vmcnt(10)\n\t"
      "s_waitcnt lgkmcnt(7)\n\t"
      "v_mfma_f32_16x16x32_bf16 v[16:19], v[108:111], v[72:75], v[16:19]\n\t"
      "ds_read_b128 v[104:107], v182 offset:2048\n\t"
      "v_mfma_f32_16x16x32_bf16 v[20:23], v[112:115], v[72:75], v[20:23]\n\t"
      "s_waitcnt lgkmcnt(7)\n\t"
      "v_mfma_f32_16x16x32_bf16 v[24:27], v[108:111], v[76:79], v[24:27]\n\t"
      "ds_read_b128 v[72:75], v179 offset:0\n\t"
      "v_mfma_f32_16x16x32_bf16 v[28:31], v[112:115], v[76:79], v[28:31]\n\t"
      "s_waitcnt lgkmcnt(7)\n\t"
      "v_mfma_f32_16x16x32_bf16 v[32:35], v[108:111], v[80:83], v[32:35]\n\t"
      "ds_read_b128 v[76:79], v179 offset:2048\n\t"
      "v_mfma_f32_16x16x32_bf16 v[36:39], v[112:115], v[80:83], v[36:39]\n\t"
      "s_waitcnt lgkmcnt(7)\n\t"
      "v_mfma_f32_16x16x32_bf16 v[40:43], v[108:111], v[84:87], v[40:43]\n\t"
      "ds_read_b128 v[80:83], v181 offset:0\n\t"
      "v_mfma_f32_16x16x32_bf16 v[44:47], v[112:115], v[84:87], v[44:47]\n\t"
      "s_waitcnt lgkmcnt(6)\n\t"
      "v_mfma_f32_16x16x32_bf16 v[0:3], v[108:111], v[92:95], v[0:3]\n\t"
      "ds_read_b128 v[84:87], v181 offset:2048\n\t"
      "v_mfma_f32_16x16x32_bf16 v[4:7], v[112:115], v[92:95], v[4:7]\n\t"
      "v_mfma_f32_16x16x32_bf16 v[64:67], v[88:91], v[92:95], v[64:67]\n\t"
      "s_waitcnt lgkmcnt(6)\n\t"
      "v_mfma_f32_16x16x32_bf16 v[8:11], v[108:111], v[96:99], v[8:11]\n\t"
      "v_mfma_f32_16x16x32_bf16 v[12:15], v[112:115], v[96:99], v[12:15]\n\t"
      "v_mfma_f32_16x16x32_bf16 v[68:71], v[88:91], v[96:99], v[68:71]\n\t"
      "ds_read_b128 v[88:91], v184 offset:1024\n\t"
      "s_waitcnt lgkmcnt(6)\n\t"
      "v_mfma_f32_16x16x32_bf16 v[48:51], v[108:111], v[100:103], v[48:51]\n\t"
      "ds_read_b128 v[92:95], v177 offset:0\n\t"
      "v_mfma_f32_16x16x32_bf16 v[52:55], v[112:115], v[100:103], v[52:55]\n\t"
      "ds_read_b128 v[96:99], v177 offset:2048\n\t"
      "s_waitcnt lgkmcnt(7)\n\t"
      "v_mfma_f32_16x16x32_bf16 v[56:59], v[108:111], v[104:107], v[56:59]\n\t"
      "ds_read_b128 v[100:103], v183 offset:0\n\t"
      "v_mfma_f32_16x16x32_bf16 v[60:63], v[112:115], v[104:107], v[60:63]\n\t"
      "s_waitcnt vmcnt(8)\n\t"
      "s_waitcnt lgkmcnt(7)\n\t"
      "v_mfma_f32_16x16x32_bf16 v[16:19], v[116:119], v[72:75], v[16:19]\n\t"
      "ds_read_b128 v[104:107], v183 offset:2048\n\t"
      "v_lshlrev_b32 v228, 16, v192\n\t"
      "v_mfma_f32_16x16x32_bf16 v[20:23], v[120:123], v[72:75], v[20:23]\n\t"
      "v_and_b32 v192, %[msk], v192\n\t"
      "s_waitcnt lgkmcnt(7)\n\t"
      "v_mfma_f32_16x16x32_bf16 v[24:27], v[116:119], v[76:79], v[24:27]\n\t"
      "ds_read_b128 v[72:75], v178 offset:18432\n\t"
      "v_lshlrev_b32 v229, 16, v193\n\t"
      "v_mfma_f32_16x16x32_bf16 v[28:31], v[120:123], v[76:79], v[28:31]\n\t"
      "v_and_b32 v193, %[msk], v193\n\t"
      "s_waitcnt lgkmcnt(7)\n\t"
      "v_mfma_f32_16x16x32_bf16 v[32:35], v[116:119], v[80:83], v[32:35]\n\t"
      "ds_read_b128 v[76:79], v178 offset:20480\n\t"
      "v_add_u32 v178, 36864, v178\n\t"
      "v_mfma_f32_16x16x32_bf16 v[36:39], v[120:123], v[80:83], v[36:39]\n\t"
      "buffer_load_dwordx4 v[156:159], v185, %[zr], %[so0] offen offset:0 sc1\n\t"
      "v_lshlrev_b32 v230, 16, v194\n\t"
      "s_waitcnt lgkmcnt(7)\n\t"
      "v_mfma_f32_16x16x32_bf16 v[40:43], v[116:119], v[84:87], v[40:43]\n\t"
      "ds_read_b128 v[80:83], v180 offset:18432\n\t"
      "v_and_b32 v194, %[msk], v194\n\t"
      "v_mfma_f32_16x16x32_bf16 v[44:47], v[120:123], v[84:87], v[44:47]\n\t"
      "v_lshlrev_b32 v231, 16, v195\n\t"
      "s_waitcnt lgkmcnt(6)\n\t"
      "v_mfma_f32_16x16x32_bf16 v[0:3], v[116:119], v[92:95], v[0:3]\n\t"
      "ds_read_b128 v[84:87], v180 offset:20480\n\t"
      "v_add_u32 v180, 36864, v180\n\t"
      "v_mfma_f32_16x16x32_bf16 v[4:7], v[120:123], v[92:95], v[4:7]\n\t"
      "buffer_load_dwordx4 v[160:163], v185, %[zr], %[so0] offen offset:1024 sc1\n\ts_add_u32 %[so0], %[so0], %[dz]\n\t"
      "v_and_b32 v195, %[msk], v195\n\t"
      "v_mfma_f32_16x16x32_bf16 v[64:67], v[88:91], v[92:95], v[64:67]\n\t"
      "v_lshlrev_b32 v232, 16, v196\n\t"
      "s_waitcnt lgkmcnt(6)\n\t"
      "v_mfma_f32_16x16x32_bf16 v[8:11], v[116:119], v[96:99], v[8:11]\n\t"
      "v_and_b32 v196, %[msk], v196\n\t"
      "v_mfma_f32_16x16x32_bf16 v[12:15], v[120:123], v[96:99], v[12:15]\n\t"
      "v_lshlrev_b32 v233, 16, v197\n\t"
      "v_mfma_f32_16x16x32_bf16 v[68:71], v[88:91], v[96:99], v[68:71]\n\t"
      "ds_read_b128 v[88:91], v184 offset:18432\n\t"
      "buffer_load_dwordx4 v[164:167], v185, %[zr], %[so1] offen offset:0 sc1\n\t"
      "s_waitcnt lgkmcnt(6)\n\t"
      "v_mfma_f32_16x16x32_bf16 v[48:51], v[116:119], v[100:103], v[48:51]\n\t"
      "ds_read_b128 v[92:95], v176 offset:18432\n\t"
      "v_and_b32 v197, %[msk], v197\n\t"
      "v_mfma_f32_16x16x32_bf16 v[52:55], v[120:123], v[100:103], v[52:55]\n\t"
      "ds_read_b128 v[96:99], v176 offset:20480\n\t"
      "v_add_u32 v176, 36864, v176\n\t"
      "v_lshlrev_b32 v234, 16, v198\n\t"
      "s_waitcnt lgkmcnt(7)\n\t"
      "v_mfma_f32_16x16x32_bf16 v[56:59], v[116:119], v[104:107], v[56:59]\n\t"
      "ds_read_b128 v[100:103], v182 offset:18432\n\t"
      "v_and_b32 v198, %[msk], v198\n\t"
      "v_mfma_f32_16x16x32_bf16 v[60:63], v[120:123], v[104:107], v[60:63]\n\t"
      "buffer_load_dwordx4 v[168:171], v185, %[zr], %[so1] offen offset:1024 sc1\n\ts_add_u32 %[so1], %[so1], %[dz]\n\t"
      "v_lshlrev_b32 v235, 16, v199\n\t"
      "s_waitcnt vmcnt(10)\n\t"
      "s_waitcnt lgkmcnt(7)\n\t"
      "v_mfma_f32_16x16x32_bf16 v[16:19], v[124:127], v[72:75], v[16:19]\n\t"
      "ds_read_b128 v[104:107], v182 offset:20480\n\t"
      "v_add_u32 v182, 36864, v182\n\t"
      "v_mfma_f32_16x16x32_bf16 v[20:23], v[128:131], v[72:75], v[20:23]\n\t"
      "v_and_b32 v199, %[msk], v199\n\t"
      "s_waitcnt lgkmcnt(7)\n\t"
      "v_mfma_f32_16x16x32_bf16 v[24:27], v[124:127], v[76:79], v[24:27]\n\t"
      "ds_read_b128 v[72:75], v179 offset:18432\n\t"
      "v_lshlrev_b32 v236, 16, v200\n\t"
      "v_mfma_f32_16x16x32_bf16 v[28:31], v[128:131], v[76:79], v[28:31]\n\t"
      "buffer_load_dwordx4 v[108:111], v185, %[zr], %[so0] offen offset:0 sc1\n\t"
      "v_and_b32 v200, %[msk], v200\n\t"
      "s_waitcnt lgkmcnt(7)\n\t"
      "v_mfma_f32_16x16x32_bf16 v[32:35], v[124:127], v[80:83], v[32:35]\n\t"
      "ds_read_b128 v[76:79], v179 offset:20480\n\t"
      "v_add_u32 v179, 36864, v179\n\t"
      "v_mfma_f32_16x16x32_bf16 v[36:39], v[128:131], v[80:83], v[36:39]\n\t"
      "v_lshlrev_b32 v237, 16, v201\n\t"
      "s_waitcnt lgkmcnt(7)\n\t"
      "v_mfma_f32_16x16x32_bf16 v[40:43], v[124:127], v[84:87], v[40:43]\n\t"
      "ds_read_b128 v[80:83], v181 offset:18432\n\t"
      "v_and_b32 v201, %[msk], v201\n\t"
      "v_mfma_f32_16x16x32_bf16 v[44:47], v[128:131], v[84:87], v[44:47]\n\t"
      "buffer_load_dwordx4 v[112:115], v185, %[zr], %[so0] offen offset:1024 sc1\n\ts_add_u32 %[so0], %[so0], 18432\n\t"
      "v_lshlrev_b32 v238, 16, v202\n\t"
      "s_waitcnt lgkmcnt(6)\n\t"
      "v_mfma_f32_16x16x32_bf16 v[0:3], v[124:127], v[92:95], v[0:3]\n\t"
      "ds_read_b128 v[84:87], v181 offset:20480\n\t"
      "v_add_u32 v181, 36864, v181\n\t"
      "v_mfma_f32_16x16x32_bf16 v[4:7], v[128:131], v[92:95], v[4:7]\n\t"
      "v_and_b32 v202, %[msk], v202\n\t"
      "v_mfma_f32_16x16x32_bf16 v[64:67], v[88:91], v[92:95], v[64:67]\n\t"
      "v_lshlrev_b32 v239, 16, v203\n\t"
      "s_waitcnt lgkmcnt(6)\n\t"
      "v_mfma_f32_16x16x32_bf16 v[8:11], v[124:127], v[96:99], v[8:11]\n\t"
      "buffer_load_dwordx4 v[116:119], v185, %[zr], %[so1] offen offset:0 sc1\n\t"
      "v_mfma_f32_16x16x32_bf16 v[12:15], v[128:131], v[96:99], v[12:15]\n\t"
      "v_and_b32 v203, %[msk], v203\n\t"
      "v_mfma_f32_16x16x32_bf16 v[68:71], v[88:91], v[96:99], v[68:71]\n\t"
      "ds_read_b128 v[88:91], v184 offset:19456\n\t"
      "v_add_u32 v184, 36864, v184\n\t"
      "v_lshlrev_b32 v240, 16, v204\n\t"
      "s_waitcnt lgkmcnt(6)\n\t"
      "v_mfma_f32_16x16x32_bf16 v[48:51], v[124:127], v[100:103], v[48:51]\n\t"
      "ds_read_b128 v[92:95], v177 offset:18432\n\t"
      "v_and_b32 v204, %[msk], v204\n\t"
      "v_mfma_f32_16x16x32_bf16 v[52:55], v[128:131], v[100:103], v[52:55]\n\t"
      "ds_read_b128 v[96:99], v177 offset:20480\n\t"
      "v_add_u32 v177, 36864, v177\n\t"
      "buffer_load_dwordx4 v[120:123], v185, %[zr], %[so1] offen offset:1024 sc1\n\ts_add_u32 %[so1], %[so1], 18432\n\t"
      "s_waitcnt lgkmcnt(7)\n\t"
      "v_mfma_f32_16x16x32_bf16 v[56:59], v[124:127], v[104:107], v[56:59]\n\t"
      "ds_read_b128 v[100:103], v183 offset:18432\n\t"
      "v_lshlrev_b32 v241, 16, v205\n\t"
      "v_mfma_f32_16x16x32_bf16 v[60:63], v[128:131], v[104:107], v[60:63]\n\t"
      "v_and_b32 v205, %[msk], v205\n\t"
      "s_waitcnt vmcnt(12)\n\t"
      "s_waitcnt lgkmcnt(7)\n\t"
      "v_mfma_f32_16x16x32_bf16 v[16:19], v[132:135], v[72:75], v[16:19]\n\t"
      "ds_read_b128 v[104:107], v183 offset:20480\n\t"
      "v_add_u32 v183, 36864, v183\n\t"
      "v_mfma_f32_16x16x32_bf16 v[20:23], v[136:139], v[72:75], v[20:23]\n\t"
      "v_lshlrev_b32 v242, 16, v206\n\t"
      "s_waitcnt lgkmcnt(7)\n\t"
      "v_mfma_f32_16x16x32_bf16 v[24:27], v[132:135], v[76:79], v[24:27]\n\t"
      "s_barrier\n\t"
      "ds_read_b128 v[72:75], v178 offset:0\n\t"
      "v_mfma_f32_16x16x32_bf16 v[28:31], v[136:139], v[76:79], v[28:31]\n\t"
      "v_and_b32 v206, %[msk], v206\n\t"
      "s_waitcnt lgkmcnt(7)\n\t"
      "v_mfma_f32_16x16x32_bf16 v[32:35], v[132:135], v[80:83], v[32:35]\n\t"
      "ds_read_b128 v[76:79], v178 offset:2048\n\t"
      "v_lshlrev_b32 v243, 16, v207\n\t"
      "v_mfma_f32_16x16x32_bf16 v[36:39], v[136:139], v[80:83], v[36:39]\n\t"
      "v_and_b32 v207, %[msk], v207\n\t"
      "s_waitcnt lgkmcnt(7)\n\t"
      "v_mfma_f32_16x16x32_bf16 v[40:43], v[132:135], v[84:87], v[40:43]\n\t"
      "ds_read_b128 v[80:83], v180 offset:0\n\t"
      "v_lshlrev_b32 v244, 16, v208\n\t"
      "v_mfma_f32_16x16x32_bf16 v[44:47], v[136:139], v[84:87], v[44:47]\n\t"
      "v_and_b32 v208, %[msk], v208\n\t"
      "s_waitcnt lgkmcnt(6)\n\t"
      "v_mfma_f32_16x16x32_bf16 v[0:3], v[132:135], v[92:95], v[0:3]\n\t"
      "ds_read_b128 v[84:87], v180 offset:2048\n\t"
      "v_lshlrev_b32 v245, 16, v209\n\t"
      "v_mfma_f32_16x16x32_bf16 v[4:7], v[136:139], v[92:95], v[4:7]\n\t"
      "v_and_b32 v209, %[msk], v209\n\t"
      "v_mfma_f32_16x16x32_bf16 v[64:67], v[88:91], v[92:95], v[64:67]\n\t"
      "v_lshlrev_b32 v246, 16, v210\n\t"
      "s_waitcnt lgkmcnt(6)\n\t"
      "v_mfma_f32_16x16x32_bf16 v[8:11], v[132:135], v[96:99], v[8:11]\n\t"
      "v_and_b32 v210, %[msk], v210\n\t"
      "v_mfma_f32_16x16x32_bf16 v[12:15], v[136:139], v[96:99], v[12:15]\n\t"
      "v_lshlrev_b32 v247, 16, v211\n\t"
      "v_mfma_f32_16x16x32_bf16 v[68:71], v[88:91], v[96:99], v[68:71]\n\t"
      "ds_read_b128 v[88:91], v184 offset:0\n\t"
      "v_and_b32 v211, %[msk], v211\n\t"
      "s_waitcnt lgkmcnt(6)\n\t"
      "v_mfma_f32_16x16x32_bf16 v[48:51], v[132:135], v[100:103], v[48:51]\n\t"
      "ds_read_b128 v[92:95], v176 offset:0\n\t"
      "v_lshlrev_b32 v248, 16, v212\n\t"
      "v_mfma_f32_16x16x32_bf16 v[52:55], v[136:139], v[100:103], v[52:55]\n\t"
      "ds_read_b128 v[96:99], v176 offset:2048\n\t"
      "v_and_b32 v212, %[msk], v212\n\t"
      "s_waitcnt lgkmcnt(7)\n\t"
      "v_mfma_f32_16x16x32_bf16 v[56:59], v[132:135], v[104:107], v[56:59]\n\t"
      "ds_read_b128 v[100:103], v182 offset:0\n\t"
      "v_lshlrev_b32 v249, 16, v213\n\t"
      "v_mfma_f32_16x16x32_bf16 v[60:63], v[136:139], v[104:107], v[60:63]\n\t"
      "v_and_b32 v213, %[msk], v213\n\t"
      "s_waitcnt vmcnt(10)\n\t"
      "s_waitcnt lgkmcnt(7)\n\t"
      "v_mfma_f32_16x16x32_bf16 v[16:19], v[140:143], v[72:75], v[16:19]\n\t"
      "ds_read_b128 v[104:107], v182 offset:2048\n\t"
      "v_lshlrev_b32 v250, 16, v214\n\t"
      "v_mfma_f32_16x16x32_bf16 v[20:23], v[144:147], v[72:75], v[20:23]\n\t"
      "v_and_b32 v214, %[msk], v214\n\t"
      "s_waitcnt lgkmcnt(7)\n\t"
      "v_mfma_f32_16x16x32_bf16 v[24:27], v[140:143], v[76:79], v[24:27]\n\t"
      "ds_read_b128 v[72:75], v179 offset:0\n\t"
      "v_lshlrev_b32 v251, 16, v215\n\t"
      "v_mfma_f32_16x16x32_bf16 v[28:31], v[144:147], v[76:79], v[28:31]\n\t"
      "v_and_b32 v215, %[msk], v215\n\t"
      "s_waitcnt lgkmcnt(7)\n\t"
      "v_mfma_f32_16x16x32_bf16 v[32:35], v[140:143], v[80:83], v[32:35]\n\t"
      "ds_read_b128 v[76:79], v179 offset:2048\n\t"
      "v_lshlrev_b32 v252, 16, v216\n\t"
      "v_mfma_f32_16x16x32_bf16 v[36:39], v[144:147], v[80:83], v[36:39]\n\t"
      "v_and_b32 v216, %[msk], v216\n\t"
      "s_waitcnt lgkmcnt(7)\n\t"
      "v_mfma_f32_16x16x32_bf16 v[40:43], v[140:143], v[84:87], v[40:43]\n\t"
      "ds_read_b128 v[80:83], v181 offset:0\n\t"
      "v_lshlrev_b32 v253, 16, v217\n\t"
      "v_mfma_f32_16x16x32_bf16 v[44:47], v[144:147], v[84:87], v[44:47]\n\t"
      "v_and_b32 v217, %[msk], v217\n\t"
      "s_waitcnt lgkmcnt(6)\n\t"
      "v_mfma_f32_16x16x32_bf16 v[0:3], v[140:143], v[92:95], v[0:3]\n\t"
      "ds_read_b128 v[84:87], v181 offset:2048\n\t"
      "v_lshlrev_b32 v186, 16, v218\n\t"
      "v_mfma_f32_16x16x32_bf16 v[4:7], v[144:147], v[92:95], v[4:7]\n\t"
      "v_and_b32 v218, %[msk], v218\n\t"
      "v_mfma_f32_16x16x32_bf16 v[64:67], v[88:91], v[92:95], v[64:67]\n\t"
      "v_lshlrev_b32 v187, 16, v219\n\t"
      "s_waitcnt lgkmcnt(6)\n\t"
      "v_mfma_f32_16x16x32_bf16 v[8:11], v[140:143], v[96:99], v[8:11]\n\t"
      "v_and_b32 v219, %[msk], v219\n\t"
      "v_mfma_f32_16x16x32_bf16 v[12:15], v[144:147], v[96:99], v[12:15]\n\t"
      "v_lshlrev_b32 v188, 16, v220\n\t"
      "v_mfma_f32_16x16x32_bf16 v[68:71], v[88:91], v[96:99], v[68:71]\n\t"
      "ds_read_b128 v[88:91], v184 offset:1024\n\t"
      "v_and_b32 v220, %[msk], v220\n\t"
      "s_waitcnt lgkmcnt(6)\n\t"
      "v_mfma_f32_16x16x32_bf16 v[48:51], v[140:143], v[100:103], v[48:51]\n\t"
      "ds_read_b128 v[92:95], v177 offset:0\n\t"
      "v_lshlrev_b32 v189, 16, v221\n\t"
      "v_mfma_f32_16x16x32_bf16 v[52:55], v[144:147], v[100:103], v[52:55]\n\t"
      "ds_read_b128 v[96:99], v177 offset:2048\n\t"
      "v_and_b32 v221, %[msk], v221\n\t"
      "s_waitcnt lgkmcnt(7)\n\t"
      "v_mfma_f32_16x16x32_bf16 v[56:59], v[140:143], v[104:107], v[56:59]\n\t"
      "ds_read_b128 v[100:103], v183 offset:0\n\t"
      "v_lshlrev_b32 v190, 16, v222\n\t"
      "v_mfma_f32_16x16x32_bf16 v[60:63], v[144:147], v[104:107], v[60:63]\n\t"
      "v_and_b32 v222, %[msk], v222\n\t"
      "s_waitcnt vmcnt(8)\n\t"
      "s_waitcnt lgkmcnt(7)\n\t"
      "v_mfma_f32_16x16x32_bf16 v[16:19], v[148:151], v[72:75], v[16:19]\n\t"
      "ds_read_b128 v[104:107], v183 offset:2048\n\t"
      "v_lshlrev_b32 v191, 16, v223\n\t"
      "v_mfma_f32_16x16x32_bf16 v[20:23], v[152:155], v[72:75], v[20:23]\n\t"
      "v_and_b32 v223, %[msk], v223\n\t"
      "s_waitcnt lgkmcnt(7)\n\t"
      "v_mfma_f32_16x16x32_bf16 v[24:27], v[148:151], v[76:79], v[24:27]\n\t"
      "ds_read_b128 v[72:75], v178 offset:18432\n\t"
      "v_mfma_f32_16x16x32_bf16 v[28:31], v[152:155], v[76:79], v[28:31]\n\t"
      "s_waitcnt lgkmcnt(7)\n\t"
      "v_mfma_f32_16x16x32_bf16 v[32:35], v[148:151], v[80:83], v[32:35]\n\t"
      "ds_read_b128 v[76:79], v178 offset:20480\n\t"
      "v_subrev_u32 v178, 110592, v178\n\t"
      "v_mfma_f32_16x16x32_bf16 v[36:39], v[152:155], v[80:83], v[36:39]\n\t"
      "buffer_load_dwordx4 v[124:127], v185, %[zr], %[so0] offen offset:0 sc1\n\t"
      "s_waitcnt lgkmcnt(7)\n\t"
      "v_mfma_f32_16x16x32_bf16 v[40:43], v[148:151], v[84:87], v[40:43]\n\t"
      "ds_read_b128 v[80:83], v180 offset:18432\n\t"
      "v_mfma_f32_16x16x32_bf16 v[44:47], v[152:155], v[84:87], v[44:47]\n\t"
      "s_waitcnt lgkmcnt(6)\n\t"
      "v_mfma_f32_16x16x32_bf16 v[0:3], v[148:151], v[92:95], v[0:3]\n\t"
      "ds_read_b128 v[84:87], v180 offset:20480\n\t"
      "v_subrev_u32 v180, 110592, v180\n\t"
      "v_mfma_f32_16x16x32_bf16 v[4:7], v[152:155], v[92:95], v[4:7]\n\t"
      "buffer_load_dwordx4 v[128:131], v185, %[zr], %[so0] offen offset:1024 sc1\n\ts_add_u32 %[so0], %[so0], 18432\n\t"
      "v_mfma_f32_16x16x32_bf16 v[64:67], v[88:91], v[92:95], v[64:67]\n\t"
      "s_waitcnt lgkmcnt(6)\n\t"
      "v_mfma_f32_16x16x32_bf16 v[8:11], v[148:151], v[96:99], v[8:11]\n\t"
      "v_mfma_f32_16x16x32_bf16 v[12:15], v[152:155], v[96:99], v[12:15]\n\t"
      "v_mfma_f32_16x16x32_bf16 v[68:71], v[88:91], v[96:99], v[68:71]\n\t"
      "ds_read_b128 v[88:91], v184 offset:18432\n\t"
      "buffer_load_dwordx4 v[132:135], v185, %[zr], %[so1] offen offset:0 sc1\n\t"
      "s_waitcnt lgkmcnt(6)\n\t"
      "v_mfma_f32_16x16x32_bf16 v[48:51], v[148:151], v[100:103], v[48:51]\n\t"
      "ds_read_b128 v[92:95], v176 offset:18432\n\t"
      "v_mfma_f32_16x16x32_bf16 v[52:55], v[152:155], v[100:103], v[52:55]\n\t"
      "ds_read_b128 v[96:99], v176 offset:20480\n\t"
      "v_subrev_u32 v176, 110592, v176\n\t"
      "s_waitcnt lgkmcnt(7)\n\t"
      "v_mfma_f32_16x16x32_bf16 v[56:59], v[148:151], v[104:107], v[56:59]\n\t"
      "ds_read_b128 v[100:103], v182 offset:18432\n\t"
      "v_mfma_f32_16x16x32_bf16 v[60:63], v[152:155], v[104:107], v[60:63]\n\t"
      "buffer_load_dwordx4 v[136:139], v185, %[zr], %[so1] offen offset:1024 sc1\n\ts_add_u32 %[so1], %[so1], 18432\n\t"
      "s_waitcnt vmcnt(10)\n\t"
      "s_waitcnt lgkmcnt(7)\n\t"
      "v_mfma_f32_16x16x32_bf16 v[16:19], v[156:159], v[72:75], v[16:19]\n\t"
      "ds_read_b128 v[104:107], v182 offset:20480\n\t"
      "v_subrev_u32 v182, 110592, v182\n\t"
      "v_mfma_f32_16x16x32_bf16 v[20:23], v[160:163], v[72:75], v[20:23]\n\t"
      "s_waitcnt lgkmcnt(7)\n\t"
      "v_mfma_f32_16x16x32_bf16 v[24:27], v[156:159], v[76:79], v[24:27]\n\t"
      "ds_read_b128 v[72:75], v179 offset:18432\n\t"
      "v_mfma_f32_16x16x32_bf16 v[28:31], v[160:163], v[76:79], v[28:31]\n\t"
      "buffer_load_dwordx4 v[140:143], v185, %[zr], %[so0] offen offset:0 sc1\n\t"
      "s_waitcnt lgkmcnt(7)\n\t"
      "v_mfma_f32_16x16x32_bf16 v[32:35], v[156:159], v[80:83], v[32:35]\n\t"
      "ds_read_b128 v[76:79], v179 offset:20480\n\t"
      "v_subrev_u32 v179, 110592, v179\n\t"
      "v_mfma_f32_16x16x32_bf16 v[36:39], v[160:163], v[80:83], v[36:39]\n\t"
      "s_waitcnt lgkmcnt(7)\n\t"
      "v_mfma_f32_16x16x32_bf16 v[40:43], v[156:159], v[84:87], v[40:43]\n\t"
      "ds_read_b128 v[80:83], v181 offset:18432\n\t"
      "v_mfma_f32_16x16x32_bf16 v[44:47], v[160:163], v[84:87], v[44:47]\n\t"
      "buffer_load_dwordx4 v[144:147], v185, %[zr], %[so0] offen offset:1024 sc1\n\ts_add_u32 %[so0], %[so0], 18432\n\t"
      "s_waitcnt lgkmcnt(6)\n\t"
      "v_mfma_f32_16x16x32_bf16 v[0:3], v[156:159], v[92:95], v[0:3]\n\t"
      "ds_read_b128 v[84:87], v181 offset:20480\n\t"
      "v_subrev_u32 v181, 110592, v181\n\t"
      "v_mfma_f32_16x16x32_bf16 v[4:7], v[160:163], v[92:95], v[4:7]\n\t"
      "v_mfma_f32_16x16x32_bf16 v[64:67], v[88:91], v[92:95], v[64:67]\n\t"
      "s_waitcnt lgkmcnt(6)\n\t"
      "v_mfma_f32_16x16x32_bf16 v[8:11], v[156:159], v[96:99], v[8:11]\n\t"
      "buffer_load_dwordx4 v[148:151], v185, %[zr], %[so1] offen offset:0 sc1\n\t"
      "v_mfma_f32_16x16x32_bf16 v[12:15], v[160:163], v[96:99], v[12:15]\n\t"
      "v_mfma_f32_16x16x32_bf16 v[68:71], v[88:91], v[96:99], v[68:71]\n\t"
      "ds_read_b128 v[88:91], v184 offset:19456\n\t"
      "v_subrev_u32 v184, 110592, v184\n\t"
      "s_waitcnt lgkmcnt(6)\n\t"
      "v_mfma_f32_16x16x32_bf16 v[48:51], v[156:159], v[100:103], v[48:51]\n\t"
      "ds_read_b128 v[92:95], v177 offset:18432\n\t"
      "v_mfma_f32_16x16x32_bf16 v[52:55], v[160:163], v[100:103], v[52:55]\n\t"
      "ds_read_b128 v[96:99], v177 offset:20480\n\t"
      "v_subrev_u32 v177, 110592, v177\n\t"
      "buffer_load_dwordx4 v[152:155], v185, %[zr], %[so1] offen offset:1024 sc1\n\ts_add_u32 %[so1], %[so1], 18432\n\t"
      "s_waitcnt lgkmcnt(7)\n\t"
      "v_mfma_f32_16x16x32_bf16 v[56:59], v[156:159], v[104:107], v[56:59]\n\t"
      "ds_read_b128 v[100:103], v183 offset:18432\n\t"
      "v_mfma_f32_16x16x32_bf16 v[60:63], v[160:163], v[104:107], v[60:63]\n\t"
      "s_waitcnt vmcnt(12)\n\t"
      "s_waitcnt lgkmcnt(7)\n\t"
      "v_mfma_f32_16x16x32_bf16 v[16:19], v[164:167], v[72:75], v[16:19]\n\t"
      "ds_read_b128 v[104:107], v183 offset:20480\n\t"
      "v_subrev_u32 v183, 110592, v183\n\t"
      "v_mfma_f32_16x16x32_bf16 v[20:23], v[168:171], v[72:75], v[20:23]\n\t"
      "s_waitcnt lgkmcnt(7)\n\t"
      "v_mfma_f32_16x16x32_bf16 v[24:27], v[164:167], v[76:79], v[24:27]\n\t"
      "s_barrier\n\t"
      "ds_read_b128 v[72:75], v178 offset:0\n\t"
      "v_mfma_f32_16x16x32_bf16 v[28:31], v[168:171], v[76:79], v[28:31]\n\t"
      "s_waitcnt lgkmcnt(7)\n\t"
      "v_mfma_f32_16x16x32_bf16 v[32:35], v[164:167], v[80:83], v[32:35]\n\t"
      "ds_read_b128 v[76:79], v178 offset:2048\n\t"
      "v_mfma_f32_16x16x32_bf16 v[36:39], v[168:171], v[80:83], v[36:39]\n\t"
      "s_waitcnt lgkmcnt(7)\n\t"
      "v_mfma_f32_16x16x32_bf16 v[40:43], v[164:167], v[84:87], v[40:43]\n\t"
      "ds_read_b128 v[80:83], v180 offset:0\n\t"
      "v_mfma_f32_16x16x32_bf16 v[44:47], v[168:171], v[84:87], v[44:47]\n\t"
      "s_waitcnt lgkmcnt(6)\n\t"
      "v_mfma_f32_16x16x32_bf16 v[0:3], v[164:167], v[92:95], v[0:3]\n\t"
      "ds_read_b128 v[84:87], v180 offset:2048\n\t"
      "v_mfma_f32_16x16x32_bf16 v[4:7], v[168:171], v[92:95], v[4:7]\n\t"
      "v_mfma_f32_16x16x32_bf16 v[64:67], v[88:91], v[92:95], v[64:67]\n\t"
      "s_waitcnt lgkmcnt(6)\n\t"
      "v_mfma_f32_16x16x32_bf16 v[8:11], v[164:167], v[96:99], v[8:11]\n\t"
      "v_mfma_f32_16x16x32_bf16 v[12:15], v[168:171], v[96:99], v[12:15]\n\t"
      "v_mfma_f32_16x16x32_bf16 v[68:71], v[88:91], v[96:99], v[68:71]\n\t"
      "ds_read_b128 v[88:91], v184 offset:0\n\t"
      "s_waitcnt lgkmcnt(6)\n\t"
      "v_mfma_f32_16x16x32_bf16 v[48:51], v[164:167], v[100:103], v[48:51]\n\t"
      "ds_read_b128 v[92:95], v176 offset:0\n\t"
      "v_mfma_f32_16x16x32_bf16 v[52:55], v[168:171], v[100:103], v[52:55]\n\t"
      "ds_read_b128 v[96:99], v176 offset:2048\n\t"
      "s_waitcnt lgkmcnt(7)\n\t"
      "v_mfma_f32_16x16x32_bf16 v[56:59], v[164:167], v[104:107], v[56:59]\n\t"
      "ds_read_b128 v[100:103], v182 offset:0\n\t"
      "v_mfma_f32_16x16x32_bf16 v[60:63], v[168:171], v[104:107], v[60:63]\n\t"
      "s_mov_b32 %[cnt], 6\n\t"
      "1:\n\t"
      "s_xor_b32 %[d0], %[d0], 4294885376\n\t"
      "s_cmp_eq_u32 %[j], %[cnt]\n\t"
      "s_cselect_b32 %[dz], %[dw], 18432\n\t"
      "s_waitcnt vmcnt(10)\n\t"
      "s_waitcnt lgkmcnt(7)\n\t"
      "v_mfma_f32_16x16x32_bf16 v[16:19], v[108:111], v[72:75], v[16:19]\n\t"
      "ds_read_b128 v[104:107], v182 offset:2048\n\t"
      "v_mfma_f32_16x16x32_bf16 v[20:23], v[112:115], v[72:75], v[20:23]\n\t"
      "s_waitcnt lgkmcnt(7)\n\t"
      "v_mfma_f32_16x16x32_bf16 v[24:27], v[108:111], v[76:79], v[24:27]\n\t"
      "ds_read_b128 v[72:75], v179 offset:0\n\t"
      "v_mfma_f32_16x16x32_bf16 v[28:31], v[112:115], v[76:79], v[28:31]\n\t"
      "s_waitcnt lgkmcnt(7)\n\t"
      "v_mfma_f32_16x16x32_bf16 v[32:35], v[108:111], v[80:83], v[32:35]\n\t"
      "ds_read_b128 v[76:79], v179 offset:2048\n\t"
      "v_mfma_f32_16x16x32_bf16 v[36:39], v[112:115], v[80:83], v[36:39]\n\t"
      "s_waitcnt lgkmcnt(7)\n\t"
      "v_mfma_f32_16x16x32_bf16 v[40:43], v[108:111], v[84:87], v[40:43]\n\t"
      "ds_read_b128 v[80:83], v181 offset:0\n\t"
      "v_mfma_f32_16x16x32_bf16 v[44:47], v[112:115], v[84:87], v[44:47]\n\t"
      "s_waitcnt lgkmcnt(6)\n\t"
      "v_mfma_f32_16x16x32_bf16 v[0:3], v[108:111], v[92:95], v[0:3]\n\t"
      "ds_read_b128 v[84:87], v181 offset:2048\n\t"
      "v_mfma_f32_16x16x32_bf16 v[4:7], v[112:115], v[92:95], v[4:7]\n\t"
      "v_mfma_f32_16x16x32_bf16 v[64:67], v[88:91], v[92:95], v[64:67]\n\t"
      "s_waitcnt lgkmcnt(6)\n\t"
      "v_mfma_f32_16x16x32_bf16 v[8:11], v[108:111], v[96:99], v[8:11]\n\t"
      "v_mfma_f32_16x16x32_bf16 v[12:15], v[112:115], v[96:99], v[12:15]\n\t"
      "v_mfma_f32_16x16x32_bf16 v[68:71], v[88:91], v[96:99], v[68:71]\n\t"
      "ds_read_b128 v[88:91], v184 offset:1024\n\t"
      "s_waitcnt lgkmcnt(6)\n\t"
      "v_mfma_f32_16x16x32_bf16 v[48:51], v[108:111], v[100:103], v[48:51]\n\t"
      "ds_read_b128 v[92:95], v177 offset:0\n\t"
      "v_mfma_f32_16x16x32_bf16 v[52:55], v[112:115], v[100:103], v[52:55]\n\t"
      "ds_read_b128 v[96:99], v177 offset:2048\n\t"
      "s_waitcnt lgkmcnt(7)\n\t"
      "v_mfma_f32_16x16x32_bf16 v[56:59], v[108:111], v[104:107], v[56:59]\n\t"
      "ds_read_b128 v[100:103], v183 offset:0\n\t"
      "v_mfma_f32_16x16x32_bf16 v[60:63], v[112:115], v[104:107], v[60:63]\n\t"
      "s_waitcnt vmcnt(8)\n\t"
      "s_waitcnt lgkmcnt(7)\n\t"
      "v_mfma_f32_16x16x32_bf16 v[16:19], v[116:119], v[72:75], v[16:19]\n\t"
      "ds_read_b128 v[104:107], v183 offset:2048\n\t"
      "v_mfma_f32_16x16x32_bf16 v[20:23], v[120:123], v[72:75], v[20:23]\n\t"
      "s_waitcnt lgkmcnt(7)\n\t"
      "v_mfma_f32_16x16x32_bf16 v[24:27], v[116:119], v[76:79], v[24:27]\n\t"
      "ds_read_b128 v[72:75], v178 offset:18432\n\t"
      "v_mfma_f32_16x16x32_bf16 v[28:31], v[120:123], v[76:79], v[28:31]\n\t"
      "s_waitcnt lgkmcnt(7)\n\t"
      "v_mfma_f32_16x16x32_bf16 v[32:35], v[116:119], v[80:83], v[32:35]\n\t"
      "ds_read_b128 v[76:79], v178 offset:20480\n\t"
      "v_add_u32 v178, 36864, v178\n\t"
      "v_mfma_f32_16x16x32_bf16 v[36:39], v[120:123], v[80:83], v[36:39]\n\t"
      "buffer_load_dwordx4 v[156:159], v185, %[zr], %[so0] offen offset:0 sc1\n\t"
      "s_waitcnt lgkmcnt(7)\n\t"
      "v_mfma_f32_16x16x32_bf16 v[40:43], v[116:119], v[84:87], v[40:43]\n\t"
      "ds_read_b128 v[80:83], v180 offset:18432\n\t"
      "v_mfma_f32_16x16x32_bf16 v[44:47], v[120:123], v[84:87], v[44:47]\n\t"
      "s_waitcnt lgkmcnt(6)\n\t"
      "v_mfma_f32_16x16x32_bf16 v[0:3], v[116:119], v[92:95], v[0:3]\n\t"
      "ds_read_b128 v[84:87], v180 offset:20480\n\t"
      "v_add_u32 v180, 36864, v180\n\t"
      "v_mfma_f32_16x16x32_bf16 v[4:7], v[120:123], v[92:95], v[4:7]\n\t"
      "buffer_load_dwordx4 v[160:163], v185, %[zr], %[so0] offen offset:1024 sc1\n\ts_add_u32 %[so0], %[so0], %[dz]\n\t"
      "v_mfma_f32_16x16x32_bf16 v[64:67], v[88:91], v[92:95], v[64:67]\n\t"
      "s_waitcnt lgkmcnt(6)\n\t"
      "v_mfma_f32_16x16x32_bf16 v[8:11], v[116:119], v[96:99], v[8:11]\n\t"
      "v_mfma_f32_16x16x32_bf16 v[12:15], v[120:123], v[96:99], v[12:15]\n\t"
      "v_mfma_f32_16x16x32_bf16 v[68:71], v[88:91], v[96:99], v[68:71]\n\t"
      "ds_read_b128 v[88:91], v184 offset:18432\n\t"
      "buffer_load_dwordx4 v[164:167], v185, %[zr], %[so1] offen offset:0 sc1\n\t"
      "s_waitcnt lgkmcnt(6)\n\t"
      "v_mfma_f32_16x16x32_bf16 v[48:51], v[116:119], v[100:103], v[48:51]\n\t"
      "ds_read_b128 v[92:95], v176 offset:18432\n\t"
      "v_mfma_f32_16x16x32_bf16 v[52:55], v[120:123], v[100:103], v[52:55]\n\t"
      "ds_read_b128 v[96:99], v176 offset:20480\n\t"
      "v_add_u32 v176, 36864, v176\n\t"
      "s_waitcnt lgkmcnt(7)\n\t"
      "v_mfma_f32_16x16x32_bf16 v[56:59], v[116:119], v[104:107], v[56:59]\n\t"
      "ds_read_b128 v[100:103], v182 offset:18432\n\t"
      "v_mfma_f32_16x16x32_bf16 v[60:63], v[120:123], v[104:107], v[60:63]\n\t"
      "buffer_load_dwordx4 v[168:171], v185, %[zr], %[so1] offen offset:1024 sc1\n\ts_add_u32 %[so1], %[so1], %[dz]\n\t"
      "s_waitcnt vmcnt(10)\n\t"
      "s_waitcnt lgkmcnt(7)\n\t"
      "v_mfma_f32_16x16x32_bf16 v[16:19], v[124:127], v[72:75], v[16:19]\n\t"
      "ds_read_b128 v[104:107], v182 offset:20480\n\t"
      "v_add_u32 v182, 36864, v182\n\t"
      "v_mfma_f32_16x16x32_bf16 v[20:23], v[128:131], v[72:75], v[20:23]\n\t"
      "s_waitcnt lgkmcnt(7)\n\t"
      "v_mfma_f32_16x16x32_bf16 v[24:27], v[124:127], v[76:79], v[24:27]\n\t"
      "ds_read_b128 v[72:75], v179 offset:18432\n\t"
      "v_mfma_f32_16x16x32_bf16 v[28:31], v[128:131], v[76:79], v[28:31]\n\t"
      "buffer_load_dwordx4 v[108:111], v185, %[zr], %[so0] offen offset:0 sc1\n\t"
      "s_waitcnt lgkmcnt(7)\n\t"
      "v_mfma_f32_16x16x32_bf16 v[32:35], v[124:127], v[80:83], v[32:35]\n\t"
      "ds_read_b128 v[76:79], v179 offset:20480\n\t"
      "v_add_u32 v179, 36864, v179\n\t"
      "v_mfma_f32_16x16x32_bf16 v[36:39], v[128:131], v[80:83], v[36:39]\n\t"
      "s_waitcnt lgkmcnt(7)\n\t"
      "v_mfma_f32_16x16x32_bf16 v[40:43], v[124:127], v[84:87], v[40:43]\n\t"
      "ds_read_b128 v[80:83], v181 offset:18432\n\t"
      "v_mfma_f32_16x16x32_bf16 v[44:47], v[128:131], v[84:87], v[44:47]\n\t"
      "buffer_load_dwordx4 v[112:115], v185, %[zr], %[so0] offen offset:1024 sc1\n\ts_add_u32 %[so0], %[so0], 18432\n\t"
      "s_waitcnt lgkmcnt(6)\n\t"
      "v_mfma_f32_16x16x32_bf16 v[0:3], v[124:127], v[92:95], v[0:3]\n\t"
      "ds_read_b128 v[84:87], v181 offset:20480\n\t"
      "v_add_u32 v181, 36864, v181\n\t"
      "v_mfma_f32_16x16x32_bf16 v[4:7], v[128:131], v[92:95], v[4:7]\n\t"
      "v_mfma_f32_16x16x32_bf16 v[64:67], v[88:91], v[92:95], v[64:67]\n\t"
      "s_waitcnt lgkmcnt(6)\n\t"
      "v_mfma_f32_16x16x32_bf16 v[8:11], v[124:127], v[96:99], v[8:11]\n\t"
      "buffer_load_dwordx4 v[116:119], v185, %[zr], %[so1] offen offset:0 sc1\n\t"
      "v_mfma_f32_16x16x32_bf16 v[12:15], v[128:131], v[96:99], v[12:15]\n\t"
      "v_mfma_f32_16x16x32_bf16 v[68:71], v[88:91], v[96:99], v[68:71]\n\t"
      "ds_read_b128 v[88:91], v184 offset:19456\n\t"
      "v_add_u32 v184, 36864, v184\n\t"
      "s_waitcnt lgkmcnt(6)\n\t"
      "v_mfma_f32_16x16x32_bf16 v[48:51], v[124:127], v[100:103], v[48:51]\n\t"
      "ds_read_b128 v[92:95], v177 offset:18432\n\t"
      "v_mfma_f32_16x16x32_bf16 v[52:55], v[128:131], v[100:103], v[52:55]\n\t"
      "ds_read_b128 v[96:99], v177 offset:20480\n\t"
      "v_add_u32 v177, 36864, v177\n\t"
      "buffer_load_dwordx4 v[120:123], v185, %[zr], %[so1] offen offset:1024 sc1\n\ts_add_u32 %[so1], %[so1], 18432\n\t"
      "s_waitcnt lgkmcnt(7)\n\t"
      "v_mfma_f32_16x16x32_bf16 v[56:59], v[124:127], v[104:107], v[56:59]\n\t"
      "ds_read_b128 v[100:103], v183 offset:18432\n\t"
      "v_mfma_f32_16x16x32_bf16 v[60:63], v[128:131], v[104:107], v[60:63]\n\t"
      "s_waitcnt vmcnt(12)\n\t"
      "s_waitcnt lgkmcnt(7)\n\t"
      "v_mfma_f32_16x16x32_bf16 v[16:19], v[132:135], v[72:75], v[16:19]\n\t"
      "ds_read_b128 v[104:107], v183 offset:20480\n\t"
      "v_add_u32 v183, 36864, v183\n\t"
      "v_mfma_f32_16x16x32_bf16 v[20:23], v[136:139], v[72:75], v[20:23]\n\t"
      "s_waitcnt lgkmcnt(7)\n\t"
      "v_mfma_f32_16x16x32_bf16 v[24:27], v[132:135], v[76:79], v[24:27]\n\t"
      "s_barrier\n\t"
      "ds_read_b128 v[72:75], v178 offset:0\n\t"
      "v_mfma_f32_16x16x32_bf16 v[28:31], v[136:139], v[76:79], v[28:31]\n\t"
      "s_waitcnt lgkmcnt(7)\n\t"
      "v_mfma_f32_16x16x32_bf16 v[32:35], v[132:135], v[80:83], v[32:35]\n\t"
      "ds_read_b128 v[76:79], v178 offset:2048\n\t"
      "v_mfma_f32_16x16x32_bf16 v[36:39], v[136:139], v[80:83], v[36:39]\n\t"
      "s_waitcnt lgkmcnt(7)\n\t"
      "v_mfma_f32_16x16x32_bf16 v[40:43], v[132:135], v[84:87], v[40:43]\n\t"
      "ds_read_b128 v[80:83], v180 offset:0\n\t"
      "v_mfma_f32_16x16x32_bf16 v[44:47], v[136:139], v[84:87], v[44:47]\n\t"
      "s_waitcnt lgkmcnt(6)\n\t"
      "v_mfma_f32_16x16x32_bf16 v[0:3], v[132:135], v[92:95], v[0:3]\n\t"
      "ds_read_b128 v[84:87], v180 offset:2048\n\t"
      "v_mfma_f32_16x16x32_bf16 v[4:7], v[136:139], v[92:95], v[4:7]\n\t"
      "v_mfma_f32_16x16x32_bf16 v[64:67], v[88:91], v[92:95], v[64:67]\n\t"
      "s_waitcnt lgkmcnt(6)\n\t"
      "v_mfma_f32_16x16x32_bf16 v[8:11], v[132:135], v[96:99], v[8:11]\n\t"
      "v_mfma_f32_16x16x32_bf16 v[12:15], v[136:139], v[96:99], v[12:15]\n\t"
      "v_mfma_f32_16x16x32_bf16 v[68:71], v[88:91], v[96:99], v[68:71]\n\t"
      "ds_read_b128 v[88:91], v184 offset:0\n\t"
      "s_waitcnt lgkmcnt(6)\n\t"
      "v_mfma_f32_16x16x32_bf16 v[48:51], v[132:135], v[100:103], v[48:51]\n\t"
      "ds_read_b128 v[92:95], v176 offset:0\n\t"
      "v_mfma_f32_16x16x32_bf16 v[52:55], v[136:139], v[100:103], v[52:55]\n\t"
      "ds_read_b128 v[96:99], v176 offset:2048\n\t"
      "s_waitcnt lgkmcnt(7)\n\t"
      "v_mfma_f32_16x16x32_bf16 v[56:59], v[132:135], v[104:107], v[56:59]\n\t"
      "ds_read_b128 v[100:103], v182 offset:0\n\t"
      "v_mfma_f32_16x16x32_bf16 v[60:63], v[136:139], v[104:107], v[60:63]\n\t"
      "s_waitcnt vmcnt(10)\n\t"
      "s_waitcnt lgkmcnt(7)\n\t"
      "v_mfma_f32_16x16x32_bf16 v[16:19], v[140:143], v[72:75], v[16:19]\n\t"
      "ds_read_b128 v[104:107], v182 offset:2048\n\t"
      "v_mfma_f32_16x16x32_bf16 v[20:23], v[144:147], v[72:75], v[20:23]\n\t"
      "s_waitcnt lgkmcnt(7)\n\t"
      "v_mfma_f32_16x16x32_bf16 v[24:27], v[140:143], v[76:79], v[24:27]\n\t"
      "ds_read_b128 v[72:75], v179 offset:0\n\t"
      "v_mfma_f32_16x16x32_bf16 v[28:31], v[144:147], v[76:79], v[28:31]\n\t"
      "s_waitcnt lgkmcnt(7)\n\t"
      "v_mfma_f32_16x16x32_bf16 v[32:35], v[140:143], v[80:83], v[32:35]\n\t"
      "ds_read_b128 v[76:79], v179 offset:2048\n\t"
      "v_mfma_f32_16x16x32_bf16 v[36:39], v[144:147], v[80:83], v[36:39]\n\t"
      "s_waitcnt lgkmcnt(7)\n\t"
      "v_mfma_f32_16x16x32_bf16 v[40:43], v[140:143], v[84:87], v[40:43]\n\t"
      "ds_read_b128 v[80:83], v181 offset:0\n\t"
      "v_mfma_f32_16x16x32_bf16 v[44:47], v[144:147], v[84:87], v[44:47]\n\t"
      "s_waitcnt lgkmcnt(6)\n\t"
      "v_mfma_f32_16x16x32_bf16 v[0:3], v[140:143], v[92:95], v[0:3]\n\t"
      "ds_read_b128 v[84:87], v181 offset:2048\n\t"
      "v_mfma_f32_16x16x32_bf16 v[4:7], v[144:147], v[92:95], v[4:7]\n\t"
      "v_mfma_f32_16x16x32_bf16 v[64:67], v[88:91], v[92:95], v[64:67]\n\t"
      "s_waitcnt lgkmcnt(6)\n\t"
      "v_mfma_f32_16x16x32_bf16 v[8:11], v[140:143], v[96:99], v[8:11]\n\t"
      "v_mfma_f32_16x16x32_bf16 v[12:15], v[144:147], v[96:99], v[12:15]\n\t"
      "v_mfma_f32_16x16x32_bf16 v[68:71], v[88:91], v[96:99], v[68:71]\n\t"
      "ds_read_b128 v[88:91], v184 offset:1024\n\t"
      "s_waitcnt lgkmcnt(6)\n\t"
      "v_mfma_f32_16x16x32_bf16 v[48:51], v[140:143], v[100:103], v[48:51]\n\t"
      "ds_read_b128 v[92:95], v177 offset:0\n\t"
      "v_mfma_f32_16x16x32_bf16 v[52:55], v[144:147], v[100:103], v[52:55]\n\t"
      "ds_read_b128 v[96:99], v177 offset:2048\n\t"
      "s_waitcnt lgkmcnt(7)\n\t"
      "v_mfma_f32_16x16x32_bf16 v[56:59], v[140:143], v[104:107], v[56:59]\n\t"
      "ds_read_b128 v[100:103], v183 offset:0\n\t"
      "v_mfma_f32_16x16x32_bf16 v[60:63], v[144:147], v[104:107], v[60:63]\n\t"
      "s_waitcnt vmcnt(8)\n\t"
      "s_waitcnt lgkmcnt(7)\n\t"
      "v_mfma_f32_16x16x32_bf16 v[16:19], v[148:151], v[72:75], v[16:19]\n\t"
      "ds_read_b128 v[104:107], v183 offset:2048\n\t"
      "v_mfma_f32_16x16x32_bf16 v[20:23], v[152:155], v[72:75], v[20:23]\n\t"
      "s_waitcnt lgkmcnt(7)\n\t"
      "v_mfma_f32_16x16x32_bf16 v[24:27], v[148:151], v[76:79], v[24:27]\n\t"
      "ds_read_b128 v[72:75], v178 offset:18432\n\t"
      "v_mfma_f32_16x16x32_bf16 v[28:31], v[152:155], v[76:79], v[28:31]\n\t"
      "s_waitcnt lgkmcnt(7)\n\t"
      "v_mfma_f32_16x16x32_bf16 v[32:35], v[148:151], v[80:83], v[32:35]\n\t"
      "ds_read_b128 v[76:79], v178 offset:20480\n\t"
      "v_add_u32 v178, %[d0], v178\n\t"
      "v_mfma_f32_16x16x32_bf16 v[36:39], v[152:155], v[80:83], v[36:39]\n\t"
      "buffer_load_dwordx4 v[124:127], v185, %[zr], %[so0] offen offset:0 sc1\n\t"
      "s_waitcnt lgkmcnt(7)\n\t"
      "v_mfma_f32_16x16x32_bf16 v[40:43], v[148:151], v[84:87], v[40:43]\n\t"
      "ds_read_b128 v[80:83], v180 offset:18432\n\t"
      "v_mfma_f32_16x16x32_bf16 v[44:47], v[152:155], v[84:87], v[44:47]\n\t"
      "s_waitcnt lgkmcnt(6)\n\t"
      "v_mfma_f32_16x16x32_bf16 v[0:3], v[148:151], v[92:95], v[0:3]\n\t"
      "ds_read_b128 v[84:87], v180 offset:20480\n\t"
      "v_add_u32 v180, %[d0], v180\n\t"
      "v_mfma_f32_16x16x32_bf16 v[4:7], v[152:155], v[92:95], v[4:7]\n\t"
      "buffer_load_dwordx4 v[128:131], v185, %[zr], %[so0] offen offset:1024 sc1\n\ts_add_u32 %[so0], %[so0], 18432\n\t"
      "v_mfma_f32_16x16x32_bf16 v[64:67], v[88:91], v[92:95], v[64:67]\n\t"
      "s_waitcnt lgkmcnt(6)\n\t"
      "v_mfma_f32_16x16x32_bf16 v[8:11], v[148:151], v[96:99], v[8:11]\n\t"
      "v_mfma_f32_16x16x32_bf16 v[12:15], v[152:155], v[96:99], v[12:15]\n\t"
      "v_mfma_f32_16x16x32_bf16 v[68:71], v[88:91], v[96:99], v[68:71]\n\t"
      "ds_read_b128 v[88:91], v184 offset:18432\n\t"
      "buffer_load_dwordx4 v[132:135], v185, %[zr], %[so1] offen offset:0 sc1\n\t"
      "s_waitcnt lgkmcnt(6)\n\t"
      "v_mfma_f32_16x16x32_bf16 v[48:51], v[148:151], v[100:103], v[48:51]\n\t"
      "ds_read_b128 v[92:95], v176 offset:18432\n\t"
      "v_mfma_f32_16x16x32_bf16 v[52:55], v[152:155], v[100:103], v[52:55]\n\t"
      "ds_read_b128 v[96:99], v176 offset:20480\n\t"
      "v_add_u32 v176, %[d0], v176\n\t"
      "s_waitcnt lgkmcnt(7)\n\t"
      "v_mfma_f32_16x16x32_bf16 v[56:59], v[148:151], v[104:107], v[56:59]\n\t"
      "ds_read_b128 v[100:103], v182 offset:18432\n\t"
      "v_mfma_f32_16x16x32_bf16 v[60:63], v[152:155], v[104:107], v[60:63]\n\t"
      "buffer_load_dwordx4 v[136:139], v185, %[zr], %[so1] offen offset:1024 sc1\n\ts_add_u32 %[so1], %[so1], 18432\n\t"
      "s_waitcnt vmcnt(10)\n\t"
      "s_waitcnt lgkmcnt(7)\n\t"
      "v_mfma_f32_16x16x32_bf16 v[16:19], v[156:159], v[72:75], v[16:19]\n\t"
      "ds_read_b128 v[104:107], v182 offset:20480\n\t"
      "v_add_u32 v182, %[d0], v182\n\t"
      "v_mfma_f32_16x16x32_bf16 v[20:23], v[160:163], v[72:75], v[20:23]\n\t"
      "s_waitcnt lgkmcnt(7)\n\t"
      "v_mfma_f32_16x16x32_bf16 v[24:27], v[156:159], v[76:79], v[24:27]\n\t"
      "ds_read_b128 v[72:75], v179 offset:18432\n\t"
      "v_mfma_f32_16x16x32_bf16 v[28:31], v[160:163], v[76:79], v[28:31]\n\t"
      "buffer_load_dwordx4 v[140:143], v185, %[zr], %[so0] offen offset:0 sc1\n\t"
      "s_waitcnt lgkmcnt(7)\n\t"
      "v_mfma_f32_16x16x32_bf16 v[32:35], v[156:159], v[80:83], v[32:35]\n\t"
      "ds_read_b128 v[76:79], v179 offset:20480\n\t"
      "v_add_u32 v179, %[d0], v179\n\t"
      "v_mfma_f32_16x16x32_bf16 v[36:39], v[160:163], v[80:83], v[36:39]\n\t"
      "s_waitcnt lgkmcnt(7)\n\t"
      "v_mfma_f32_16x16x32_bf16 v[40:43], v[156:159], v[84:87], v[40:43]\n\t"
      "ds_read_b128 v[80:83], v181 offset:18432\n\t"
      "v_mfma_f32_16x16x32_bf16 v[44:47], v[160:163], v[84:87], v[44:47]\n\t"
      "buffer_load_dwordx4 v[144:147], v185, %[zr], %[so0] offen offset:1024 sc1\n\ts_add_u32 %[so0], %[so0], 18432\n\t"
      "s_waitcnt lgkmcnt(6)\n\t"
      "v_mfma_f32_16x16x32_bf16 v[0:3], v[156:159], v[92:95], v[0:3]\n\t"
      "ds_read_b128 v[84:87], v181 offset:20480\n\t"
      "v_add_u32 v181, %[d0], v181\n\t"
      "v_mfma_f32_16x16x32_bf16 v[4:7], v[160:163], v[92:95], v[4:7]\n\t"
      "v_mfma_f32_16x16x32_bf16 v[64:67], v[88:91], v[92:95], v[64:67]\n\t"
      "s_waitcnt lgkmcnt(6)\n\t"
      "v_mfma_f32_16x16x32_bf16 v[8:11], v[156:159], v[96:99], v[8:11]\n\t"
      "buffer_load_dwordx4 v[148:151], v185, %[zr], %[so1] offen offset:0 sc1\n\t"
      "v_mfma_f32_16x16x32_bf16 v[12:15], v[160:163], v[96:99], v[12:15]\n\t"
      "v_mfma_f32_16x16x32_bf16 v[68:71], v[88:91], v[96:99], v[68:71]\n\t"
      "ds_read_b128 v[88:91], v184 offset:19456\n\t"
      "v_add_u32 v184, %[d0], v184\n\t"
      "s_waitcnt lgkmcnt(6)\n\t"
      "v_mfma_f32_16x16x32_bf16 v[48:51], v[156:159], v[100:103], v[48:51]\n\t"
      "ds_read_b128 v[92:95], v177 offset:18432\n\t"
      "v_mfma_f32_16x16x32_bf16 v[52:55], v[160:163], v[100:103], v[52:55]\n\t"
      "ds_read_b128 v[96:99], v177 offset:20480\n\t"
      "v_add_u32 v177, %[d0], v177\n\t"
      "buffer_load_dwordx4 v[152:155], v185, %[zr], %[so1] offen offset:1024 sc1\n\ts_add_u32 %[so1], %[so1], 18432\n\t"
      "s_waitcnt lgkmcnt(7)\n\t"
      "v_mfma_f32_16x16x32_bf16 v[56:59], v[156:159], v[104:107], v[56:59]\n\t"
      "ds_read_b128 v[100:103], v183 offset:18432\n\t"
      "v_mfma_f32_16x16x32_bf16 v[60:63], v[160:163], v[104:107], v[60:63]\n\t"
      "s_waitcnt vmcnt(12)\n\t"
      "s_waitcnt lgkmcnt(7)\n\t"
      "v_mfma_f32_16x16x32_bf16 v[16:19], v[164:167], v[72:75], v[16:19]\n\t"
      "ds_read_b128 v[104:107], v183 offset:20480\n\t"
      "v_add_u32 v183, %[d0], v183\n\t"
      "v_mfma_f32_16x16x32_bf16 v[20:23], v[168:171], v[72:75], v[20:23]\n\t"
      "s_waitcnt lgkmcnt(7)\n\t"
      "v_mfma_f32_16x16x32_bf16 v[24:27], v[164:167], v[76:79], v[24:27]\n\t"
      "s_barrier\n\t"
      "ds_read_b128 v[72:75], v178 offset:0\n\t"
      "v_mfma_f32_16x16x32_bf16 v[28:31], v[168:171], v[76:79], v[28:31]\n\t"
      "s_waitcnt lgkmcnt(7)\n\t"
      "v_mfma_f32_16x16x32_bf16 v[32:35], v[164:167], v[80:83], v[32:35]\n\t"
      "ds_read_b128 v[76:79], v178 offset:2048\n\t"
      "v_mfma_f32_16x16x32_bf16 v[36:39], v[168:171], v[80:83], v[36:39]\n\t"
      "s_waitcnt lgkmcnt(7)\n\t"
      "v_mfma_f32_16x16x32_bf16 v[40:43], v[164:167], v[84:87], v[40:43]\n\t"
      "ds_read_b128 v[80:83], v180 offset:0\n\t"
      "v_mfma_f32_16x16x32_bf16 v[44:47], v[168:171], v[84:87], v[44:47]\n\t"
      "s_waitcnt lgkmcnt(6)\n\t"
      "v_mfma_f32_16x16x32_bf16 v[0:3], v[164:167], v[92:95], v[0:3]\n\t"
      "ds_read_b128 v[84:87], v180 offset:2048\n\t"
      "v_mfma_f32_16x16x32_bf16 v[4:7], v[168:171], v[92:95], v[4:7]\n\t"
      "v_mfma_f32_16x16x32_bf16 v[64:67], v[88:91], v[92:95], v[64:67]\n\t"
      "s_waitcnt lgkmcnt(6)\n\t"
      "v_mfma_f32_16x16x32_bf16 v[8:11], v[164:167], v[96:99], v[8:11]\n\t"
      "v_mfma_f32_16x16x32_bf16 v[12:15], v[168:171], v[96:99], v[12:15]\n\t"
      "v_mfma_f32_16x16x32_bf16 v[68:71], v[88:91], v[96:99], v[68:71]\n\t"
      "ds_read_b128 v[88:91], v184 offset:0\n\t"
      "s_waitcnt lgkmcnt(6)\n\t"
      "v_mfma_f32_16x16x32_bf16 v[48:51], v[164:167], v[100:103], v[48:51]\n\t"
      "ds_read_b128 v[92:95], v176 offset:0\n\t"
      "v_mfma_f32_16x16x32_bf16 v[52:55], v[168:171], v[100:103], v[52:55]\n\t"
      "ds_read_b128 v[96:99], v176 offset:2048\n\t"
      "s_waitcnt lgkmcnt(7)\n\t"
      "v_mfma_f32_16x16x32_bf16 v[56:59], v[164:167], v[104:107], v[56:59]\n\t"
      "ds_read_b128 v[100:103], v182 offset:0\n\t"
      "v_mfma_f32_16x16x32_bf16 v[60:63], v[168:171], v[104:107], v[60:63]\n\t"
      "s_sub_u32 %[cnt], %[cnt], 1\n\t"
      "s_cmp_lg_u32 %[cnt], 0\n\t"
      "s_cbranch_scc1 1b\n\t"
      "v_mov_b32 v173, 32\n\t"
      "ds_write_b32 v172, v173\n\t"
      "s_waitcnt vmcnt(10)\n\t"
      "s_waitcnt lgkmcnt(8)\n\t"
      "v_mfma_f32_16x16x32_bf16 v[16:19], v[108:111], v[72:75], v[16:19]\n\t"
      "ds_read_b128 v[104:107], v182 offset:2048\n\t"
      "v_mfma_f32_16x16x32_bf16 v[20:23], v[112:115], v[72:75], v[20:23]\n\t"
      "s_waitcnt lgkmcnt(8)\n\t"
      "v_mfma_f32_16x16x32_bf16 v[24:27], v[108:111], v[76:79], v[24:27]\n\t"
      "ds_read_b128 v[72:75], v179 offset:0\n\t"
      "v_mfma_f32_16x16x32_bf16 v[28:31], v[112:115], v[76:79], v[28:31]\n\t"
      "buffer_load_dwordx4 v[156:159], v185, %[zr], %[so0] offen offset:0 sc1\n\t"
      "s_waitcnt lgkmcnt(8)\n\t"
      "v_mfma_f32_16x16x32_bf16 v[32:35], v[108:111], v[80:83], v[32:35]\n\t"
      "ds_read_b128 v[76:79], v179 offset:2048\n\t"
      "v_mfma_f32_16x16x32_bf16 v[36:39], v[112:115], v[80:83], v[36:39]\n\t"
      "s_waitcnt lgkmcnt(8)\n\t"
      "v_mfma_f32_16x16x32_bf16 v[40:43], v[108:111], v[84:87], v[40:43]\n\t"
      "ds_read_b128 v[80:83], v181 offset:0\n\t"
      "v_mfma_f32_16x16x32_bf16 v[44:47], v[112:115], v[84:87], v[44:47]\n\t"
      "buffer_load_dwordx4 v[160:163], v185, %[zr], %[so0] offen offset:1024 sc1\n\ts_add_u32 %[so0], %[so0], %[dz]\n\t"
      "s_waitcnt lgkmcnt(7)\n\t"
      "v_mfma_f32_16x16x32_bf16 v[0:3], v[108:111], v[92:95], v[0:3]\n\t"
      "ds_read_b128 v[84:87], v181 offset:2048\n\t"
      "v_mfma_f32_16x16x32_bf16 v[4:7], v[112:115], v[92:95], v[4:7]\n\t"
      "v_mfma_f32_16x16x32_bf16 v[64:67], v[88:91], v[92:95], v[64:67]\n\t"
      "s_waitcnt lgkmcnt(7)\n\t"
      "v_mfma_f32_16x16x32_bf16 v[8:11], v[108:111], v[96:99], v[8:11]\n\t"
      "buffer_load_dwordx4 v[164:167], v185, %[zr], %[so1] offen offset:0 sc1\n\t"
      "v_mfma_f32_16x16x32_bf16 v[12:15], v[112:115], v[96:99], v[12:15]\n\t"
      "v_mfma_f32_16x16x32_bf16 v[68:71], v[88:91], v[96:99], v[68:71]\n\t"
      "ds_read_b128 v[88:91], v184 offset:1024\n\t"
      "s_waitcnt lgkmcnt(7)\n\t"
      "v_mfma_f32_16x16x32_bf16 v[48:51], v[108:111], v[100:103], v[48:51]\n\t"
      "ds_read_b128 v[92:95], v177 offset:0\n\t"
      "v_mfma_f32_16x16x32_bf16 v[52:55], v[112:115], v[100:103], v[52:55]\n\t"
      "ds_read_b128 v[96:99], v177 offset:2048\n\t"
      "buffer_load_dwordx4 v[168:171], v185, %[zr], %[so1] offen offset:1024 sc1\n\ts_add_u32 %[so1], %[so1], %[dz]\n\t"
      "s_waitcnt lgkmcnt(7)\n\t"
      "v_mfma_f32_16x16x32_bf16 v[56:59], v[108:111], v[104:107], v[56:59]\n\t"
      "ds_read_b128 v[100:103], v183 offset:0\n\t"
      "v_mfma_f32_16x16x32_bf16 v[60:63], v[112:115], v[104:107], v[60:63]\n\t"
      "s_waitcnt vmcnt(12)\n\t"
      "s_waitcnt lgkmcnt(7)\n\t"
      "v_mfma_f32_16x16x32_bf16 v[16:19], v[116:119], v[72:75], v[16:19]\n\t"
      "ds_read_b128 v[104:107], v183 offset:2048\n\t"
      "v_mfma_f32_16x16x32_bf16 v[20:23], v[120:123], v[72:75], v[20:23]\n\t"
      "s_waitcnt lgkmcnt(7)\n\t"
      "v_mfma_f32_16x16x32_bf16 v[24:27], v[116:119], v[76:79], v[24:27]\n\t"
      "ds_read_b128 v[72:75], v178 offset:18432\n\t"
      "v_mfma_f32_16x16x32_bf16 v[28:31], v[120:123], v[76:79], v[28:31]\n\t"
      "s_waitcnt lgkmcnt(7)\n\t"
      "v_mfma_f32_16x16x32_bf16 v[32:35], v[116:119], v[80:83], v[32:35]\n\t"
      "ds_read_b128 v[76:79], v178 offset:20480\n\t"
      "v_add_u32 v178, 36864, v178\n\t"
      "v_mfma_f32_16x16x32_bf16 v[36:39], v[120:123], v[80:83], v[36:39]\n\t"
      "s_waitcnt lgkmcnt(7)\n\t"
      "v_mfma_f32_16x16x32_bf16 v[40:43], v[116:119], v[84:87], v[40:43]\n\t"
      "ds_read_b128 v[80:83], v180 offset:18432\n\t"
      "v_mfma_f32_16x16x32_bf16 v[44:47], v[120:123], v[84:87], v[44:47]\n\t"
      "s_waitcnt lgkmcnt(6)\n\t"
      "v_mfma_f32_16x16x32_bf16 v[0:3], v[116:119], v[92:95], v[0:3]\n\t"
      "ds_read_b128 v[84:87], v180 offset:20480\n\t"
      "v_add_u32 v180, 36864, v180\n\t"
      "v_mfma_f32_16x16x32_bf16 v[4:7], v[120:123], v[92:95], v[4:7]\n\t"
      "v_mfma_f32_16x16x32_bf16 v[64:67], v[88:91], v[92:95], v[64:67]\n\t"
      "s_waitcnt lgkmcnt(6)\n\t"
      "v_mfma_f32_16x16x32_bf16 v[8:11], v[116:119], v[96:99], v[8:11]\n\t"
      "v_mfma_f32_16x16x32_bf16 v[12:15], v[120:123], v[96:99], v[12:15]\n\t"
      "v_mfma_f32_16x16x32_bf16 v[68:71], v[88:91], v[96:99], v[68:71]\n\t"
      "ds_read_b128 v[88:91], v184 offset:18432\n\t"
      "s_waitcnt lgkmcnt(6)\n\t"
      "v_mfma_f32_16x16x32_bf16 v[48:51], v[116:119], v[100:103], v[48:51]\n\t"
      "ds_read_b128 v[92:95], v176 offset:18432\n\t"
      "v_mfma_f32_16x16x32_bf16 v[52:55], v[120:123], v[100:103], v[52:55]\n\t"
      "ds_read_b128 v[96:99], v176 offset:20480\n\t"
      "v_add_u32 v176, 36864, v176\n\t"
      "s_waitcnt lgkmcnt(7)\n\t"
      "v_mfma_f32_16x16x32_bf16 v[56:59], v[116:119], v[104:107], v[56:59]\n\t"
      "ds_read_b128 v[100:103], v182 offset:18432\n\t"
      "v_mfma_f32_16x16x32_bf16 v[60:63], v[120:123], v[104:107], v[60:63]\n\t"
      "s_waitcnt vmcnt(10)\n\t"
      "s_waitcnt lgkmcnt(7)\n\t"
      "v_mfma_f32_16x16x32_bf16 v[16:19], v[124:127], v[72:75], v[16:19]\n\t"
      "ds_read_b32 v174, v172 offset:64\n\t"
      "ds_read_b128 v[104:107], v182 offset:20480\n\t"
      "v_add_u32 v182, 36864, v182\n\t"
      "v_mfma_f32_16x16x32_bf16 v[20:23], v[128:131], v[72:75], v[20:23]\n\t"
      "s_waitcnt lgkmcnt(8)\n\t"
      "v_mfma_f32_16x16x32_bf16 v[24:27], v[124:127], v[76:79], v[24:27]\n\t"
      "ds_read_b128 v[72:75], v179 offset:18432\n\t"
      "v_mfma_f32_16x16x32_bf16 v[28:31], v[128:131], v[76:79], v[28:31]\n\t"
      "s_waitcnt lgkmcnt(8)\n\t"
      "v_mfma_f32_16x16x32_bf16 v[32:35], v[124:127], v[80:83], v[32:35]\n\t"
      "ds_read_b128 v[76:79], v179 offset:20480\n\t"
      "v_add_u32 v179, 36864, v179\n\t"
      "v_mfma_f32_16x16x32_bf16 v[36:39], v[128:131], v[80:83], v[36:39]\n\t"
      "s_waitcnt lgkmcnt(8)\n\t"
      "v_mfma_f32_16x16x32_bf16 v[40:43], v[124:127], v[84:87], v[40:43]\n\t"
      "ds_read_b128 v[80:83], v181 offset:18432\n\t"
      "v_mfma_f32_16x16x32_bf16 v[44:47], v[128:131], v[84:87], v[44:47]\n\t"
      "s_waitcnt lgkmcnt(7)\n\t"
      "v_mfma_f32_16x16x32_bf16 v[0:3], v[124:127], v[92:95], v[0:3]\n\t"
      "ds_read_b128 v[84:87], v181 offset:20480\n\t"
      "v_add_u32 v181, 36864, v181\n\t"
      "v_mfma_f32_16x16x32_bf16 v[4:7], v[128:131], v[92:95], v[4:7]\n\t"
      "v_mfma_f32_16x16x32_bf16 v[64:67], v[88:91], v[92:95], v[64:67]\n\t"
      "s_waitcnt lgkmcnt(7)\n\t"
      "v_mfma_f32_16x16x32_bf16 v[8:11], v[124:127], v[96:99], v[8:11]\n\t"
      "v_mfma_f32_16x16x32_bf16 v[12:15], v[128:131], v[96:99], v[12:15]\n\t"
      "v_mfma_f32_16x16x32_bf16 v[68:71], v[88:91], v[96:99], v[68:71]\n\t"
      "ds_read_b128 v[88:91], v184 offset:19456\n\t"
      "v_add_u32 v184, 36864, v184\n\t"
      "s_waitcnt lgkmcnt(7)\n\t"
      "v_mfma_f32_16x16x32_bf16 v[48:51], v[124:127], v[100:103], v[48:51]\n\t"
      "ds_read_b128 v[92:95], v177 offset:18432\n\t"
      "v_mfma_f32_16x16x32_bf16 v[52:55], v[128:131], v[100:103], v[52:55]\n\t"
      "ds_read_b128 v[96:99], v177 offset:20480\n\t"
      "v_add_u32 v177, 36864, v177\n\t"
      "s_waitcnt lgkmcnt(7)\n\t"
      "v_mfma_f32_16x16x32_bf16 v[56:59], v[124:127], v[104:107], v[56:59]\n\t"
      "v_cmp_gt_u32 vcc, 34, v174\n\t"
      "ds_read_b128 v[100:103], v183 offset:18432\n\t"
      "v_mfma_f32_16x16x32_bf16 v[60:63], v[128:131], v[104:107], v[60:63]\n\t"
      "s_waitcnt vmcnt(8)\n\t"
      "s_waitcnt lgkmcnt(7)\n\t"
      "v_mfma_f32_16x16x32_bf16 v[16:19], v[132:135], v[72:75], v[16:19]\n\t"
      "ds_read_b128 v[104:107], v183 offset:20480\n\t"
      "v_add_u32 v183, 36864, v183\n\t"
      "v_mfma_f32_16x16x32_bf16 v[20:23], v[136:139], v[72:75], v[20:23]\n\t"
      "s_waitcnt lgkmcnt(7)\n\t"
      "v_mfma_f32_16x16x32_bf16 v[24:27], v[132:135], v[76:79], v[24:27]\n\t"
      "s_cbranch_vccnz 11f\n\t"
      "12:\n\t"
      "ds_read_b128 v[72:75], v178 offset:0\n\t"
      "v_mfma_f32_16x16x32_bf16 v[28:31], v[136:139], v[76:79], v[28:31]\n\t"
      "s_waitcnt lgkmcnt(7)\n\t"
      "v_mfma_f32_16x16x32_bf16 v[32:35], v[132:135], v[80:83], v[32:35]\n\t"
      "ds_read_b128 v[76:79], v179 offset:0\n\t"
      "v_mfma_f32_16x16x32_bf16 v[36:39], v[136:139], v[80:83], v[36:39]\n\t"
      "s_waitcnt lgkmcnt(7)\n\t"
      "v_mfma_f32_16x16x32_bf16 v[40:43], v[132:135], v[84:87], v[40:43]\n\t"
      "ds_read_b128 v[80:83], v178 offset:18432\n\t"
      "v_mfma_f32_16x16x32_bf16 v[44:47], v[136:139], v[84:87], v[44:47]\n\t"
      "s_waitcnt lgkmcnt(6)\n\t"
      "v_mfma_f32_16x16x32_bf16 v[0:3], v[132:135], v[92:95], v[0:3]\n\t"
      "ds_read_b128 v[84:87], v179 offset:18432\n\t"
      "v_mfma_f32_16x16x32_bf16 v[4:7], v[136:139], v[92:95], v[4:7]\n\t"
      "v_mfma_f32_16x16x32_bf16 v[64:67], v[88:91], v[92:95], v[64:67]\n\t"
      "s_waitcnt lgkmcnt(6)\n\t"
      "v_mfma_f32_16x16x32_bf16 v[8:11], v[132:135], v[96:99], v[8:11]\n\t"
      "v_mfma_f32_16x16x32_bf16 v[12:15], v[136:139], v[96:99], v[12:15]\n\t"
      "v_mfma_f32_16x16x32_bf16 v[68:71], v[88:91], v[96:99], v[68:71]\n\t"
      "ds_read_b128 v[88:91], v178 offset:2048\n\t"
      "s_waitcnt lgkmcnt(6)\n\t"
      "v_mfma_f32_16x16x32_bf16 v[48:51], v[132:135], v[100:103], v[48:51]\n\t"
      "ds_read_b128 v[92:95], v179 offset:2048\n\t"
      "v_mfma_f32_16x16x32_bf16 v[52:55], v[136:139], v[100:103], v[52:55]\n\t"
      "ds_read_b128 v[96:99], v178 offset:20480\n\t"
      "s_waitcnt lgkmcnt(7)\n\t"
      "v_mfma_f32_16x16x32_bf16 v[56:59], v[132:135], v[104:107], v[56:59]\n\t"
      "ds_read_b128 v[100:103], v179 offset:20480\n\t"
      "v_mfma_f32_16x16x32_bf16 v[60:63], v[136:139], v[104:107], v[60:63]\n\t"
      "s_waitcnt vmcnt(6)\n\t"
      "s_waitcnt lgkmcnt(7)\n\t"
      "v_mfma_f32_16x16x32_bf16 v[16:19], v[140:143], v[72:75], v[16:19]\n\t"
      "ds_read_b128 v[104:107], v180 offset:0\n\t"
      "v_lshlrev_b32 v124, 16, v224\n\t"
      "v_and_b32 v224, %[msk], v224\n\t"
      "v_mfma_f32_16x16x32_bf16 v[20:23], v[144:147], v[72:75], v[20:23]\n\t"
      "v_lshlrev_b32 v125, 16, v225\n\t"
      "v_and_b32 v225, %[msk], v225\n\t"
      "s_waitcnt vmcnt(4)\n\t"
      "s_waitcnt lgkmcnt(7)\n\t"
      "v_mfma_f32_16x16x32_bf16 v[16:19], v[148:151], v[76:79], v[16:19]\n\t"
      "ds_read_b128 v[72:75], v181 offset:0\n\t"
      "v_lshlrev_b32 v126, 16, v226\n\t"
      "v_and_b32 v226, %[msk], v226\n\t"
      "v_mfma_f32_16x16x32_bf16 v[20:23], v[152:155], v[76:79], v[20:23]\n\t"
      "v_lshlrev_b32 v127, 16, v227\n\t"
      "v_and_b32 v227, %[msk], v227\n\t"
      "s_waitcnt vmcnt(2)\n\t"
      "s_waitcnt lgkmcnt(7)\n\t"
      "v_mfma_f32_16x16x32_bf16 v[16:19], v[156:159], v[80:83], v[16:19]\n\t"
      "ds_read_b128 v[76:79], v180 offset:18432\n\t"
      "v_mfma_f32_16x16x32_bf16 v[20:23], v[160:163], v[80:83], v[20:23]\n\t"
      "s_waitcnt vmcnt(0)\n\t"
      "s_waitcnt lgkmcnt(7)\n\t"
      "v_mfma_f32_16x16x32_bf16 v[16:19], v[164:167], v[84:87], v[16:19]\n\t"
      "ds_read_b128 v[80:83], v181 offset:18432\n\t"
      "v_mfma_f32_16x16x32_bf16 v[20:23], v[168:171], v[84:87], v[20:23]\n\t"
      "s_waitcnt lgkmcnt(7)\n\t"
      "v_mfma_f32_16x16x32_bf16 v[24:27], v[140:143], v[88:91], v[24:27]\n\t"
      "ds_read_b128 v[84:87], v180 offset:2048\n\t"
      "v_mfma_f32_16x16x32_bf16 v[28:31], v[144:147], v[88:91], v[28:31]\n\t"
      "s_waitcnt lgkmcnt(7)\n\t"
      "v_mfma_f32_16x16x32_bf16 v[24:27], v[148:151], v[92:95], v[24:27]\n\t"
      "ds_read_b128 v[88:91], v181 offset:2048\n\t"
      "v_mfma_f32_16x16x32_bf16 v[28:31], v[152:155], v[92:95], v[28:31]\n\t"
      "s_waitcnt lgkmcnt(7)\n\t"
      "v_mfma_f32_16x16x32_bf16 v[24:27], v[156:159], v[96:99], v[24:27]\n\t"
      "ds_read_b128 v[92:95], v180 offset:20480\n\t"
      "v_mul_f32 v16, v236, v16\n\t"
      "v_mul_f32 v17, v200, v17\n\t"
      "v_mfma_f32_16x16x32_bf16 v[28:31], v[160:163], v[96:99], v[28:31]\n\t"
      "v_mul_f32 v18, v237, v18\n\t"
      "v_mul_f32 v19, v201, v19\n\t"
      "s_waitcnt lgkmcnt(7)\n\t"
      "v_mfma_f32_16x16x32_bf16 v[24:27], v[164:167], v[100:103], v[24:27]\n\t"
      "ds_read_b128 v[96:99], v181 offset:20480\n\t"
      "v_mul_f32 v20, v238, v20\n\t"
      "v_mul_f32 v21, v202, v21\n\t"
      "v_mfma_f32_16x16x32_bf16 v[28:31], v[168:171], v[100:103], v[28:31]\n\t"
      "v_mul_f32 v22, v239, v22\n\t"
      "v_mul_f32 v23, v203, v23\n\t"
      "s_waitcnt lgkmcnt(7)\n\t"
      "v_mfma_f32_16x16x32_bf16 v[32:35], v[140:143], v[104:107], v[32:35]\n\t"
      "ds_read_b128 v[108:111], v184 offset:0\n\t"
      "v_cvt_pk_bf16_f32 v200, v16, v17\n\t"
      "v_cvt_pk_bf16_f32 v201, v18, v19\n\t"
      "v_mfma_f32_16x16x32_bf16 v[36:39], v[144:147], v[104:107], v[36:39]\n\t"
      "v_cvt_pk_bf16_f32 v202, v20, v21\n\t"
      "v_cvt_pk_bf16_f32 v203, v22, v23\n\t"
      "s_waitcnt lgkmcnt(7)\n\t"
      "v_mfma_f32_16x16x32_bf16 v[32:35], v[148:151], v[72:75], v[32:35]\n\t"
      "ds_read_b128 v[112:115], v184 offset:1024\n\t"
      "s_xor_b32 %[cnt], %[wm2], 2\n\ts_mul_i32 %[cnt], %[cnt], 73728\n\t"
      "buffer_store_dwordx4 v[200:203], v185, %[ur], %[cnt] offen sc1\n\t"
      "v_mfma_f32_16x16x32_bf16 v[36:39], v[152:155], v[72:75], v[36:39]\n\t"
      "v_mul_f32 v24, v240, v24\n\t"
      "v_mul_f32 v25, v204, v25\n\t"
      "s_waitcnt lgkmcnt(7)\n\t"
      "v_mfma_f32_16x16x32_bf16 v[32:35], v[156:159], v[76:79], v[32:35]\n\t"
      "ds_read_b128 v[116:119], v184 offset:18432\n\t"
      "v_mul_f32 v26, v241, v26\n\t"
      "v_mul_f32 v27, v205, v27\n\t"
      "v_mfma_f32_16x16x32_bf16 v[36:39], v[160:163], v[76:79], v[36:39]\n\t"
      "v_mul_f32 v28, v242, v28\n\t"
      "v_mul_f32 v29, v206, v29\n\t"
      "s_waitcnt lgkmcnt(7)\n\t"
      "v_mfma_f32_16x16x32_bf16 v[32:35], v[164:167], v[80:83], v[32:35]\n\t"
      "ds_read_b128 v[120:123], v184 offset:19456\n\t"
      "v_mul_f32 v30, v243, v30\n\t"
      "v_mul_f32 v31, v207, v31\n\t"
      "v_mfma_f32_16x16x32_bf16 v[36:39], v[168:171], v[80:83], v[36:39]\n\t"
      "v_cvt_pk_bf16_f32 v204, v24, v25\n\t"
      "v_cvt_pk_bf16_f32 v205, v26, v27\n\t"
      "s_waitcnt lgkmcnt(7)\n\t"
      "v_mfma_f32_16x16x32_bf16 v[40:43], v[140:143], v[84:87], v[40:43]\n\t"
      "ds_read_b128 v[100:103], v176 offset:0\n\t"
      "v_cvt_pk_bf16_f32 v206, v28, v29\n\t"
      "v_cvt_pk_bf16_f32 v207, v30, v31\n\t"
      "v_mfma_f32_16x16x32_bf16 v[44:47], v[144:147], v[84:87], v[44:47]\n\t"
      "s_xor_b32 %[cnt], %[wm2], 3\n\ts_mul_i32 %[cnt], %[cnt], 73728\n\t"
      "buffer_store_dwordx4 v[204:207], v185, %[ur], %[cnt] offen sc1\n\t"
      "s_waitcnt lgkmcnt(7)\n\t"
      "v_mfma_f32_16x16x32_bf16 v[40:43], v[148:151], v[88:91], v[40:43]\n\t"
      "ds_read_b128 v[104:107], v177 offset:0\n\t"
      "v_mul_f32 v32, v244, v32\n\t"
      "v_mul_f32 v33, v208, v33\n\t"
      "v_mfma_f32_16x16x32_bf16 v[44:47], v[152:155], v[88:91], v[44:47]\n\t"
      "v_mul_f32 v34, v245, v34\n\t"
      "v_mul_f32 v35, v209, v35\n\t"
      "s_waitcnt lgkmcnt(7)\n\t"
      "v_mfma_f32_16x16x32_bf16 v[40:43], v[156:159], v[92:95], v[40:43]\n\t"
      "ds_read_b128 v[72:75], v176 offset:18432\n\t"
      "v_mul_f32 v36, v246, v36\n\t"
      "v_mul_f32 v37, v210, v37\n\t"
      "v_mfma_f32_16x16x32_bf16 v[44:47], v[160:163], v[92:95], v[44:47]\n\t"
      "v_mul_f32 v38, v247, v38\n\t"
      "v_mul_f32 v39, v211, v39\n\t"
      "s_waitcnt lgkmcnt(7)\n\t"
      "v_mfma_f32_16x16x32_bf16 v[40:43], v[164:167], v[96:99], v[40:43]\n\t"
      "ds_read_b128 v[76:79], v177 offset:18432\n\t"
      "v_cvt_pk_bf16_f32 v208, v32, v33\n\t"
      "v_cvt_pk_bf16_f32 v209, v34, v35\n\t"
      "v_mfma_f32_16x16x32_bf16 v[44:47], v[168:171], v[96:99], v[44:47]\n\t"
      "v_cvt_pk_bf16_f32 v210, v36, v37\n\t"
      "v_cvt_pk_bf16_f32 v211, v38, v39\n\t"
      "s_waitcnt lgkmcnt(3)\n\t"
      "v_mfma_f32_16x16x32_bf16 v[0:3], v[140:143], v[100:103], v[0:3]\n\t"
      "ds_read_b128 v[80:83], v176 offset:2048\n\t"
      "ds_read_b128 v[84:87], v177 offset:2048\n\t"
      "s_xor_b32 %[cnt], %[wm2], 4\n\ts_mul_i32 %[cnt], %[cnt], 73728\n\t"
      "buffer_store_dwordx4 v[208:211], v185, %[ur], %[cnt] offen sc1\n\t"
      "v_mfma_f32_16x16x32_bf16 v[4:7], v[144:147], v[100:103], v[4:7]\n\t"
      "ds_read_b128 v[88:91], v176 offset:20480\n\t"
      "ds_read_b128 v[92:95], v177 offset:20480\n\t"
      "v_mfma_f32_16x16x32_bf16 v[64:67], v[108:111], v[100:103], v[64:67]\n\t"
      "ds_read_b128 v[96:99], v182 offset:0\n\t"
      "v_mul_f32 v40, v248, v40\n\t"
      "v_mul_f32 v41, v212, v41\n\t"
      "s_waitcnt lgkmcnt(7)\n\t"
      "v_mfma_f32_16x16x32_bf16 v[0:3], v[148:151], v[104:107], v[0:3]\n\t"
      "ds_read_b128 v[100:103], v183 offset:0\n\t"
      "v_mul_f32 v42, v249, v42\n\t"
      "v_mul_f32 v43, v213, v43\n\t"
      "v_mfma_f32_16x16x32_bf16 v[4:7], v[152:155], v[104:107], v[4:7]\n\t"
      "v_mul_f32 v44, v250, v44\n\t"
      "v_mul_f32 v45, v214, v45\n\t"
      "v_mfma_f32_16x16x32_bf16 v[64:67], v[112:115], v[104:107], v[64:67]\n\t"
      "v_mul_f32 v46, v251, v46\n\t"
      "v_mul_f32 v47, v215, v47\n\t"
      "s_waitcnt lgkmcnt(7)\n\t"
      "v_mfma_f32_16x16x32_bf16 v[0:3], v[156:159], v[72:75], v[0:3]\n\t"
      "ds_read_b128 v[104:107], v182 offset:18432\n\t"
      "v_cvt_pk_bf16_f32 v212, v40, v41\n\t"
      "v_cvt_pk_bf16_f32 v213, v42, v43\n\t"
      "v_mfma_f32_16x16x32_bf16 v[4:7], v[160:163], v[72:75], v[4:7]\n\t"
      "v_cvt_pk_bf16_f32 v214, v44, v45\n\t"
      "v_cvt_pk_bf16_f32 v215, v46, v47\n\t"
      "v_mfma_f32_16x16x32_bf16 v[64:67], v[116:119], v[72:75], v[64:67]\n\t"
      "s_xor_b32 %[cnt], %[wm2], 5\n\ts_mul_i32 %[cnt], %[cnt], 73728\n\t"
      "buffer_store_dwordx4 v[212:215], v185, %[ur], %[cnt] offen sc1\n\t"
      "s_waitcnt lgkmcnt(7)\n\t"
      "v_mfma_f32_16x16x32_bf16 v[0:3], v[164:167], v[76:79], v[0:3]\n\t"
      "ds_read_b128 v[72:75], v183 offset:18432\n\t"
      "v_mfma_f32_16x16x32_bf16 v[4:7], v[168:171], v[76:79], v[4:7]\n\t"
      "v_mfma_f32_16x16x32_bf16 v[64:67], v[120:123], v[76:79], v[64:67]\n\t"
      "s_waitcnt lgkmcnt(7)\n\t"
      "v_mfma_f32_16x16x32_bf16 v[8:11], v[140:143], v[80:83], v[8:11]\n\t"
      "ds_read_b128 v[76:79], v182 offset:2048\n\t"
      "v_mfma_f32_16x16x32_bf16 v[12:15], v[144:147], v[80:83], v[12:15]\n\t"
      "v_mfma_f32_16x16x32_bf16 v[68:71], v[108:111], v[80:83], v[68:71]\n\t"
      "s_waitcnt lgkmcnt(7)\n\t"
      "v_mfma_f32_16x16x32_bf16 v[8:11], v[148:151], v[84:87], v[8:11]\n\t"
      "ds_read_b128 v[80:83], v183 offset:2048\n\t"
      "v_mfma_f32_16x16x32_bf16 v[12:15], v[152:155], v[84:87], v[12:15]\n\t"
      "v_mfma_f32_16x16x32_bf16 v[68:71], v[112:115], v[84:87], v[68:71]\n\t"
      "v_mul_f32 v0, v228, v0\n\t"
      "v_mul_f32 v1, v192, v1\n\t"
      "s_waitcnt lgkmcnt(7)\n\t"
      "v_mfma_f32_16x16x32_bf16 v[8:11], v[156:159], v[88:91], v[8:11]\n\t"
      "ds_read_b128 v[84:87], v182 offset:20480\n\t"
      "v_mul_f32 v2, v229, v2\n\t"
      "v_mul_f32 v3, v193, v3\n\t"
      "v_mfma_f32_16x16x32_bf16 v[12:15], v[160:163], v[88:91], v[12:15]\n\t"
      "v_mul_f32 v4, v230, v4\n\t"
      "v_mul_f32 v5, v194, v5\n\t"
      "v_mfma_f32_16x16x32_bf16 v[68:71], v[116:119], v[88:91], v[68:71]\n\t"
      "v_mul_f32 v6, v231, v6\n\t"
      "v_mul_f32 v7, v195, v7\n\t"
      "s_waitcnt lgkmcnt(7)\n\t"
      "v_mfma_f32_16x16x32_bf16 v[8:11], v[164:167], v[92:95], v[8:11]\n\t"
      "ds_read_b128 v[88:91], v183 offset:20480\n\t"
      "v_cvt_pk_bf16_f32 v192, v0, v1\n\t"
      "v_cvt_pk_bf16_f32 v193, v2, v3\n\t"
      "v_mfma_f32_16x16x32_bf16 v[12:15], v[168:171], v[92:95], v[12:15]\n\t"
      "v_cvt_pk_bf16_f32 v194, v4, v5\n\t"
      "v_cvt_pk_bf16_f32 v195, v6, v7\n\t"
      "v_mfma_f32_16x16x32_bf16 v[68:71], v[120:123], v[92:95], v[68:71]\n\t"
      "s_xor_b32 %[cnt], %[wm2], 0\n\ts_mul_i32 %[cnt], %[cnt], 73728\n\t"
      "buffer_store_dwordx4 v[192:195], v185, %[ur], %[cnt] offen sc1\n\t"
      "s_waitcnt lgkmcnt(7)\n\t"
      "v_mfma_f32_16x16x32_bf16 v[48:51], v[140:143], v[96:99], v[48:51]\n\t"
      "v_mfma_f32_16x16x32_bf16 v[52:55], v[144:147], v[96:99], v[52:55]\n\t"
      "s_waitcnt lgkmcnt(6)\n\t"
      "v_mfma_f32_16x16x32_bf16 v[48:51], v[148:151], v[100:103], v[48:51]\n\t"
      "v_mfma_f32_16x16x32_bf16 v[52:55], v[152:155], v[100:103], v[52:55]\n\t"
      "s_waitcnt lgkmcnt(5)\n\t"
      "v_mfma_f32_16x16x32_bf16 v[48:51], v[156:159], v[104:107], v[48:51]\n\t"
      "v_mfma_f32_16x16x32_bf16 v[52:55], v[160:163], v[104:107], v[52:55]\n\t"
      "s_waitcnt lgkmcnt(4)\n\t"
      "v_mfma_f32_16x16x32_bf16 v[48:51], v[164:167], v[72:75], v[48:51]\n\t"
      "v_mul_f32 v8, v232, v8\n\t"
      "v_mul_f32 v9, v196, v9\n\t"
      "v_mfma_f32_16x16x32_bf16 v[52:55], v[168:171], v[72:75], v[52:55]\n\t"
      "v_mul_f32 v10, v233, v10\n\t"
      "v_mul_f32 v11, v197, v11\n\t"
      "s_waitcnt lgkmcnt(3)\n\t"
      "v_mfma_f32_16x16x32_bf16 v[56:59], v[140:143], v[76:79], v[56:59]\n\t"
      "v_mul_f32 v12, v234, v12\n\t"
      "v_mul_f32 v13, v198, v13\n\t"
      "v_mfma_f32_16x16x32_bf16 v[60:63], v[144:147], v[76:79], v[60:63]\n\t"
      "v_mul_f32 v14, v235, v14\n\t"
      "v_mul_f32 v15, v199, v15\n\t"
      "s_waitcnt lgkmcnt(2)\n\t"
      "v_mfma_f32_16x16x32_bf16 v[56:59], v[148:151], v[80:83], v[56:59]\n\t"
      "v_cvt_pk_bf16_f32 v196, v8, v9\n\t"
      "v_cvt_pk_bf16_f32 v197, v10, v11\n\t"
      "v_mfma_f32_16x16x32_bf16 v[60:63], v[152:155], v[80:83], v[60:63]\n\t"
      "v_cvt_pk_bf16_f32 v198, v12, v13\n\t"
      "v_cvt_pk_bf16_f32 v199, v14, v15\n\t"
      "s_waitcnt lgkmcnt(1)\n\t"
      "v_mfma_f32_16x16x32_bf16 v[56:59], v[156:159], v[84:87], v[56:59]\n\t"
      "s_xor_b32 %[cnt], %[wm2], 1\n\ts_mul_i32 %[cnt], %[cnt], 73728\n\t"
      "buffer_store_dwordx4 v[196:199], v185, %[ur], %[cnt] offen sc1\n\t"
      "v_mfma_f32_16x16x32_bf16 v[60:63], v[160:163], v[84:87], v[60:63]\n\t"
      "v_mul_f32 v64, v124, v64\n\t"
      "v_mul_f32 v65, v224, v65\n\t"
      "s_waitcnt lgkmcnt(0)\n\t"
      "v_mfma_f32_16x16x32_bf16 v[56:59], v[164:167], v[88:91], v[56:59]\n\t"
      "v_mul_f32 v66, v125, v66\n\t"
      "v_mul_f32 v67, v225, v67\n\t"
      "v_mfma_f32_16x16x32_bf16 v[60:63], v[168:171], v[88:91], v[60:63]\n\t"
      "v_mul_f32 v68, v126, v68\n\t"
      "v_mul_f32 v69, v226, v69\n\t"
      "v_mul_f32 v70, v127, v70\n\t"
      "v_mul_f32 v71, v227, v71\n\t"
      "v_cvt_pk_bf16_f32 v224, v64, v65\n\t"
      "v_cvt_pk_bf16_f32 v225, v66, v67\n\t"
      "v_cvt_pk_bf16_f32 v226, v68, v69\n\t"
      "v_cvt_pk_bf16_f32 v227, v70, v71\n\t"
      "s_nop 1\n\tv_permlane16_swap_b32 v224, v226\n\t"
      "v_permlane16_swap_b32 v225, v227\n\t"
      "s_nop 0\n\tbuffer_store_dwordx4 v[224:227], v175, %[ur], %[u8] offen sc1\n\t"
      "v_mul_f32 v48, v252, v48\n\t"
      "v_mul_f32 v49, v216, v49\n\t"
      "v_mul_f32 v50, v253, v50\n\t"
      "v_mul_f32 v51, v217, v51\n\t"
      "v_mul_f32 v52, v186, v52\n\t"
      "v_mul_f32 v53, v218, v53\n\t"
      "v_mul_f32 v54, v187, v54\n\t"
      "v_mul_f32 v55, v219, v55\n\t"
      "v_cvt_pk_bf16_f32 v216, v48, v49\n\t"
      "v_cvt_pk_bf16_f32 v217, v50, v51\n\t"
      "v_cvt_pk_bf16_f32 v218, v52, v53\n\t"
      "v_cvt_pk_bf16_f32 v219, v54, v55\n\t"
      "s_xor_b32 %[cnt], %[wm2], 6\n\ts_mul_i32 %[cnt], %[cnt], 73728\n\t"
      "buffer_store_dwordx4 v[216:219], v185, %[ur], %[cnt] offen sc1\n\t"
      "v_mul_f32 v56, v188, v56\n\t"
      "v_mul_f32 v57, v220, v57\n\t"
      "v_mul_f32 v58, v189, v58\n\t"
      "v_mul_f32 v59, v221, v59\n\t"
      "v_mul_f32 v60, v190, v60\n\t"
      "v_mul_f32 v61, v222, v61\n\t"
      "v_mul_f32 v62, v191, v62\n\t"
      "v_mul_f32 v63, v223, v63\n\t"
      "v_cvt_pk_bf16_f32 v220, v56, v57\n\t"
      "v_cvt_pk_bf16_f32 v221, v58, v59\n\t"
      "v_cvt_pk_bf16_f32 v222, v60, v61\n\t"
      "v_cvt_pk_bf16_f32 v223, v62, v63\n\t"
      "s_xor_b32 %[cnt], %[wm2], 7\n\ts_mul_i32 %[cnt], %[cnt], 73728\n\t"
      "buffer_store_dwordx4 v[220:223], v185, %[ur], %[cnt] offen sc1\n\t"
      "v_mov_b32 v173, 34\n\t"
      "ds_write_b32 v172, v173\n\t"
      "s_branch 9f\n\t"
      "11:\n\t"
      "ds_read_b32 v174, v172 offset:64\n\t"
      "s_waitcnt lgkmcnt(0)\n\t"
      "v_cmp_gt_u32 vcc, 34, v174\n\t"
      "s_cbranch_vccnz 11b\n\t"
      "s_branch 12b\n\t"
      "9:\n\t"
      "s_nop 1\n\t"
      : "+{v[192:193]}"(z1[0]), "+{v[194:195]}"(z1[1]), "+{v[196:197]}"(z1[2]), "+{v[198:199]}"(z1[3]), "+{v[200:201]}"(z1[4]), "+{v[202:203]}"(z1[5]), "+{v[204:205]}"(z1[6]), "+{v[206:207]}"(z1[7]), "+{v[208:209]}"(z1[8]), "+{v[210:211]}"(z1[9]), "+{v[212:213]}"(z1[10]), "+{v[214:215]}"(z1[11]), "+{v[216:217]}"(z1[12]), "+{v[218:219]}"(z1[13]), "+{v[220:221]}"(z1[14]), "+{v[222:223]}"(z1[15]), "+{v[224:225]}"(z1[16]), "+{v[226:227]}"(z1[17]), [cnt] "=&s"(cnt), [d0] "=&s"(d0), [dz] "=&s"(dz), [so0] "+s"(so0), [so1] "+s"(so1), "+{v[176:183]}"(ad), "+{v[184:185]}"(bd), "+{v[186:189]}"(ba),
        "+{v[108:123]}"(zf[0]), "+{v[124:139]}"(zf[1]), "+{v[140:155]}"(zf[2])
      : [zr] "s"(zr), [j] "s"(j), [dw] "s"(CSL - 32 * CSL), "{v172}"(lb + FL_CP + wm * 16 + (lane & 3) * 4), "{v175}"((g & 1) * US + (g >> 1) * 256 + r * 16),
        [ur] "s"(ur), [wm2] "s"(2 * wm), [u8] "s"(wm * (2 * US + 1024) + 8192 - (tn & 1) * 3584), [msk] "s"(0xffff0000u) : "memory", "scc", "vcc", "v0", "v1", "v2", "v3", "v4", "v5", "v6", "v7", "v8", "v9", "v10", "v11", "v12", "v13", "v14", "v15", "v16", "v17", "v18", "v19", "v20", "v21", "v22", "v23", "v24", "v25", "v26", "v27", "v28", "v29", "v30", "v31", "v32", "v33", "v34", "v35", "v36", "v37", "v38", "v39", "v40", "v41", "v42", "v43", "v44", "v45", "v46", "v47", "v48", "v49", "v50", "v51", "v52", "v53", "v54", "v55", "v56", "v57", "v58", "v59", "v60", "v61", "v62", "v63", "v64", "v65", "v66", "v67", "v68", "v69", "v70", "v71", "v72", "v73", "v74", "v75", "v76", "v77", "v78", "v79", "v80", "v81", "v82", "v83", "v84", "v85", "v86", "v87", "v88", "v89", "v90", "v91", "v92", "v93", "v94", "v95", "v96", "v97", "v98", "v99", "v100", "v101", "v102", "v103", "v104", "v105", "v106", "v107", "v156", "v157", "v158", "v159", "v160", "v161", "v162", "v163", "v164", "v165", "v166", "v167", "v168", "v169", "v170", "v171", "v173", "v174", "v190", "v191", "v228", "v229", "v230", "v231", "v232", "v233", "v234", "v235", "v236", "v237", "v238", "v239", "v240", "v241", "v242", "v243", "v244", "v245", "v246", "v247", "v248", "v249", "v250", "v251", "v252", "v253"
        );
  }
  TS(6);
  const int t3 = tm * 2 + DTH, j = DJ, xw = EPI >= 2 ? (wm & 1) : wm;
  int lane, r, g, vo, vh, hx;
  auto lanes = [&]() AINL {
    lane = lane_id(); r = lane & 15; g = lane >> 4; vo = lane * 16; hx = (g * 8 + (r & 7)) * 16;
    vh = 4096 + (g * 16 + (j & 1) * 8 + (r & 7)) * 16;
  };
  auto ru = RSRC(ws + WS_U + (size_t)(t3 * 4 * US + 2 * wm * 9216), 4 * US);
  auto rw = RSRC(ws + WS_WO + (size_t)wm * 18 * 36864 + (j >> 1) * 9216, 18 * 36864);
  const int drot = d_rot(tm, tn);
  int su, sw_;
  auto dst = [&](int q) AINL { const int s = d_step(drot, q); su = s * 1024; sw_ = s * 36864; };
  auto ldu = [&](int q, int m) AINL {
    if (DR) return __builtin_bit_cast(bf16x8, __builtin_amdgcn_raw_buffer_load_b128(ru, vo, su + (m ^ xw) * US, 16));
    return __builtin_bit_cast(bf16x8, __builtin_amdgcn_raw_buffer_load_b128(ru, vo + (q & 3) * 1024, (q >> 2) * 4096 + (m ^ xw) * US, 16)); };
  auto ldw = [&](int q, int n) AINL { return __builtin_bit_cast(bf16x8, __builtin_amdgcn_raw_buffer_load_b128(rw, n < 4 ? vo + n * 1024 : vh, sw_ + (n < 4 ? (j & 1) * 5120 : 0), 16)); };
  f32x4 oc[4][5];
  bf16x8 a[4][4], bb[4][5];
  char* p1 = smem + d_p1(wm);
  char* px = p1 + 16384;
  char* p2 = smem + D_PB + wm * CSL;
  lds_add1(lb + FL_UISS);
  lds_wait1(lb + FL_UISS, 4u);
  lanes();
  const int xo = T_XS + wm * 1024;
  auto xg = [&]() AINL { GLDS(RSRC(A->x + (size_t)(t3 * 64 + xw * 16) * D + j * 72 + (j & 1) * 8 + (EPI == 3 ? 32 * (wm >> 1) : 0), 15 * D * 2 + 64), xo, (lane >> 2) * (D * 2) + (lane & 3) * 16, 0); };
  {
    unsigned f4;
    char* f4p = ws + WS_F4 + (tm * 16 + tn) * 256 + wm * 64 + r * 4;
    const int oh = (lane >> 5) * 36864 + 4096 + (((lane >> 3) & 3) * 16 + (j & 1) * 8 + (lane & 7)) * 16;
#pragma unroll
    for (int k = 0; k <= 38; ++k) {
      if (k == FK) {
        FENCE();
        wait_vm<FK>();
        if (lane < 16) flag_set(ws + WS_F4 + (tm * 16 + lane) * 256 + tn * 16 + wm * 4, ep);
        TS(8);
        FENCE();
      }
      if (k == XK) { FENCE(); xg(); FENCE(); }
      if (k == LK) { FENCE(); f4 = flag_get(f4p); FENCE(); }
      if (k == UP) {
        FENCE();
        TS(20);
        asm volatile("" : "+v"(f4));
        if (!all_lanes(f4 == ep)) poll_flags(f4p, ep);
        asm volatile("" ::: "memory");
        TS(9);
#pragma unroll
        for (int h = 0; h < 4; ++h) {
          dst(h);
#pragma unroll
          for (int m = 3; m >= 0; --m) { a[h][m] = ldu(h, m); SB(); }
        }
        FENCE();
      }
      if (k == 38) break;
      const int kb = k - NA0;
      if (kb >= 0 && kb < 20) { if (kb % 5 == 0 || k == UP) dst(kb / 5); bb[kb / 5][kb % 5] = ldw(kb / 5, kb % 5); }
      else {
        const int ka = kb < 0 ? k : k - 20, i = ka < 4 ? ka : (ka == 4 ? 16 : (ka < 13 ? ka - 1 : (ka == 13 ? 17 : ka - 2)));
        __builtin_amdgcn_raw_ptr_buffer_load_lds(rw, (LAS void*)((LAS char*)smem + d_p1(wm) + i * 1024), 16, i < 16 ? vo : oh,
          i < 16 ? d_step(drot, 4 + (i >> 2)) * 36864 + (j & 1) * 5120 + (i & 3) * 1024 : d_step(drot, 4 + 2 * (i - 16)) * 36864, 0, 16);
      }
    }
    FENCE();
    TS(21);
  }
  auto put = [&](int m) AINL {
    char* wp = p1 + vo + (m ^ xw) * 4096;
#pragma unroll
    for (int n = m == 0 && EPI < 2 ? 2 : 0; n < 4; ++n) *(LAS f32x4*)(wp + n * 1024) = oc[m][n];
    if (g < 2) *(LAS f32x4*)(px + (m ^ xw) * 512 + lane * 16) = oc[m][4];
  };
  float e_st[4]; u32x2 e_x0, e_x1; GAS u16 *e_ob, *e_op; int e_oo; unsigned e_ya, e_pa[3]; DPre e_d0, e_d1; float e_s3;
#pragma unroll
  for (int p = 0; p < 18; ++p) {
    const int h = p & 3, q = p + 4, ql = p + LL;
    if (ql == 8) { TS(14); lds_wait1(lb + FL_PB + wm * 4, 1u); TS(15); }
    if (p == 1) TS(16);
    if (p == 8) TS(17);
    if (p == 12) TS(18);
    if (ql == 4 || ql == 8 || q == 12 || p == 17) { SB(); lanes(); }
    if (p == EP0) {
      SB();
      const int l = lane_id(), r_ = l & 15, g_ = l >> 4;
      { const LAS float* sp = (const LAS float*)(smem + T_ROW + (DTH * 64 + wm * 16 + r_) * 16); e_st[0] = sp[0]; e_st[3] = sp[3]; }
      e_x0 = *(const LAS u32x2*)(smem + xo + r_ * 64 + g_ * 8); e_x1 = *(const LAS u32x2*)(smem + xo + r_ * 64 + g_ * 8 + 32);
      e_ob = A->out;
      e_oo = (t3 * 64 + wm * 16 + r_) * D + j * 72 + 4 * g_ + (j & 1) * 8;
      e_ya = lb + D_Y + (4 * g_ + (j & 1) * 8 + (r_ >> 2)) * 128 + (wm * 16 + (r_ & 3) * 4) * 2;
#pragma unroll
      for (int k = 1; k < 4; ++k) e_pa[k - 1] = lb + d_p1(wm ^ k) + wm * 4096 + l * 16;
      asm volatile("" : "+v"(e_oo), "+v"(e_ya), "+v"(e_pa[0]), "+v"(e_pa[1]), "+v"(e_pa[2]));
      SB();
    }
    if (p == EP0 + 1) {
      SB();
      e_d0 = d_pre(e_x0, e_st[0]); e_d1 = d_pre(e_x1, e_st[0]);
      e_s3 = e_st[3]; e_op = e_ob + e_oo;
      asm volatile("" : "+v"(e_s3), "+v"(e_op));
      SB();
    }
    if (q < 18) dst(q);
#pragma unroll
    for (int mi = 0; mi < 4; ++mi) {
      const int m = 3 - mi;
      if (p >= 18 - TGM(m)) continue;
#pragma unroll
      for (int n = 0; n < 5; ++n) {
        oc[m][n] = mfma32(bb[h][n], a[h][m], p == 0 ? (f32x4){0.f, 0.f, 0.f, 0.f} : oc[m][n]);
        if (mi == 3 && q >= 12 && q < 18) { SB(); bb[h][n] = ldw(q, n); SB(); }
        if (mi == (LL == 4 ? 3 : LM) && ql >= 4 && ql < 12) {
          char* pp = ql < 8 ? p1 : p2;
          SB();
          bb[ql & 3][n] = n < 4 ? *(const LAS bf16x8*)(pp + vo + ((ql & 3) * 4 + n) * 1024) : *(const LAS bf16x8*)(pp + 16384 + (ql & 3) * 512 + hx);
          SB();
        }
      }
      if (q < 18 - TGM(m)) { SB(); a[h][m] = ldu(q, m); SB(); }
      if (p == 17 && mi > 0) { SB(); put(m + 1); SB(); }
    }
    if (q == 17) {
#pragma unroll
      for (int m = 2; m >= 0; --m)
#pragma unroll
        for (int s_ = 18 - TGM(m); s_ < 18; ++s_) { SB(); dst(s_); a[s_ & 3][m] = ldu(s_, m); SB(); }
    }
  }
#pragma unroll
  for (int m = 2; m >= 0; --m)
#pragma unroll
    for (int s_ = 18 - TGM(m); s_ < 18; ++s_) {
      if (TPE && s_ == 18 - TGM(m)) { SB(); put(m + 1); SB(); }
#pragma unroll
      for (int n = 0; n < 5; ++n) oc[m][n] = mfma32(bb[s_ & 3][n], a[s_ & 3][m], oc[m][n]);
      if (!TPE && s_ == 18 - TGM(m)) { SB(); put(m + 1); SB(); }
      if (EPI >= 2 && m == 1 && s_ == 18 - TG1) { SB(); wait_lgkm0(); lds_add1(lb + FL_UISS); SB(); }
    }
  TS(10);
  put(0);
  wait_lgkm0();
  bar();
  TS(11);
  {
    f32x4 pa[3], pb[3];
#pragma unroll
    for (int k = 0; k < 3; ++k) pa[k] = d_ld(e_pa[k], 0);
    const u32x2 y0 = d_y(e_ya, 0);
    SB();
#pragma unroll
    for (int k = 0; k < 3; ++k) pb[k] = d_ld(e_pa[k], 1024);
    const u32x2 y1 = d_y(e_ya, 2048);
    SB();
    d_post(((oc[0][0] + pa[0]) + pa[1]) + pa[2], y0, e_d0, e_s3, e_op);
    SB();
    d_post(((oc[0][1] + pb[0]) + pb[1]) + pb[2], y1, e_d1, e_s3, e_op + 16);
  }
  TS(12);
  asm volatile(".Lk_end0:");
  if (wm == 0) TS_FLUSH(0, 0, 24);
  if (wm == 3) TS_FLUSH(0, 1, 24);
}

__device__ __forceinline__ void conv_stage_h(char* smem, int ho, int lane, const u32x4 (&hx)[4]) {
  const int i = lane & 15, g = lane >> 4;
  if (i + g < 15) *(LAS unsigned*)(smem + ho + g * CV_HS + i * 4) = 0u;
  char* p = smem + ho + lane * 16;
#pragma unroll
  for (int n = 0; n < 4; ++n) {
    *(LAS u32x4*)(p + 3 * CV_HS + 48 + n * 1024) = hx[n];
    *(LAS u32x2*)(p + CV_HS + 56 + n * 1024) = (u32x2){hx[n][0], hx[n][1]};
    *(LAS u32x2*)(p + CV_HS + 64 + n * 1024) = (u32x2){hx[n][2], hx[n][3]};
    *(LAS unsigned*)(p + 60 + n * 1024) = hx[n][0];
    *(LAS u32x2*)(p + 64 + n * 1024) = (u32x2){hx[n][1], hx[n][2]};
    *(LAS unsigned*)(p + 72 + n * 1024) = hx[n][3];
    *(LAS unsigned*)(p + 2 * CV_HS + 52 + n * 1024) = hx[n][0];
    *(LAS u32x2*)(p + 2 * CV_HS + 56 + n * 1024) = (u32x2){hx[n][1], hx[n][2]};
    *(LAS unsigned*)(p + 2 * CV_HS + 64 + n * 1024) = hx[n][3];
  }
}
__device__ __forceinline__ void conv_stage_x(char* smem, int xo, int lane, const u32x4 (&kx)[4], const unsigned (&kn)[4]) {
  *(LAS u32x4*)(smem + xo + lane * 16) = (u32x4){0u, 0u, 0u, 0u};
  char* p = smem + xo + 1024 + (lane >> 3) * 256 + (lane & 7) * 16;
#pragma unroll
  for (int n = 0; n < 4; ++n) {
    *(LAS u32x4*)(p + n * 2048 + 128) = kx[n];
    *(LAS u32x4*)(p + n * 2048) = (u32x4){__builtin_amdgcn_alignbit(kx[n][1], kx[n][0], 16), __builtin_amdgcn_alignbit(kx[n][2], kx[n][1], 16), __builtin_amdgcn_alignbit(kx[n][3], kx[n][2], 16), __builtin_amdgcn_alignbit(kn[n], kx[n][3], 16)};
  }
}
constexpr int cv_h(int q) { return q == 0 ? CV_H0 : (q == 1 ? CV_H1 : CV_H2); }
constexpr int cv_x(int q) { return q == 0 ? CV_X0 : (q == 1 ? CV_X1 : CV_X2); }
#define PCMP "s_waitcnt lgkmcnt(0)\n\tv_cmp_gt_u32 vcc, %2, %0\n\ts_cbranch_vccz 2f\n\t"
#define PCHK "ds_read_b32 %0, %1\n\t" PCMP
#define PSIG(w, s) "s_waitcnt vmcnt(" w ")\n\tv_mov_b32 %0, " s "\n\ts_mov_b64 exec, 15\n\tds_max_u32 %1, %0 offset:64\n\ts_mov_b64 exec, -1\n\t"
#define PLOOP "1:\n\t" PCHK "s_sleep " PSL "\n\ts_branch 1b\n2:"
#define CPRE() asm volatile("ds_read_b32 %0, %1" : "=v"(prv) : "v"(pra) : "memory")
#define CWAIT(n) asm volatile(PCMP PLOOP : "+v"(prv) : "v"(pra), "n"(n) : "memory", "vcc")
#define CWAIT1(n, w1, s1) asm volatile(PCMP PSIG("%3", "%4") PLOOP : "+v"(prv) : "v"(pra), "n"(n), "n"(w1), "n"(s1) : "memory", "vcc")
#define CWAIT2(n, w1, s1, w2, s2) asm volatile(PCMP PSIG("%3", "%4") PCHK PSIG("%5", "%6") PLOOP : "+v"(prv) : "v"(pra), "n"(n), "n"(w1), "n"(s1), "n"(w2), "n"(s2) : "memory", "vcc")
#define LSIG(w, n) do { unsigned t_; asm volatile(PSIG("%2", "%3") : "=&v"(t_) : "v"(pra), "n"(w), "n"(n) : "memory"); } while (0)
template <int MASK, int NC, class FP>
__device__ __forceinline__ void conv_run(char* smem, int q0, int cd, unsigned ddv, int lane, char* __restrict__ ws, unsigned ep, u32x4 (&pb)[18], int lw, FP&& parka TS_PAR) {
  const int i = lane & 15, g = lane >> 4;
  const unsigned lb = (unsigned)(uintptr_t)(LAS char*)smem, xo = lb + cv_x(q0) + 1024;
  unsigned aH = lb + cv_h(q0) + (i & 3) * CV_HS + ((i >> 2) + g) * 16;
  const unsigned aX = xo + (2 * g + (i & 1)) * 128;
  auto xadr = [&](int c) AINL { const int t = (i >> 1) - c; return aX + (t >> 3) * 1024 + (t & 7) * 16; };
  auto ld = [&](unsigned a) AINL { return *(const LAS bf16x8*)(uintptr_t)a; };
  auto ho = [&](int q) AINL { return cv_h(q) - CV_H0; };
  auto xq = [&](int q) AINL { return (unsigned)(cv_x(q) - CV_X0); };
  f32x4 acc[NC][8];
  bf16x8 Hh[NC][8], B[NC][8];
#define MM(q, a, e) acc[q][a + e] = mfma32(B[q][e], Hh[q][a], acc[q][a + e]); SB()
#define LH(q, a) Hh[q][a] = ld(aH + ho(q) + a * 512); SB()
#define LB(q, e) B[q][e] = ld(xa + xq(q) + e * 1024); SB()
  {
    const unsigned xa = xadr(0);
    if constexpr (MASK == 0xc3) { LB(0, 0); LH(0, 7); LB(0, 7); LH(0, 0); LH(0, 6); LH(0, 1); LB(0, 6); LB(0, 1); LB(0, 5); LH(0, 5); LH(0, 2); LB(0, 2); LB(0, 4); LH(0, 3); LH(0, 4); LB(0, 3); }
    if constexpr (MASK == 0x3c) { LB(0, 5); LH(0, 0); LB(0, 4); LB(0, 3); LH(0, 1); LB(0, 2); LH(0, 2); LB(0, 1); LH(0, 3); LB(0, 0); LH(0, 4); LH(0, 5); }
    if constexpr (MASK == 0xf8) { LB(0, 0); LH(0, 7); LH(0, 6); LH(0, 5); LB(0, 1); LH(0, 4); LH(0, 3); LB(0, 2); LH(0, 2); LB(0, 3); LB(0, 4); LH(0, 1); LB(0, 5); LH(0, 0); LB(0, 6); LB(0, 7); }
    if constexpr (MASK == 0x7) { LB(0, 2); LH(0, 0); LB(0, 0); LB(0, 1); LH(0, 2); LH(0, 1); LB(2, 2); LH(2, 0); LB(2, 1); LB(2, 0); LH(2, 1); LH(2, 2); LB(1, 0); LH(1, 2); LH(1, 1); LH(1, 0); LB(1, 1); LB(1, 2); }
#pragma unroll
    for (int J = 0; J < 8 * NC; ++J) acc[J >> 3][J & 7] = (f32x4){0.f, 0.f, 0.f, 0.f};
  }
  const unsigned pra = lb + FL_CP + (lane & 3) * 16 + lw * 4;
  unsigned prv;
  if (CVW == 0) { CPRE(); CWAIT(34); TS(11); }
#pragma unroll 1
  for (int c = 0; c < 8; ++c) {
    if (CVW > 0 && c == CVW) { CPRE(); CWAIT(34); TS(11); if (CVP == 2) __builtin_amdgcn_s_setprio(3); }
    parka(c - CVW);
    const unsigned xa = xadr(c + 1);
    aH += 64;
    if constexpr (MASK == 0xc3) { MM(0, 7, 0); LH(0, 7); MM(0, 0, 7); LB(0, 7); MM(0, 0, 0); MM(0, 6, 0); MM(0, 1, 0); LB(0, 0); MM(0, 0, 6); MM(0, 0, 1); LH(0, 0); MM(0, 6, 1); LH(0, 6); MM(0, 1, 6); LB(0, 6); MM(0, 1, 5); LH(0, 1); MM(0, 5, 1); LB(0, 1); MM(0, 2, 5); LB(0, 5); MM(0, 5, 2); LH(0, 5); MM(0, 2, 4); LH(0, 2); MM(0, 3, 4); LB(0, 4); MM(0, 4, 2); LB(0, 2); MM(0, 3, 3); LH(0, 3); MM(0, 4, 3); LB(0, 3); LH(0, 4); }
    if constexpr (MASK == 0x3c) { MM(0, 0, 5); LB(0, 5); MM(0, 0, 4); MM(0, 0, 3); MM(0, 1, 4); LB(0, 4); MM(0, 1, 3); MM(0, 0, 2); LH(0, 0); MM(0, 1, 2); MM(0, 2, 3); LB(0, 3); MM(0, 1, 1); LH(0, 1); MM(0, 2, 2); MM(0, 3, 2); LB(0, 2); MM(0, 2, 1); MM(0, 2, 0); LH(0, 2); MM(0, 3, 1); MM(0, 4, 1); LB(0, 1); MM(0, 3, 0); LH(0, 3); MM(0, 4, 0); LH(0, 4); MM(0, 5, 0); LB(0, 0); LH(0, 5); }
    if constexpr (MASK == 0xf8) { MM(0, 7, 0); LH(0, 7); MM(0, 6, 0); MM(0, 5, 0); MM(0, 5, 1); MM(0, 6, 1); LH(0, 6); MM(0, 4, 1); MM(0, 4, 0); MM(0, 3, 0); LB(0, 0); MM(0, 3, 1); MM(0, 5, 2); LH(0, 5); MM(0, 3, 2); MM(0, 4, 2); MM(0, 2, 1); LB(0, 1); MM(0, 4, 3); LH(0, 4); MM(0, 2, 2); MM(0, 3, 3); MM(0, 3, 4); LH(0, 3); MM(0, 2, 4); MM(0, 1, 2); LB(0, 2); MM(0, 2, 3); MM(0, 2, 5); LH(0, 2); MM(0, 1, 3); MM(0, 0, 3); LB(0, 3); MM(0, 0, 4); MM(0, 1, 4); LB(0, 4); MM(0, 1, 5); MM(0, 1, 6); LH(0, 1); MM(0, 0, 5); LB(0, 5); MM(0, 0, 6); LB(0, 6); MM(0, 0, 7); LB(0, 7); LH(0, 0); }
    if constexpr (MASK == 0x7) { MM(0, 0, 2); LB(0, 2); MM(0, 0, 0); MM(0, 0, 1); LH(0, 0); MM(0, 2, 0); LH(0, 2); MM(0, 1, 0); LB(0, 0); MM(0, 1, 1); LB(0, 1); LH(0, 1); MM(2, 0, 2); LB(2, 2); MM(2, 0, 1); MM(2, 0, 0); LH(2, 0); MM(2, 1, 1); LB(2, 1); MM(2, 1, 0); LH(2, 1); MM(2, 2, 0); LB(2, 0); LH(2, 2); MM(1, 2, 0); LH(1, 2); MM(1, 1, 0); MM(1, 0, 0); LB(1, 0); MM(1, 1, 1); LH(1, 1); MM(1, 0, 1); LB(1, 1); MM(1, 0, 2); LB(1, 2); LH(1, 0); }
  }
  parka(8 - CVW);
  TS(23);
  if (CVP == 1 || (CVP == 3 && (MASK == CM3 || MASK == (0xff ^ CM3)))) __builtin_amdgcn_s_setprio(3);
  const unsigned aK = xo + (2 * ((15 - i) >> 2) + 1) * 128 + g * 32 + ((15 - i) & 3) * 4;
  auto ry = RSRC(ws + WS_Y2T + (size_t)(cd + 32 * q0) * 8192, (NC * 32 - 31) * 8192);
  const int yo = g * 256 + (i & 1) * 128 + (i >> 1) * 16;
  unsigned x0[NC][8], x1[NC][8];
#pragma unroll
  for (int J = 0; J < 8 * NC; ++J)
    if (MASK >> (J & 7) & 1) {
      const unsigned a = aK + xq(J >> 3) + (J & 7) * 1024;
      x0[J >> 3][J & 7] = *(const LAS unsigned*)(uintptr_t)a; x1[J >> 3][J & 7] = *(const LAS unsigned*)(uintptr_t)(a + 16);
    }
  FENCE();
  lds_add1(lb + FL_CCNT);
  auto park = [&]() AINL {
    lds_wait1(lb + FL_CCNT, 8u);
    TS(25);
#pragma unroll
    for (int i = 0; i < 18; ++i) *(LAS u32x4*)(smem + D_PB + lw * CSL + i * 1024 + lane * 16) = pb[i];
    FENCE();
    lds_add1(lb + FL_PB + lw * 4);
    TS(13);
  };
  constexpr bool yo_ = YO == 1 || (YO == 2 && (MASK == CM3 || MASK == (0xff ^ CM3)));
  if (yo_) park(); else asm volatile("" : "+v"(pb[17]));
#pragma unroll
  for (int q = 0; q < NC; ++q) {
    const float dd = lo_f(__builtin_amdgcn_readlane(ddv, q0 + q));
#pragma unroll
    for (int J = 0; J < 8; ++J) {
      if (MASK >> J & 1) {
        const unsigned d0 = pk_bf16(hi_f(x0[q][J]) * dd + acc[q][J][0], lo_f(x0[q][J]) * dd + acc[q][J][1]), d1 = pk_bf16(hi_f(x1[q][J]) * dd + acc[q][J][2], lo_f(x1[q][J]) * dd + acc[q][J][3]);
        const unsigned rv = __builtin_amdgcn_update_dpp(0u, (i & 1) ? d0 : d1, 0xb1, 0xf, 0xf, true);
        const u32x4 o = (i & 1) ? (u32x4){rv, ep, d1, ep} : (u32x4){d0, ep, rv, ep};
        st128(o, ry, yo, q * 32 * 8192 + J * 1024);
      }
    }
  }
  TS(24);
  if (!yo_) park();
  if (CVP) __builtin_amdgcn_s_setprio(0);
}

struct G1 {
  __amdgpu_buffer_rsrc_t rx, rwi;
  int xz[4], xn[4], vb[4], vb8, mx, mw, m8, rot;
  int st0;
  __device__ __forceinline__ G1(char* smem, gp16& px_, gp16& pwi_, int tm, int tn, int lw, int h) {
    const int lane = lane_id(), lr = lane >> 3, sl = lane & 7;
    const int sw0 = (sl ^ (lr >> 1)) * 16;
    const int xz0 = (8 * (lr >> 2) + (lr & 3)) * (D * 2) + sw0, xn0 = lr * (D * 2) + sw0;
    const int vb0 = (32 * (lw & 1) + 8 * (lw >> 1) + (lane >> 4)) * (F * 2) + (((lane & 15) ^ ((lane >> 4) << 1)) * 16);
#pragma unroll
    for (int e = 0; e < 4; ++e) {
      vb[e] = vb0 + (16 * (e >> 1) + 4 * (e & 1)) * (F * 2) - e * 1024;
      xz[e] = (xz0 ^ ((e & 1) * 64)) + (16 * (e & 1) + 4 * (e >> 1)) * (D * 2) - e * 1024;
      xn[e] = (xn0 ^ ((e & 1) * 64)) + 8 * e * (D * 2) - e * 1024;
    }
    vb8 = (lw * 16 + ((lane >> 3) & 1) * 8 + ((lane >> 4) & 1) * 4 + ((lane >> 1) & 3)) * (F * 2) + (512 - 112 * (tn & 3)) * 2 + (lane & 1) * 16;
    const int lb = (int)(uintptr_t)(LAS char*)smem;
    mx = lb + RING + lw * 4096; mw = mx + 16384 + (lw >> 1) * 128; m8 = lb + RING + 32896 + lw * 512;
    rot = 2 * (tm & 3) + (tn & 7); rot = rot >= 9 ? rot - 9 : rot;
    st0 = stage(h);
    unsigned ox = (tm * 128 + lw * 32) * (D * 2), ow = (tn >> 2) * 1152 + (tn & 3) * 256;
    asm volatile("" : "+v"(vb[0]), "+v"(vb[1]), "+v"(vb[2]), "+v"(vb[3]), "+v"(xz[0]), "+v"(xz[1]), "+v"(xz[2]), "+v"(xz[3]), "+v"(vb8), "+s"(mx), "+s"(mw), "+s"(m8), "+s"(st0), "+s"(ox), "+s"(ow));
    FENCE();
    asm volatile("" : "+s"(px_), "+s"(pwi_));
    rx = RSRC((const GAS char*)px_ + ox, 32 * D * 2);
    rwi = RSRC((const GAS char*)pwi_ + ow, D * F * 2 - ow);
  }
  __device__ __forceinline__ int stage(int st_) const { return opq(rot) + st_ >= 9 ? rot + st_ - 9 : rot + st_; }
  template <int SO, int I> __device__ __forceinline__ void piece(const bool z2, int st, int so_) const {
    const int sw = st * 64 * (F * 2) + (z2 ? FH * 2 : 0);
    if (I < 8) asm volatile("s_add_i32 m0, %0, %1\n\ts_nop 0\n\tbuffer_load_dwordx4 %4, %2, %3 offen offset:%5 lds"
      :: "s"((I < 4 ? mw : mx) + so_), "n"(SO), "s"(I < 4 ? rwi : rx), "s"(I < 4 ? sw : st * XST), "v"(I < 4 ? vb[I & 3] : (z2 ? xz[I & 3] : xn[I & 3])), "n"((I & 3) * 1024) : "memory", "scc", "m0");
    else asm volatile("s_add_i32 m0, %0, %1\n\ts_mov_b32 exec_hi, 0\n\tbuffer_load_dwordx4 %4, %2, %3 offen lds\n\ts_mov_b32 exec_hi, -1"
      :: "s"(m8 + so_), "n"(SO), "s"(rwi), "s"((NO8 & (z2 ? 1 : 2)) ? 0x40000000 : sw), "v"(vb8) : "memory", "scc", "m0");
  }
  template <int SO, int I = 0> __device__ __forceinline__ void issue(const bool z2, int st, int so_) const {
    if constexpr (I < 9) { piece<SO, I>(z2, st, so_); issue<SO, I + 1>(z2, st, so_); }
  }
};

template <int V>
__device__ __forceinline__ void loader(char* smem, const G1& g1, char* ws, int b, int tm, int tn, int lw, unsigned long long t00) {
  const int lane = lane_id(), lr = lane >> 3, sl = lane & 7;
  TS_DECL(24); TS(0); TQ_DECL;
  TS(16);
  const int sw0 = (sl ^ (lr >> 1)) * 16, lwo = RING + lw * 4096, t3 = tm * 2 + DTH, j = DJ, j8 = tn & 7, jr_ = ROTC;
  auto rsp = RSRC(A->w_sp + (size_t)(tm * 128 + lw * 32) * S, 32 * S * 2);
  auto rzn = RSRC(ws + WS_ZN + (size_t)tn * 32 * CSL, 32 * CSL);
  const int sa0 = lr * (S * 2) + sw0, sa1 = sa0 ^ 64, vo = lane * 16, vz = vo + (lw >> 1) * CSL + (lw & 1) * 9216 + 8192;
  char* lp8 = smem + RING + 16384 + (lw >> 1) * CSL + (lw & 1) * 1024 + vo;
  u32x4 z8[5];
  auto spo = [&](int c, int e) AINL { return e * 8 * (S * 2) + ((opq(jr_) * 512 + c * 128) & 4095); };
  auto dmasp = [&](int c, int e) AINL { GLDS(rsp, opq(lwo) + ((c + 4) & 7) * CSL + e * 1024, (e & 1) ? sa1 : sa0, spo(c, e)); };
  auto wline = [&](int row, int st) AINL { return row * (S * 2) + ((jr_ * 512 + st * 128) & 4095); };
  int wv[4];
  auto wpre = [&]() AINL {
    const int q = j8 - 1 - (j8 >> 2);
    wv[0] = lane < 32 ? wline(j8 * 4 + lr, sl) : 0x7ffffff0; wv[1] = lane < 32 ? wline(j8 * 4 + lr, 8 + sl) : 0x7ffffff0;
    wv[2] = wline(lane >> 1, 16 + 3 * q + (lane & 1)); wv[3] = lane < 32 ? wline(lane, 18 + 3 * q) : 0x7ffffff0;
    asm volatile("" : "+v"(wv[0]), "+v"(wv[1]), "+v"(wv[2]), "+v"(wv[3]));
  };
  auto wtouch = [&](int q, int n) AINL { TOUCH(rsp, wv[0], (unsigned)(b >> 3) % n == q ? 0 : 0x40000000, TSA); };
  auto wcoop = [&](int q, int n) AINL { TOUCH(rsp, wv[1], (unsigned)(b >> 3) % n == q ? 0 : 0x40000000, TDA); };
  const GAS char* psp = (const GAS char*)(A->w_sp + (size_t)(tm * 128 + lw * 32 + j8 * 4) * S);
  unsigned sdm = 0;
  auto stouch = [&](int i0, int i1) AINL {
#pragma unroll
    for (int q = i0 >> 4; q <= (i1 - 1) >> 4; ++q) {
      const GAS char* p = psp + (((opq(jr_) + q) * 512) & 4095);
#pragma unroll
      for (int i = 0; i < 16; ++i)
        if (16 * q + i >= i0 && 16 * q + i < i1) asm volatile("s_load_dword %0, %1, %2" : "+s"(sdm) : "s"(p), "n"((i >> 2) * 4096 + (i & 3) * 128 + SOF) : "memory");
    }
  };
  auto sdone = [&]() AINL { asm volatile("s_waitcnt lgkmcnt(0)" : "+s"(sdm) :: "memory"); };
  auto wdose = [&](int q) AINL { if (j8 == q + 1 + q / 3) { TOUCH(rsp, wv[2], 0, TDA); TOUCH(rsp, wv[3], 0, TDA); } };
  auto ctouch = [&]() AINL {
    if ((j8 & 3) == 0) {
      u32x2 pc; int sz;
      asm volatile("s_getpc_b64 %0\n.Lk_c%=:\n\ts_mov_b32 %1, .Lk_c%=-.Lk_top" : "=s"(pc), "=s"(sz));
      const unsigned long long p = (((unsigned long long)pc[1] << 32) | pc[0]) - (unsigned)sz;
      asm volatile("s_mov_b32 %0, .Lk_end0-.Lk_top\n\ts_max_i32 %0, %0, .Lk_end10-.Lk_top\n\ts_max_i32 %0, %0, .Lk_end11-.Lk_top" : "=s"(sz) :: "scc");
      PREFETCH(RSRC(p, sz), lane < 32 ? lane * 128 : 0x7ffffff0, (((tm & 3) * 2 + (j8 >> 2)) * 4 + lw) * 4096);
    }
  };
  auto tlb = [&]() AINL {
    if (V == 1) {
      constexpr int CH = 64 * TLS, NW = ((int)WS_E + CH - 1) / CH, NS = (S * S * 2 + CH - 1) / CH, NO = (FH * D * 2 + CH - 1) / CH;
      static_assert(NW + NS + 5 * NO <= 32, "");
      const int i = b >> 3, q = (i - NW - NS) / NO;
      kap_t a = A;
      const GAS char* p = i < NW ? (const GAS char*)a->ws : (i < NW + NS ? (const GAS char*)a->w_sp : (q == 0 ? (const GAS char*)a->k : (q == 1 ? (const GAS char*)a->v : (q == 2 ? (const GAS char*)a->ssm_k : (q == 3 ? (const GAS char*)a->w_out : (const GAS char*)a->out)))));
      const int n = i < NW ? (int)WS_E : (i < NW + NS ? S * S * 2 : (q == 3 ? FH * D * 2 : (q < 5 ? S * D * 2 : 0)));
      const int c = i < NW ? i : (i < NW + NS ? i - NW : i - NW - NS - q * NO);
      TOUCH(RSRC(p, n), c * CH + lane * TLS, 0, 0);
    }
  };
  auto w9 = [&]() AINL {
    const int s9 = (tn & 7) ? (tn & 7) - 1 : 8, k = lane & 3;
    TOUCH(g1.rwi, k < 3 && lane < 16 ? ((tm & 3) * 16 + lw * 4 + (lane >> 2)) * (F * 2) + (k < 2 ? k * 128 : (512 - 112 * (tn & 3)) * 2) : 0x7ffffff0, s9 * 64 * (F * 2) + FH * 2, 0);
  };
  auto ld8 = [&](int g) AINL { return __builtin_amdgcn_raw_buffer_load_b128(rzn, vz, ((opq(jr_) * 2 + g) & 15) * (2 * CSL), 16); };
  auto zt = [&](int first, int n) AINL {
    int o = (opq(jr_) * 4 + first) * CSL + (((tm & 3) * 4 + lw) * n + lane) * 128;
    o = o >= 32 * CSL ? o - 32 * CSL : o;
    TOUCH(rzn, lane < n ? o : 0x7ffffff0, 0, 16);
  };
  auto put8 = [&](int g, u32x4 d) AINL { *(LAS u32x4*)(lp8 + ((g + 2) & 3) * (2 * CSL)) = d; };
  unsigned ep, f3, braw;
  u32x4 g0, g2[8], f1;
  auto g0l = [&]() AINL { g0 = __builtin_amdgcn_raw_buffer_load_b128(RSRC(ws + WS_G0 + (size_t)(tm * 128 + lw * 32) * 32, 1024), vo, 0, 16); };
  auto bld = [&]() AINL { asm volatile("buffer_load_ushort %0, %1, %2, 0 offen" : "=v"(braw) : "v"((lane & 31) * 2), "s"(RSRC(A->b_sp + tm * 128 + lw * 32, 64)) : "memory"); };
  auto g0a = [&]() AINL { asm volatile("buffer_load_dwordx4 %0, %1, %2, 0 offen sc1" : "=v"(g0) : "v"(vo), "s"(RSRC(ws + WS_G0 + (size_t)(tm * 128 + lw * 32) * 32, 1024)) : "memory"); };
  int st;
  unsigned ga[4];
  auto gpre = [&]() AINL {
    const unsigned lb = (unsigned)(uintptr_t)(LAS char*)smem;
    ga[0] = lb + T_ROW + lw * 512 + lane * 8; ga[1] = lb + T_RS + (lane & 1) * 1024 + lw * 128 + (lane >> 1) * 4;
    ga[2] = lb + T_B4 + lw * 512 + (lane & 31) * 16 + (lane >> 5) * 8; ga[3] = lb + T_NM + (lw * 16 + ((lane & 31) >> 1)) * 16 + (lane >> 5) * 8 + (lane & 1) * 4;
    asm volatile("" : "+v"(ga[0]), "+v"(ga[1]), "+v"(ga[2]), "+v"(ga[3]));
  };
  auto g0s = [&](int k) AINL {
    if (k == 0) {
      asm volatile("" : "+v"(g0));
      if (__builtin_expect(!all_lanes(g0[1] == ep && g0[3] == ep), 0))
        _Pragma("unroll 1") for (int spin = 0; spin < SPIN_G; ++spin) { g0l(); if (all_lanes(g0[1] == ep && g0[3] == ep)) break; __builtin_amdgcn_s_sleep(4); }
    } else if (k == 1) asm volatile("ds_write2_b32 %0, %2, %3 offset1:1\n\tds_write2st64_b32 %1, %2, %3 offset1:2" :: "v"(ga[0]), "v"(ga[1]), "v"(g0[0]), "v"(g0[2]) : "memory");
    else asm volatile("v_lshlrev_b32 %0, 16, %0\n\tds_write2_b32 %1, %0, %0 offset1:1" : "+v"(braw) : "v"(ga[2]) : "memory");
    FENCE();
  };
  auto g2ld = [&]() AINL {
    auto rg2 = RSRC(ws + WS_G2 + (size_t)tm * 16 * 2048 + lw * 512, 16 * 2048);
#pragma unroll
    for (int i = 0; i < 8; ++i) g2[i] = __builtin_amdgcn_raw_buffer_load_b128(rg2, (lane >> 5) * 8 * 2048 + (lane & 31) * 16, i * 2048, 16);
  };
  auto g2a = [&]() AINL {
    auto rg2 = RSRC(ws + WS_G2 + (size_t)tm * 16 * 2048 + lw * 512, 16 * 2048);
    const int o = (lane >> 5) * 8 * 2048 + (lane & 31) * 16;
#pragma unroll
    for (int i = 0; i < 8; i += 2)
      asm volatile("buffer_load_dwordx4 %0, %2, %3, %4 offen sc1\n\tbuffer_load_dwordx4 %1, %2, %3, %4 offen offset:2048 sc1" : "=&v"(g2[i]), "=&v"(g2[i + 1]) : "v"(o), "s"(rg2), "s"(i * 2048) : "memory");
  };
  unsigned tg, th;
  float gs, gq;
  auto g2s = [&](int k) AINL {
    auto acc = [&](int i) AINL {
      tg = __builtin_amdgcn_bitop3_b32(i ? tg : 0u, g2[i][1], ep, 0xf6); th = __builtin_amdgcn_bitop3_b32(i ? th : 0u, g2[i][3], ep, 0xf6);
      gs = i ? gs + __uint_as_float(g2[i][0]) : __uint_as_float(g2[i][0]); gq = i ? gq + __uint_as_float(g2[i][2]) : __uint_as_float(g2[i][2]);
    };
    if (k == 0) asm volatile("" : "+v"(g2[0]), "+v"(g2[1]), "+v"(g2[2]), "+v"(g2[3]), "+v"(g2[4]), "+v"(g2[5]), "+v"(g2[6]), "+v"(g2[7]));
    if (k < 8) { acc(k); asm volatile("" : "+v"(tg), "+v"(th)); }
    if (k == 8 && __builtin_expect(!all_lanes((tg | th) == 0u), 0))
      _Pragma("unroll 1") for (int spin = 0; spin < SPIN_G; ++spin) {
        g2ld();
#pragma unroll
        for (int i = 0; i < 8; ++i) acc(i);
        if (all_lanes((tg | th) == 0u)) break;
        __builtin_amdgcn_s_sleep(1);
      }
    if (k == 9) { gs = xsum32(gs); gq = xsum32(gq); }
    if (k == 10) { gs *= 1.0f / FH; gq = fmaxf(gq * (1.0f / FH) - gs * gs, 0.f); }
    if (k == 11) { gq = rbf(gq) + 1e-5f; gs = rbf(gs); }
    if (k == 12) gq = __builtin_amdgcn_sqrtf(rbf(gq));
    if (k == 13) gq = __builtin_amdgcn_rcpf(rbf(gq));
    if (k < 14) asm volatile("" : "+v"(gs), "+v"(gq));
    if (k == 14) *(LAS float*)(uintptr_t)ga[3] = lane < 32 ? gq : -gs * gq;
    FENCE();
  };
  u32x4 zq[9];
  u32x2 zh[18];
  f32x4 nc[4];
  auto zrd = [&]() AINL {
    const u32x3 ha = z2adr((unsigned)(uintptr_t)(LAS char*)smem, lw, HO == 3 ? 4 : 16);
#pragma unroll
    for (int n = 0; n < 9; ++n) {
      if (HO == 3) {
        u32x2 x, y;
        asm volatile("ds_read2st64_b32 %0, %2 offset0:%3 offset1:%4\n\tds_read2st64_b32 %1, %2 offset0:%5 offset1:%6" : "=&v"(x), "=&v"(y) : "v"(ha[n >> 2]), "n"((n & 3) * 4), "n"((n & 3) * 4 + 1), "n"((n & 3) * 4 + 2), "n"((n & 3) * 4 + 3) : "memory");
        zh[2 * n] = x; zh[2 * n + 1] = y;
      } else asm volatile("ds_read_b128 %0, %1 offset:%2" : "=v"(zq[n]) : "v"(ha[n >> 2]), "n"((n & 3) * 1024) : "memory");
    }
    if (HO == 3) {
#pragma unroll
      for (int n = 0; n < 18; n += 6) asm volatile("s_waitcnt lgkmcnt(0)" : "+v"(zh[n]), "+v"(zh[n + 1]), "+v"(zh[n + 2]), "+v"(zh[n + 3]), "+v"(zh[n + 4]), "+v"(zh[n + 5]) :: "memory");
#pragma unroll
      for (int n = 0; n < 9; ++n) zq[n] = (u32x4){zh[2 * n][0], zh[2 * n][1], zh[2 * n + 1][0], zh[2 * n + 1][1]};
    }
    asm volatile("s_waitcnt lgkmcnt(0)" : "+v"(zq[0]), "+v"(zq[1]), "+v"(zq[2]), "+v"(zq[3]), "+v"(zq[4]), "+v"(zq[5]), "+v"(zq[6]), "+v"(zq[7]), "+v"(zq[8]) :: "memory");
  };
  auto zgl = [&]() AINL {
    auto r = RSRC(ws + WS_ZR + (size_t)((tn * 32 + tm * 2) * 2 + lw) * 9216, 9216);
#pragma unroll
    for (int n = 0; n < 9; ++n) asm volatile("buffer_load_dwordx4 %0, %1, %2, %3 offen sc1" : "=v"(zq[n]) : "v"(vo), "s"(r), "s"(n < 8 ? (n ^ lw) * 1024 : 8192) : "memory");
  };
  auto ncr = [&](int k) AINL {
    const unsigned a = (unsigned)(uintptr_t)(LAS char*)smem + T_NM + (lw * 32 + (lane >> 4) * 8) * 8;
    if (k == 0) asm volatile("ds_read_b128 %0, %4\n\tds_read_b128 %1, %4 offset:16\n\tds_read_b128 %2, %4 offset:32\n\tds_read_b128 %3, %4 offset:48" : "=&v"(nc[0]), "=&v"(nc[1]), "=&v"(nc[2]), "=&v"(nc[3]) : "v"(a) : "memory");
    else asm volatile("s_waitcnt lgkmcnt(0)" : "+v"(nc[0]), "+v"(nc[1]), "+v"(nc[2]), "+v"(nc[3]), "+v"(zq[0]), "+v"(zq[1]), "+v"(zq[2]), "+v"(zq[3]), "+v"(zq[4]), "+v"(zq[5]), "+v"(zq[6]), "+v"(zq[7]), "+v"(zq[8]) :: "memory");
    FENCE();
  };
  auto nrm = [&](int n, int h) AINL {
    float t[4];
#pragma unroll
    for (int i = 0; i < 4; ++i) t[i] = unp(zq[n][2 * h + (i >> 1)], i & 1);
#pragma unroll
    for (int i = 0; i < 4; ++i) t[i] = vfma(t[i], nc[2 * h + (i >> 1)][i & 1], nc[2 * h + (i >> 1)][2 + (i & 1)]);
    zq[n][2 * h] = pk_bf16(t[0], t[1]); zq[n][2 * h + 1] = pk_bf16(t[2], t[3]);
    asm volatile("" : "+v"(zq[n]));
    if (h) st128(zq[n], RSRC(ws + WS_ZN + (size_t)((tn * 32 + tm * 2) * 2 + lw) * 9216, 9216), vo, n < 8 ? (n ^ lw) * 1024 : 8192);
    FENCE();
  };
  auto nfl = [&]() AINL { __builtin_amdgcn_raw_buffer_store_b32(ep, RSRC(ws + WS_F3 + tn * 4096 + tm * 16 + lw * 4, 4096), lane < 16 ? lane * 256 : 0x7ffffff0, 0, 16); };
  char* f3p = ws + WS_F3 + (tn * 16 + tm) * 256 + lane * 4;
  const unsigned lb = (unsigned)(uintptr_t)(LAS char*)smem, pra = lb + FL_CP + (lane & 3) * 16 + lw * 4;
  unsigned prv;
  auto getep = [&]() AINL { unsigned t; asm volatile("ds_read_b32 %0, %1\n\ts_waitcnt lgkmcnt(0)" : "=v"(t) : "v"(lb + FL_EP) : "memory"); ep = __builtin_amdgcn_readfirstlane(t); };
  auto f3chk = [&]() AINL { asm volatile("" : "+v"(f3)); if (!all_lanes(f3 == ep)) poll_flags(f3p, ep); asm volatile("" ::: "memory"); };
  auto f1ld = [&]() AINL { f1 = __builtin_amdgcn_raw_buffer_load_b128(RSRC(ws + WS_F1, 1024), vo, 0, 16); };
  auto f1chk = [&]() AINL {
    asm volatile("" : "+v"(f1));
    if (!all_lanes(f1[0] == ep && f1[1] == ep && f1[2] == ep && f1[3] == ep))
      _Pragma("unroll 1") for (int spin = 0; spin < SPIN_G; ++spin) { f1ld(); if (all_lanes(f1[0] == ep && f1[1] == ep && f1[2] == ep && f1[3] == ep)) break; __builtin_amdgcn_s_sleep(4); }
  };
  const int js0 = ((b & 7) * 4 + (b >> 6)) * 64, jd0 = ((b >> 3) & 7) * 72;
  u32x2 tw[3];
  gp16 pjk, pjv, pjw;
  int jo[3], jvs[3], jso[3];
  auto tld = [&]() AINL {
    if (V == 0) {
      auto rt = RSRC(A->shift_k + (jd0 + lw * 24) * 4, 192);
#pragma unroll
      for (int i = 0; i < 3; ++i) tw[i] = __builtin_amdgcn_raw_buffer_load_b64(rt, (lane & 7) * 8 + i * 64, 0, 0);
    }
  };
  auto jpre = [&]() AINL {
    const int rl = (lane * 57) >> 9;
    jo[0] = (js0 + 63 - rl - 7 * lw) * (D * 2) + jd0 * 2 + (lane - 9 * rl) * 16;
    jo[1] = jo[0] - 28 * D * 2; jo[2] = lw < 2 ? jo[0] - 56 * D * 2 : 0x7ffffff0;
    { kap_t a = A; pjk = a->k; pjv = a->v; pjw = a->w_out; }
#pragma unroll
    for (int h = 0; h < 3; ++h) {
      const int u = b + 256 * h, ks = u / 9, cb = u - ks * 9, w = ks / 9, sq = ks - w * 9;
      const int r0 = 576 * (w >> 1) + (sq < 8 ? 256 * (w & 1) + 32 * sq : 512 + 32 * (w & 1));
      jvs[h] = u < 648 ? ((lr & 3) + (lr >> 2) * (sq < 8 ? 16 : 4)) * (D * 2) + (sl ^ ((lr & 3) << 1)) * 16 : 0x7ffffff0;
      jso[h] = u < 648 ? (r0 + (sq < 8 ? 4 : 8) * lw) * (D * 2) + cb * 128 : 0;
      asm volatile("" : "+v"(jvs[h]), "+v"(jo[h]));
    }
    asm volatile("" : "+s"(jso[0]), "+s"(jso[1]), "+s"(jso[2]), "+s"(pjk), "+s"(pjv), "+s"(pjw));
  };
  u32x4 jq[9];
  auto jld = [&](int i0, int i1) AINL {
#pragma unroll
    for (int i = i0; i < i1; ++i) {
      if (i < 3) jq[i] = __builtin_amdgcn_raw_buffer_load_b128(RSRC(pjw, FH * D * 2), jvs[i], jso[i], NTA);
      else if (i < 7 || V == 0) jq[i] = __builtin_amdgcn_raw_buffer_load_b128(RSRC(i & 1 ? pjk : pjv, S * D * 2), jo[(i - 3) >> 1], 0, NTA);
    }
  };
#define TRD(d, a, o) asm volatile("ds_read_b64_tr_b16 %0, %1 offset:%2" : "=v"(d) : "v"(a), "n"(o) : "memory")
#define WRT(a, d, o) asm volatile("ds_write_b128 %0, %1 offset:%2" :: "v"(a), "v"(d), "n"(o) : "memory")
  u32x2 jr[3][8];
  auto jrd = [&](int ph) AINL {
    const int i = lane & 15, g = lane >> 4;
    if (ph != 2) {
      const unsigned a = lb + J_W + lw * 1024 + vo;
      WRT(a, jq[0], 0); WRT(a, jq[1], 4096); WRT(a, jq[2], 8192);
    }
    if (ph != 2 && lane < 63) {
      const unsigned a = lb + J_K + lw * 1008 + vo;
      WRT(a, jq[3], 0); WRT(a, jq[4], J_V - J_K); WRT(a, jq[5], 4032); WRT(a, jq[6], J_V - J_K + 4032);
      if (V == 0) { WRT(a, jq[7], 8064); WRT(a, jq[8], J_V - J_K + 8064); }
    }
    if (ph == 1) return;
    if (ph == 0) { lds_add1(lb + FL_JCNT); lds_wait1(lb + FL_JCNT, 4u); }
    if (V == 0) {
      const unsigned a = lb + J_K + (16 * g + 8 * ((i >> 1) & 1) + (i >> 2)) * 144 + (i & 1) * 8 + lw * 48;
#pragma unroll
      for (int h = 0; h < 3; ++h) {
        TRD(jr[h][0], a, h * 16); TRD(jr[h][1], a, h * 16 + 576); TRD(jr[h][2], a, h * 16 + 1152); TRD(jr[h][3], a, h * 16 + (J_V - J_K)); TRD(jr[h][4], a, h * 16 + (J_V - J_K) + 576);
      }
      if (ph == 0) lds_add1(lb + FL_JCNT);
#pragma unroll
      for (int h = 0; h < 3; ++h) asm volatile("s_waitcnt lgkmcnt(0)" : "+v"(jr[h][0]), "+v"(jr[h][1]), "+v"(jr[h][2]), "+v"(jr[h][3]), "+v"(jr[h][4]));
    } else {
      const unsigned a = lb + J_W + (g * 8 + (i >> 2)) * 128 + (lane & 3) * 8;
#pragma unroll
      for (int n = 0; n < 4; ++n) {
        const unsigned an = a + ((n ^ (i >> 2)) * 32);
#pragma unroll
        for (int h = 0; h < 3; ++h) { TRD(jr[h][2 * n], an, h * 4096); TRD(jr[h][2 * n + 1], an, h * 4096 + 512); }
      }
      if (ph == 0) lds_add1(lb + FL_JCNT);
#pragma unroll
      for (int h = 0; h < 3; ++h) asm volatile("s_waitcnt lgkmcnt(0)" : "+v"(jr[h][0]), "+v"(jr[h][1]), "+v"(jr[h][2]), "+v"(jr[h][3]), "+v"(jr[h][4]), "+v"(jr[h][5]), "+v"(jr[h][6]), "+v"(jr[h][7]));
    }
    if (ph == 0) lds_wait1(lb + FL_JCNT, 8u);
  };
  u32x4 jo_[3];
  auto jcp = [&](int h, int q) AINL {
    if (V == 0) {
      const unsigned kd[6] = {jr[h][0][0], jr[h][0][1], jr[h][1][0], jr[h][1][1], jr[h][2][0], jr[h][2][1]}, vd[4] = {jr[h][3][0], jr[h][3][1], jr[h][4][0], jr[h][4][1]};
      const unsigned wa = tw[h][0] << 16, wb = __builtin_amdgcn_alignbit(tw[h][1], tw[h][0], 16), wc = tw[h][1] >> 16;
      auto dot = [&](unsigned x, unsigned y, float c) AINL { return __builtin_amdgcn_fdot2_f32_bf16(__builtin_bit_cast(bf2_t, x), __builtin_bit_cast(bf2_t, y), c, false); };
      unsigned o = pk_bf16(dot(kd[q], tw[h][0], dot(kd[q + 1], tw[h][1], 0.f)) * lo_f(vd[q]), dot(kd[q], wa, dot(kd[q + 1], wb, dot(kd[q + 2], wc, 0.f))) * hi_f(vd[q]));
      asm volatile("" : "+v"(o));
      jo_[h][q] = o;
      if (q == 3) {
        const unsigned sw = lb + T_X9 + lw * 1152 + (lane & 7) * 144 + (lane >> 3) * 16, sr = lb + T_X9 + lw * 1152 + lr * 144 + sl * 16;
        asm volatile("ds_write_b128 %1, %0\n\tds_read_b128 %0, %2\n\ts_waitcnt lgkmcnt(0)" : "+v"(jo_[h]) : "v"(sw), "v"(sr) : "memory");
      }
    }
  };
  auto jst = [&](int h) AINL {
    if (V == 0) st128(jo_[h], RSRC(ws + WS_KVR + (size_t)(jd0 + lw * 24) * 4096 + (1984 - js0) * 2, 24 * 4096), lr * 4096 + sl * 16, h * 32768);
    else {
      const int u = b + 256 * h;
#pragma unroll
      for (int n = 0; n < 4; ++n) st128((u32x4){jr[h][2 * n][0], jr[h][2 * n][1], jr[h][2 * n + 1][0], jr[h][2 * n + 1][1]}, RSRC(ws + WS_WO + (size_t)u * 4096, u < 648 ? 4096 : 0), vo, n * 1024);
    }
  };
  const int cd = (b >> 5) * 72 + (b & 31);
  const bool c3 = (b & 31) < 8, cv = lw < 2 || (lw == 2 && c3);
  const int cho = cv_h(lw), cxo = cv_x(lw);
  unsigned ddv;
  u32x4 kx[4], hx[4], pb[18], pa[18];
  unsigned kn[4];
  u32x2 x0, x1, xh;
  auto cvs = [&]() AINL {
    ddv = __builtin_amdgcn_raw_buffer_load_b16(RSRC(A->ssm_D, D * 2), (cd + 32 * (lane & 3)) * 2, 0, 0);
  };
  auto cvx = [&]() AINL {
    if (V == 0) {
      auto rk = RSRC(ws + WS_KVR + (size_t)(cd + 32 * lw) * 4096, cv ? 4096 : 0);
      const int ko = (60 - 32 * (lane >> 5) - 4 * sl + (lr & 3)) * 16;
#pragma unroll
      for (int n = 0; n < 4; ++n) { kx[n] = __builtin_amdgcn_raw_buffer_load_b128(rk, ko + (3 - n) * 1024, 0, 16); kn[n] = __builtin_amdgcn_raw_buffer_load_b32(rk, ko + (3 - n) * 1024 + 16, 0, 16); }
    }
  };
  auto ldwo = [&](int w, int q, int n) AINL {
    return __builtin_amdgcn_raw_buffer_load_b128(RSRC(ws + WS_WO + (size_t)w * 18 * 36864 + (j >> 1) * 9216, 18 * 36864),
      n < 4 ? vo + n * 1024 : (lane >> 5) * 36864 + 4096 + (((lane >> 3) & 3) * 16 + (j & 1) * 8 + sl) * 16, d_step(d_rot(tm, tn), q) * 36864 + (n < 4 ? (j & 1) * 5120 : 0), 16);
  };
  auto ldpb = [&](int q0, int i) AINL { return i < 16 ? ldwo(lw, q0 + (i >> 2), i & 3) : ldwo(lw, q0 + 2 * (i - 16), 4); };
  auto parka = [&](int k) AINL {
    if (PL == 2) {
      if (k >= 0 && k < 5) {
        const int l = lane_id(), o = k < 4 ? l * 16 : (l >> 5) * 36864 + 4096 + (((l >> 3) & 3) * 16 + (j & 1) * 8 + (l & 7)) * 16;
        auto r = RSRC(ws + WS_WO + (size_t)lw * 18 * 36864 + (j >> 1) * 9216, 18 * 36864);
        const int so = k < 4 ? (4 + k) * 36864 + (j & 1) * 5120 : 4 * 36864, s1 = k < 4 ? so : 6 * 36864 - 1024;
        asm volatile("s_mov_b32 m0, %0\n\ts_nop 0\n\tbuffer_load_dwordx4 %1, %2, %3 offen sc1 lds\n\tbuffer_load_dwordx4 %1, %2, %4 offen offset:1024 sc1 lds"
          :: "s"(lb + d_p1(lw) + k * 4096), "v"(o), "s"(r), "s"(so), "s"(s1) : "memory", "m0");
        if (k < 4) asm volatile("buffer_load_dwordx4 %0, %1, %2 offen offset:2048 sc1 lds\n\tbuffer_load_dwordx4 %0, %1, %2 offen offset:3072 sc1 lds" :: "v"(o), "s"(r), "s"(so) : "memory");
      }
      if (k == PSG) { wait_vm<0>(); lds_add1(lb + FL_PA + lw * 4); }
    } else if (PL && k == 0) {
      const int l = lane_id();
#pragma unroll
      for (int i = 0; i < 18; ++i) *(LAS u32x4*)(smem + d_p1(lw) + i * 1024 + l * 16) = V ? pb[i] : pa[i];
      FENCE();
      lds_add1(lb + FL_PA + lw * 4);
      if (V) {
#pragma unroll
        for (int i = 0; i < 18; ++i) pb[i] = ldpb(PB0, i);
      }
      FENCE();
    }
  };
  auto ot = [&](int q0) AINL {
    const int l = lane_id();
    TOUCH(RSRC(ws + WS_WO + (size_t)lw * 18 * 36864 + (j >> 1) * 9216, 18 * 36864), l < 32 ? (j & 1) * 5120 + l * 128 : (l < 36 ? 4096 + (l - 32) * 256 + (j & 1) * 128 : 0x7ffffff0), (q0 + (tm & 3)) * 36864, 16);
  };
  auto sot = [&](int q0) AINL {
    const GAS char* p = (const GAS char*)ws + WS_WO + (size_t)((lw * 18 + q0 + (tm & 3)) * 36864 + (j >> 1) * 9216);
    const GAS char* pm = p + (j & 1) * 5120;
    const GAS char* ph = p + 4096 + (j & 1) * 128;
#pragma unroll
    for (int i = 0; i < 36; ++i) {
      asm volatile("s_load_dword %0, %1, %2" : "+s"(sdm) : "s"(i < 32 ? pm : ph), "n"(i < 32 ? i * 128 : (i - 32) * 256) : "memory");
      if (S64) asm volatile("s_load_dword %0, %1, %2" : "+s"(sdm) : "s"(i < 32 ? pm : ph), "n"((i < 32 ? i * 128 : (i - 32) * 256) + 64) : "memory");
    }
  };
  auto hld = [&]() AINL {
    auto rh = RSRC(A->ssm_k + (size_t)(cd + 32 * lw) * S, cv ? S * 2 : 0);
#pragma unroll
    for (int n = 0; n < 4; ++n) hx[n] = __builtin_amdgcn_raw_buffer_load_b128(rh, vo + n * 1024, 0, NTA);
  };
  const int r = lane & 15, g = lane >> 4, mh = (lw & 1) * 2 + (g >> 1), dh = ((j & 1) ? 0 : 64) + 4 * (g & 1);
  const int re = EPI >= 2 ? 2 + (lw & 1) : lw, ce = EPI >= 2 ? 32 * (lw >> 1) : 32;
  auto oo2 = [&]() AINL { const int l = lane_id(); return (t3 * 64 + re * 16 + (l & 15)) * D + j * 72 + ce + 4 * (l >> 4) + (j & 1) * 8; };
  auto ooh = [&]() AINL { const int l = lane_id(); return (t3 * 64 + ((lw & 1) * 2 + (l >> 5)) * 16 + (l & 15)) * D + j * 72 + ((j & 1) ? 0 : 64) + 4 * ((l >> 4) & 1); };
  auto xld = [&]() AINL { gp16 p = A->x; const int o = oo2(); x0 = *(const GAS u32x2*)(p + o); x1 = *(const GAS u32x2*)(p + o + 16); xh = *(const GAS u32x2*)(p + ooh()); };

  if constexpr (V == 0) {
  tlb(); FENCE();
  bld(); tld(); cvs(); FENCE();
  wpre(); FENCE();
  FENCE();
  jpre(); gpre(); FENCE();
  FENCE();
  bar(); FENCE();
  TS(1);
  FENCE();
  st = g1.stage(2);
  g1.piece<73728, 0>(true, st, 0); FENCE();
  g1.piece<73728, 1>(true, st, 0); FENCE();
  g1.piece<73728, 2>(true, st, 0); FENCE();
  g1.piece<73728, 3>(true, st, 0); FENCE();
  g1.piece<73728, 4>(true, st, 0); FENCE();
  g1.piece<73728, 5>(true, st, 0); FENCE();
  g1.piece<73728, 6>(true, st, 0); FENCE();
  g1.piece<73728, 7>(true, st, 0); FENCE();
  g1.piece<73728, 8>(true, st, 0); FENCE();
  FENCE();
  wait_vm<14>(); bar(); FENCE();
  FENCE();
  st = g1.stage(3);
  g1.piece<110592, 0>(true, st, 0); FENCE();
  g1.piece<110592, 1>(true, st, 0); FENCE();
  g1.piece<110592, 2>(true, st, 0); FENCE();
  g1.piece<110592, 3>(true, st, 0); FENCE();
  g1.piece<110592, 4>(true, st, 0); FENCE();
  g1.piece<110592, 5>(true, st, 0); FENCE();
  g1.piece<110592, 6>(true, st, 0); FENCE();
  g1.piece<110592, 7>(true, st, 0); FENCE();
  g1.piece<110592, 8>(true, st, 0); FENCE();
  st = g1.stage(4);
  g1.piece<0, 0>(true, st, 0); FENCE();
  g1.piece<0, 1>(true, st, 0); FENCE();
  g1.piece<0, 2>(true, st, 0); FENCE();
  g1.piece<0, 3>(true, st, 0); FENCE();
  g1.piece<0, 4>(true, st, 0); FENCE();
  g1.piece<0, 5>(true, st, 0); FENCE();
  g1.piece<0, 6>(true, st, 0); FENCE();
  g1.piece<0, 7>(true, st, 0); FENCE();
  g1.piece<0, 8>(true, st, 0); FENCE();
  FENCE();
  wait_vm<18>(); bar(); FENCE();
  FENCE();
  st = g1.stage(5);
  g1.piece<36864, 0>(true, st, 0); FENCE();
  g1.piece<36864, 1>(true, st, 0); FENCE();
  g1.piece<36864, 2>(true, st, 0); FENCE();
  g1.piece<36864, 3>(true, st, 0); FENCE();
  g1.piece<36864, 4>(true, st, 0); FENCE();
  g1.piece<36864, 5>(true, st, 0); FENCE();
  g1.piece<36864, 6>(true, st, 0); FENCE();
  g1.piece<36864, 7>(true, st, 0); FENCE();
  g1.piece<36864, 8>(true, st, 0); FENCE();
  getep();
  wtouch(0, 4); FENCE();
  FENCE();
  wait_vm<19>(); bar(); FENCE();
  FENCE();
  st = g1.stage(6);
  g1.piece<73728, 0>(true, st, 0); FENCE();
  g1.piece<73728, 1>(true, st, 0); FENCE();
  g1.piece<73728, 2>(true, st, 0); FENCE();
  g1.piece<73728, 3>(true, st, 0); FENCE();
  g1.piece<73728, 4>(true, st, 0); FENCE();
  g1.piece<73728, 5>(true, st, 0); FENCE();
  g1.piece<73728, 6>(true, st, 0); FENCE();
  g1.piece<73728, 7>(true, st, 0); FENCE();
  g1.piece<73728, 8>(true, st, 0); FENCE();
  wtouch(1, 4); FENCE();
  FENCE();
  wait_vm<20>(); bar(); FENCE();
  FENCE();
  st = g1.stage(7);
  g1.piece<110592, 0>(true, st, 0); FENCE();
  g1.piece<110592, 1>(true, st, 0); FENCE();
  g1.piece<110592, 2>(true, st, 0); FENCE();
  g1.piece<110592, 3>(true, st, 0); FENCE();
  g1.piece<110592, 4>(true, st, 0); FENCE();
  g1.piece<110592, 5>(true, st, 0); FENCE();
  g1.piece<110592, 6>(true, st, 0); FENCE();
  g1.piece<110592, 7>(true, st, 0); FENCE();
  g1.piece<110592, 8>(true, st, 0); FENCE();
  wtouch(2, 4); FENCE();
  FENCE();
  wait_vm<21>(); bar(); FENCE();
  FENCE();
  st = g1.stage(8);
  g1.piece<0, 0>(true, st, 0); FENCE();
  g1.piece<0, 1>(true, st, 0); FENCE();
  g1.piece<0, 2>(true, st, 0); FENCE();
  g1.piece<0, 3>(true, st, 0); FENCE();
  g1.piece<0, 4>(true, st, 0); FENCE();
  g1.piece<0, 5>(true, st, 0); FENCE();
  g1.piece<0, 6>(true, st, 0); FENCE();
  g1.piece<0, 7>(true, st, 0); FENCE();
  g1.piece<0, 8>(true, st, 0); FENCE();
  wtouch(3, 4); FENCE();
  FENCE();
  wait_vm<21>(); bar(); FENCE();
  FENCE();
  g0a(); FENCE();
  st = g1.stage(0);
  g1.piece<36864, 0>(false, st, 0); FENCE();
  g1.piece<36864, 1>(false, st, 0); FENCE();
  g1.piece<36864, 2>(false, st, 0); FENCE();
  g1.piece<36864, 3>(false, st, 0); FENCE();
  g1.piece<36864, 4>(false, st, 0); FENCE();
  g1.piece<36864, 5>(false, st, 0); FENCE();
  g1.piece<36864, 6>(false, st, 0); FENCE();
  g1.piece<36864, 7>(false, st, 0); FENCE();
  g1.piece<36864, 8>(false, st, 0); FENCE();
  FENCE();
  wait_vm<11>(); bar(); FENCE();
  st = g1.stage(1);
  g1.piece<73728, 0>(false, st, 0); FENCE();
  g1.piece<73728, 1>(false, st, 0); FENCE();
  g1.piece<73728, 2>(false, st, 0); FENCE();
  wait_vm<12>(); g0s(0);
  g1.piece<73728, 3>(false, st, 0); FENCE();
  g1.piece<73728, 4>(false, st, 0); FENCE();
  g0s(1);
  g1.piece<73728, 5>(false, st, 0); FENCE();
  g1.piece<73728, 6>(false, st, 0); FENCE();
  g0s(2); lds_add1(lb + FL_C9A);
  g1.piece<73728, 7>(false, st, 0); FENCE();
  g1.piece<73728, 8>(false, st, 0); FENCE();
  sdone(); FENCE();
  FENCE();
  CPRE();
  CWAIT(8); TQ(8);
  st = g1.stage(2);
  g1.piece<110592, 0>(false, st, 0); FENCE();
  g1.piece<110592, 1>(false, st, 0); FENCE();
  g1.piece<110592, 2>(false, st, 0); FENCE();
  g1.piece<110592, 3>(false, st, 0); FENCE();
  g1.piece<110592, 4>(false, st, 0); FENCE();
  g1.piece<110592, 5>(false, st, 0); FENCE();
  g1.piece<110592, 6>(false, st, 0); FENCE();
  g1.piece<110592, 7>(false, st, 0); FENCE();
  g1.piece<110592, 8>(false, st, 0); FENCE();
  FENCE();
  LSIG(18, 10);
  LSIG(9, 11);
  jld(0, 9); FENCE();
  hld(); FENCE();
  FENCE();
  LSIG(13, 12);
  TS(18);
  CPRE();
  CWAIT(9); TQ(9);
  zrd(); FENCE();
  st = g1.stage(3);
  g1.piece<0, 0>(false, st, 0); FENCE();
  g1.piece<0, 1>(false, st, 0); FENCE();
  g1.piece<0, 2>(false, st, 0); FENCE();
  g1.piece<0, 3>(false, st, 0); FENCE();
  g1.piece<0, 4>(false, st, 0); FENCE();
  g1.piece<0, 5>(false, st, 0); FENCE();
  g1.piece<0, 6>(false, st, 0); FENCE();
  g1.piece<0, 7>(false, st, 0); FENCE();
  g1.piece<0, 8>(false, st, 0); FENCE();
  FENCE();
  TS(19);
  FENCE();
  CPRE();
  CWAIT(10); TQ(10);
  st = g1.stage(4);
  g1.piece<36864, 0>(false, st, 0); FENCE();
  g1.piece<36864, 1>(false, st, 0); FENCE();
  g1.piece<36864, 2>(false, st, 0); FENCE();
  g1.piece<36864, 3>(false, st, 0); FENCE();
  g1.piece<36864, 4>(false, st, 0); FENCE();
  g1.piece<36864, 5>(false, st, 0); FENCE();
  g1.piece<36864, 6>(false, st, 0); FENCE();
  g1.piece<36864, 7>(false, st, 0); FENCE();
  g1.piece<36864, 8>(false, st, 0); FENCE();
  FENCE();
  FENCE();
  CPRE();
  CWAIT1(11, 9, 13); TQ(11);
  st = g1.stage(5);
  g1.piece<73728, 0>(false, st, 0); FENCE();
  g1.piece<73728, 1>(false, st, 0); FENCE();
  g1.piece<73728, 2>(false, st, 0); FENCE();
  g1.piece<73728, 3>(false, st, 0); FENCE();
  g1.piece<73728, 4>(false, st, 0); FENCE();
  g1.piece<73728, 5>(false, st, 0); FENCE();
  g1.piece<73728, 6>(false, st, 0); FENCE();
  g1.piece<73728, 7>(false, st, 0); FENCE();
  g1.piece<73728, 8>(false, st, 0); FENCE();
  LSIG(18, 13);
  FENCE();
  g2a(); FENCE();
  FENCE();
  CPRE();
  CWAIT(12); TQ(12);
  st = g1.stage(6);
  g1.piece<110592, 0>(false, st, 0); FENCE();
  g1.piece<110592, 1>(false, st, 0); FENCE();
  g1.piece<110592, 2>(false, st, 0); FENCE();
  g1.piece<110592, 3>(false, st, 0); FENCE();
  g1.piece<110592, 4>(false, st, 0); FENCE();
  g1.piece<110592, 5>(false, st, 0); FENCE();
  g1.piece<110592, 6>(false, st, 0); FENCE();
  g1.piece<110592, 7>(false, st, 0); FENCE();
  g1.piece<110592, 8>(false, st, 0); FENCE();
  wait_vm<9>(); g2s(0);
  g2s(1);
  g2s(2);
  g2s(3);
  g2s(4);
  g2s(5);
  g2s(6);
  g2s(7);
  FENCE();
  TS(21);
  wait_vm<26>(); bar(); FENCE(); TQ(13);
  FENCE();
  st = g1.stage(7);
  g1.piece<0, 0>(false, st, 0); FENCE();
  g2s(8);
  g1.piece<0, 1>(false, st, 0); FENCE();
  g2s(9);
  g1.piece<0, 2>(false, st, 0); FENCE();
  g2s(10);
  g1.piece<0, 3>(false, st, 0); FENCE();
  g2s(11);
  g1.piece<0, 4>(false, st, 0); FENCE();
  g2s(12);
  g1.piece<0, 5>(false, st, 0); FENCE();
  g2s(13);
  g1.piece<0, 6>(false, st, 0); FENCE();
  g2s(14);
  g1.piece<0, 7>(false, st, 0); FENCE();
  ncr(0);
  g1.piece<0, 8>(false, st, 0); FENCE();
  ncr(1);
  FENCE();
  wait_vm<9>(); bar(); FENCE(); TQ(14);
  FENCE();
  st = g1.stage(8);
  g1.piece<36864, 0>(false, st, 0); FENCE();
  nrm(0, 0);
  g1.piece<36864, 1>(false, st, 0); FENCE();
  nrm(0, 1); FENCE();
  g1.piece<36864, 2>(false, st, 0); FENCE();
  nrm(1, 0);
  g1.piece<36864, 3>(false, st, 0); FENCE();
  nrm(1, 1); FENCE();
  g1.piece<36864, 4>(false, st, 0); FENCE();
  nrm(2, 0);
  g1.piece<36864, 5>(false, st, 0); FENCE();
  nrm(2, 1); FENCE();
  g1.piece<36864, 6>(false, st, 0); FENCE();
  nrm(3, 0);
  g1.piece<36864, 7>(false, st, 0); FENCE();
  nrm(3, 1); FENCE();
  g1.piece<36864, 8>(false, st, 0); FENCE();
  nrm(4, 0);
  FENCE();
  nrm(4, 1); FENCE();
  FENCE();
  LSIG(14, 17);
  TQ(15);
  nrm(5, 0);
  nrm(5, 1); FENCE();
  nrm(6, 0);
  nrm(6, 1); FENCE();
  nrm(7, 0);
  nrm(7, 1); FENCE();
  nrm(8, 0);
  nrm(8, 1); FENCE();
  FENCE();
  LSIG(5, 18);
  TQ(16);
  FENCE();
  CPRE();
  CWAIT(16);
  dmasp(0, 0); FENCE();
  dmasp(0, 1); FENCE();
  dmasp(0, 2); FENCE();
  dmasp(0, 3); FENCE();
  dmasp(1, 0); FENCE();
  dmasp(1, 1); FENCE();
  dmasp(1, 2); FENCE();
  dmasp(1, 3); FENCE();
  FENCE();
  wait_vm<8>(); nfl(); TS(4); FENCE();
  HI();
  dmasp(2, 0); FENCE();
  dmasp(2, 1); FENCE();
  dmasp(2, 2); FENCE();
  dmasp(2, 3); FENCE();
  dmasp(3, 0); FENCE();
  dmasp(3, 1); FENCE();
  dmasp(3, 2); FENCE();
  dmasp(3, 3); FENCE();
  FENCE();
  FENCE();
  CPRE();
  CWAIT(18);
  TS(20);
  TS(5); jrd(0); FENCE(); TS(22);
  TS(6);
  f3 = flag_get(f3p); FENCE();
  FENCE();
  dmasp(4, 0); FENCE();
  dmasp(4, 1); FENCE();
  dmasp(4, 2); FENCE();
  dmasp(4, 3); FENCE();
  dmasp(5, 0); FENCE();
  dmasp(5, 1); FENCE();
  dmasp(5, 2); FENCE();
  dmasp(5, 3); FENCE();
  FENCE();
  f3chk(); TS(7);
  LO();
  FENCE();
  z8[0] = ld8(0); FENCE();
  lds_add1(lb + FL_F3); FENCE();
  z8[1] = ld8(1); FENCE();
  z8[2] = ld8(2); FENCE();
  z8[3] = ld8(3); FENCE();
  z8[4] = ld8(4); FENCE();
  xld(); FENCE();
  FENCE();
  put8(0, z8[0]); FENCE();
  TS(8);
  wait_vm<26>(); wait_lgkm0(); bar(); FENCE(); TQ(18);
  TS(9);
  put8(1, z8[1]); FENCE();
  put8(2, z8[2]); FENCE();
  put8(3, z8[3]); FENCE();
  dmasp(6, 0); FENCE();
  dmasp(6, 1); FENCE();
  dmasp(6, 2); FENCE();
  dmasp(6, 3); FENCE();
  dmasp(7, 0); FENCE();
  dmasp(7, 1); FENCE();
  dmasp(7, 2); FENCE();
  dmasp(7, 3); FENCE();
  wcoop(0, 1); FENCE();
  jcp(0, 0);
  jcp(0, 1);
  jcp(0, 2);
  jcp(0, 3);
  jcp(1, 0);
  jcp(1, 1);
  FENCE();
  wait_vm<26>(); wait_lgkm0(); bar(); FENCE(); TQ(19);
  jcp(1, 2);
  jcp(1, 3);
  jcp(2, 0);
  jcp(2, 1);
  jcp(2, 2);
  jcp(2, 3);
  FENCE();
  wait_vm<17>(); wait_lgkm0(); bar(); FENCE(); TQ(20);
  put8(4, z8[4]); FENCE();
  FENCE();
  z8[0] = ld8(5); FENCE();
  dmasp(8, 0); FENCE();
  dmasp(8, 1); FENCE();
  dmasp(8, 2); FENCE();
  dmasp(8, 3); FENCE();
  dmasp(9, 0); FENCE();
  dmasp(9, 1); FENCE();
  dmasp(9, 2); FENCE();
  dmasp(9, 3); FENCE();
  FENCE();
  jst(0); FENCE();
  jst(1); FENCE();
  jst(2); FENCE();
  wait_vm<13>(); wait_lgkm0(); bar(); FENCE(); TQ(21);
  put8(5, z8[0]); FENCE();
  FENCE();
  z8[1] = ld8(6); FENCE();
  dmasp(10, 0); FENCE();
  dmasp(10, 1); FENCE();
  dmasp(10, 2); FENCE();
  dmasp(10, 3); FENCE();
  dmasp(11, 0); FENCE();
  dmasp(11, 1); FENCE();
  dmasp(11, 2); FENCE();
  dmasp(11, 3); FENCE();
  FENCE();
  wait_vm<12>(); wait_lgkm0(); bar(); FENCE(); TQ(22);
  put8(6, z8[1]); FENCE();
  FENCE();
  z8[2] = ld8(7); FENCE();
  dmasp(12, 0); FENCE();
  dmasp(12, 1); FENCE();
  dmasp(12, 2); FENCE();
  dmasp(12, 3); FENCE();
  dmasp(13, 0); FENCE();
  dmasp(13, 1); FENCE();
  dmasp(13, 2); FENCE();
  dmasp(13, 3); FENCE();
  FENCE();
  wait_vm<18>(); if (lds_add1r(lb + FL_F1C) == 3 && lane == 0) flag_set(ws + WS_F1 + b * 4, ep); TS(2);
  wait_vm<9>(); wait_lgkm0(); bar(); FENCE(); TQ(23);
  put8(7, z8[2]); FENCE();
  FENCE();
  z8[3] = ld8(8); FENCE();
  dmasp(14, 0); FENCE();
  dmasp(14, 1); FENCE();
  dmasp(14, 2); FENCE();
  dmasp(14, 3); FENCE();
  dmasp(15, 0); FENCE();
  dmasp(15, 1); FENCE();
  dmasp(15, 2); FENCE();
  dmasp(15, 3); FENCE();
  FENCE();
  wait_vm<9>(); wait_lgkm0(); bar(); FENCE(); TQ(24);
  put8(8, z8[3]); FENCE();
  FENCE();
  z8[4] = ld8(9); FENCE();
  dmasp(16, 0); FENCE();
  dmasp(16, 1); FENCE();
  dmasp(16, 2); FENCE();
  dmasp(16, 3); FENCE();
  dmasp(17, 0); FENCE();
  dmasp(17, 1); FENCE();
  dmasp(17, 2); FENCE();
  dmasp(17, 3); FENCE();
  FENCE();
  wait_vm<9>(); wait_lgkm0(); bar(); FENCE(); TQ(25);
  put8(9, z8[4]); FENCE();
  FENCE();
  z8[0] = ld8(10); FENCE();
  dmasp(18, 0); FENCE();
  dmasp(18, 1); FENCE();
  dmasp(18, 2); FENCE();
  dmasp(18, 3); FENCE();
  dmasp(19, 0); FENCE();
  dmasp(19, 1); FENCE();
  dmasp(19, 2); FENCE();
  dmasp(19, 3); FENCE();
  FENCE();
  wait_vm<9>(); wait_lgkm0(); bar(); FENCE(); TQ(26);
  put8(10, z8[0]); FENCE();
  FENCE();
  z8[1] = ld8(11); FENCE();
  dmasp(20, 0); FENCE();
  dmasp(20, 1); FENCE();
  dmasp(20, 2); FENCE();
  dmasp(20, 3); FENCE();
  dmasp(21, 0); FENCE();
  dmasp(21, 1); FENCE();
  dmasp(21, 2); FENCE();
  dmasp(21, 3); FENCE();
  f1ld(); FENCE();
  FENCE();
  wait_vm<10>(); wait_lgkm0(); bar(); FENCE(); TQ(27);
  put8(11, z8[1]); FENCE();
  FENCE();
  z8[2] = ld8(12); FENCE();
  dmasp(22, 0); FENCE();
  dmasp(22, 1); FENCE();
  dmasp(22, 2); FENCE();
  dmasp(22, 3); FENCE();
  dmasp(23, 0); FENCE();
  dmasp(23, 1); FENCE();
  dmasp(23, 2); FENCE();
  dmasp(23, 3); FENCE();
  ctouch(); FENCE();
  FENCE();
  f1chk();
  wait_vm<10>(); wait_lgkm0(); bar(); FENCE(); TQ(28);
  put8(12, z8[2]); FENCE();
  FENCE();
  z8[3] = ld8(13); FENCE();
  dmasp(24, 0); FENCE();
  dmasp(24, 1); FENCE();
  dmasp(24, 2); FENCE();
  dmasp(24, 3); FENCE();
  dmasp(25, 0); FENCE();
  dmasp(25, 1); FENCE();
  dmasp(25, 2); FENCE();
  dmasp(25, 3); FENCE();
  FENCE();
  wait_vm<9>(); wait_lgkm0(); bar(); FENCE(); TQ(29);
  put8(13, z8[3]); FENCE();
  FENCE();
  z8[4] = ld8(14); FENCE();
  dmasp(26, 0); FENCE();
  dmasp(26, 1); FENCE();
  dmasp(26, 2); FENCE();
  dmasp(26, 3); FENCE();
  dmasp(27, 0); FENCE();
  dmasp(27, 1); FENCE();
  dmasp(27, 2); FENCE();
  dmasp(27, 3); FENCE();
  FENCE();
  wait_vm<9>(); wait_lgkm0(); bar(); FENCE(); TQ(30);
  put8(14, z8[4]); FENCE();
  FENCE();
  z8[0] = ld8(15); FENCE();
  dmasp(28, 0); FENCE();
  dmasp(28, 1); FENCE();
  dmasp(28, 2); FENCE();
  dmasp(28, 3); FENCE();
  dmasp(29, 0); FENCE();
  dmasp(29, 1); FENCE();
  dmasp(29, 2); FENCE();
  dmasp(29, 3); FENCE();
  FENCE();
  wait_vm<9>(); wait_lgkm0(); bar(); FENCE(); TQ(31);
  put8(15, z8[0]); FENCE();
  FENCE();
  dmasp(30, 0); FENCE();
  dmasp(30, 1); FENCE();
  dmasp(30, 2); FENCE();
  dmasp(30, 3); FENCE();
  dmasp(31, 0); FENCE();
  dmasp(31, 1); FENCE();
  dmasp(31, 2); FENCE();
  dmasp(31, 3); FENCE();
  FENCE();
  wait_vm<8>(); wait_lgkm0(); bar(); FENCE(); TQ(32);
  if (lw < 2) conv_stage_h(smem, cho, lane, hx); FENCE();
  LSIG(0, 34);
  FENCE();
  cvx(); FENCE();
  FENCE();
  pb[0] = ldpb(PB0, 0); FENCE();
  pb[1] = ldpb(PB0, 1); FENCE();
  pb[2] = ldpb(PB0, 2); FENCE();
  pb[3] = ldpb(PB0, 3); FENCE();
  pb[4] = ldpb(PB0, 4); FENCE();
  pb[5] = ldpb(PB0, 5); FENCE();
  pb[6] = ldpb(PB0, 6); FENCE();
  pb[7] = ldpb(PB0, 7); FENCE();
  pb[8] = ldpb(PB0, 8); FENCE();
  pb[9] = ldpb(PB0, 9); FENCE();
  pb[10] = ldpb(PB0, 10); FENCE();
  pb[11] = ldpb(PB0, 11); FENCE();
  pb[12] = ldpb(PB0, 12); FENCE();
  pb[13] = ldpb(PB0, 13); FENCE();
  pb[14] = ldpb(PB0, 14); FENCE();
  pb[15] = ldpb(PB0, 15); FENCE();
  pb[16] = ldpb(PB0, 16); FENCE();
  pb[17] = ldpb(PB0, 17); FENCE();
  FENCE();
  CPRE();
  CWAIT(32);
  if (lw == 2) conv_stage_h(smem, cho, lane, hx); FENCE(); conv_stage_x(smem, cxo, lane, kx, kn); FENCE();
  lds_add1(lb + FL_CCNT); lds_wait1(lb + FL_CCNT, 4u); TS(3);
  } else {
  tlb(); FENCE();
  bld(); tld(); cvs(); FENCE();
  wpre(); FENCE();
  FENCE();
  jpre(); gpre(); FENCE();
  FENCE();
  bar(); FENCE();
  TS(1);
  FENCE();
  st = g1.stage(2);
  g1.piece<73728, 0>(true, st, 0); FENCE();
  g1.piece<73728, 1>(true, st, 0); FENCE();
  g1.piece<73728, 2>(true, st, 0); FENCE();
  g1.piece<73728, 3>(true, st, 0); FENCE();
  g1.piece<73728, 4>(true, st, 0); FENCE();
  g1.piece<73728, 5>(true, st, 0); FENCE();
  g1.piece<73728, 6>(true, st, 0); FENCE();
  g1.piece<73728, 7>(true, st, 0); FENCE();
  g1.piece<73728, 8>(true, st, 0); FENCE();
  FENCE();
  wait_vm<12>(); bar(); FENCE();
  FENCE();
  st = g1.stage(3);
  g1.piece<110592, 0>(true, st, 0); FENCE();
  g1.piece<110592, 1>(true, st, 0); FENCE();
  g1.piece<110592, 2>(true, st, 0); FENCE();
  g1.piece<110592, 3>(true, st, 0); FENCE();
  g1.piece<110592, 4>(true, st, 0); FENCE();
  g1.piece<110592, 5>(true, st, 0); FENCE();
  g1.piece<110592, 6>(true, st, 0); FENCE();
  g1.piece<110592, 7>(true, st, 0); FENCE();
  g1.piece<110592, 8>(true, st, 0); FENCE();
  st = g1.stage(4);
  g1.piece<0, 0>(true, st, 0); FENCE();
  g1.piece<0, 1>(true, st, 0); FENCE();
  g1.piece<0, 2>(true, st, 0); FENCE();
  g1.piece<0, 3>(true, st, 0); FENCE();
  g1.piece<0, 4>(true, st, 0); FENCE();
  g1.piece<0, 5>(true, st, 0); FENCE();
  g1.piece<0, 6>(true, st, 0); FENCE();
  g1.piece<0, 7>(true, st, 0); FENCE();
  g1.piece<0, 8>(true, st, 0); FENCE();
  FENCE();
  wait_vm<18>(); bar(); FENCE();
  FENCE();
  st = g1.stage(5);
  g1.piece<36864, 0>(true, st, 0); FENCE();
  g1.piece<36864, 1>(true, st, 0); FENCE();
  g1.piece<36864, 2>(true, st, 0); FENCE();
  g1.piece<36864, 3>(true, st, 0); FENCE();
  g1.piece<36864, 4>(true, st, 0); FENCE();
  g1.piece<36864, 5>(true, st, 0); FENCE();
  g1.piece<36864, 6>(true, st, 0); FENCE();
  g1.piece<36864, 7>(true, st, 0); FENCE();
  g1.piece<36864, 8>(true, st, 0); FENCE();
  getep();
  wtouch(0, 4); FENCE();
  FENCE();
  wait_vm<19>(); bar(); FENCE();
  FENCE();
  st = g1.stage(6);
  g1.piece<73728, 0>(true, st, 0); FENCE();
  g1.piece<73728, 1>(true, st, 0); FENCE();
  g1.piece<73728, 2>(true, st, 0); FENCE();
  g1.piece<73728, 3>(true, st, 0); FENCE();
  g1.piece<73728, 4>(true, st, 0); FENCE();
  g1.piece<73728, 5>(true, st, 0); FENCE();
  g1.piece<73728, 6>(true, st, 0); FENCE();
  g1.piece<73728, 7>(true, st, 0); FENCE();
  g1.piece<73728, 8>(true, st, 0); FENCE();
  wtouch(1, 4); FENCE();
  FENCE();
  wait_vm<20>(); bar(); FENCE();
  FENCE();
  st = g1.stage(7);
  g1.piece<110592, 0>(true, st, 0); FENCE();
  g1.piece<110592, 1>(true, st, 0); FENCE();
  g1.piece<110592, 2>(true, st, 0); FENCE();
  g1.piece<110592, 3>(true, st, 0); FENCE();
  g1.piece<110592, 4>(true, st, 0); FENCE();
  g1.piece<110592, 5>(true, st, 0); FENCE();
  g1.piece<110592, 6>(true, st, 0); FENCE();
  g1.piece<110592, 7>(true, st, 0); FENCE();
  g1.piece<110592, 8>(true, st, 0); FENCE();
  wtouch(2, 4); FENCE();
  FENCE();
  wait_vm<21>(); bar(); FENCE();
  FENCE();
  st = g1.stage(8);
  g1.piece<0, 0>(true, st, 0); FENCE();
  g1.piece<0, 1>(true, st, 0); FENCE();
  g1.piece<0, 2>(true, st, 0); FENCE();
  g1.piece<0, 3>(true, st, 0); FENCE();
  g1.piece<0, 4>(true, st, 0); FENCE();
  g1.piece<0, 5>(true, st, 0); FENCE();
  g1.piece<0, 6>(true, st, 0); FENCE();
  g1.piece<0, 7>(true, st, 0); FENCE();
  g1.piece<0, 8>(true, st, 0); FENCE();
  wtouch(3, 4); FENCE();
  FENCE();
  wait_vm<21>(); bar(); FENCE();
  FENCE();
  g0a(); FENCE();
  st = g1.stage(0);
  g1.piece<36864, 0>(false, st, 0); FENCE();
  g1.piece<36864, 1>(false, st, 0); FENCE();
  g1.piece<36864, 2>(false, st, 0); FENCE();
  g1.piece<36864, 3>(false, st, 0); FENCE();
  g1.piece<36864, 4>(false, st, 0); FENCE();
  g1.piece<36864, 5>(false, st, 0); FENCE();
  g1.piece<36864, 6>(false, st, 0); FENCE();
  g1.piece<36864, 7>(false, st, 0); FENCE();
  g1.piece<36864, 8>(false, st, 0); FENCE();
  FENCE();
  wait_vm<11>(); bar(); FENCE();
  st = g1.stage(1);
  g1.piece<73728, 0>(false, st, 0); FENCE();
  g1.piece<73728, 1>(false, st, 0); FENCE();
  g1.piece<73728, 2>(false, st, 0); FENCE();
  wait_vm<12>(); g0s(0);
  g1.piece<73728, 3>(false, st, 0); FENCE();
  g1.piece<73728, 4>(false, st, 0); FENCE();
  g0s(1);
  g1.piece<73728, 5>(false, st, 0); FENCE();
  g1.piece<73728, 6>(false, st, 0); FENCE();
  g0s(2); lds_add1(lb + FL_C9A);
  g1.piece<73728, 7>(false, st, 0); FENCE();
  g1.piece<73728, 8>(false, st, 0); FENCE();
  sdone(); FENCE();
  FENCE();
  CPRE();
  CWAIT(8); TQ(8);
  st = g1.stage(2);
  g1.piece<110592, 0>(false, st, 0); FENCE();
  g1.piece<110592, 1>(false, st, 0); FENCE();
  g1.piece<110592, 2>(false, st, 0); FENCE();
  g1.piece<110592, 3>(false, st, 0); FENCE();
  g1.piece<110592, 4>(false, st, 0); FENCE();
  g1.piece<110592, 5>(false, st, 0); FENCE();
  g1.piece<110592, 6>(false, st, 0); FENCE();
  g1.piece<110592, 7>(false, st, 0); FENCE();
  g1.piece<110592, 8>(false, st, 0); FENCE();
  FENCE();
  LSIG(18, 10);
  LSIG(9, 11);
  jld(0, 9); FENCE();
  FENCE();
  LSIG(7, 12);
  TS(18);
  CPRE();
  CWAIT(9); TQ(9);
  zrd(); FENCE();
  st = g1.stage(3);
  g1.piece<0, 0>(false, st, 0); FENCE();
  g1.piece<0, 1>(false, st, 0); FENCE();
  g1.piece<0, 2>(false, st, 0); FENCE();
  g1.piece<0, 3>(false, st, 0); FENCE();
  g1.piece<0, 4>(false, st, 0); FENCE();
  g1.piece<0, 5>(false, st, 0); FENCE();
  g1.piece<0, 6>(false, st, 0); FENCE();
  g1.piece<0, 7>(false, st, 0); FENCE();
  g1.piece<0, 8>(false, st, 0); FENCE();
  FENCE();
  TS(19);
  FENCE();
  CPRE();
  CWAIT(10); TQ(10);
  st = g1.stage(4);
  g1.piece<36864, 0>(false, st, 0); FENCE();
  g1.piece<36864, 1>(false, st, 0); FENCE();
  g1.piece<36864, 2>(false, st, 0); FENCE();
  g1.piece<36864, 3>(false, st, 0); FENCE();
  g1.piece<36864, 4>(false, st, 0); FENCE();
  g1.piece<36864, 5>(false, st, 0); FENCE();
  g1.piece<36864, 6>(false, st, 0); FENCE();
  g1.piece<36864, 7>(false, st, 0); FENCE();
  g1.piece<36864, 8>(false, st, 0); FENCE();
  FENCE();
  FENCE();
  CPRE();
  CWAIT1(11, 9, 13); TQ(11);
  st = g1.stage(5);
  g1.piece<73728, 0>(false, st, 0); FENCE();
  g1.piece<73728, 1>(false, st, 0); FENCE();
  g1.piece<73728, 2>(false, st, 0); FENCE();
  g1.piece<73728, 3>(false, st, 0); FENCE();
  g1.piece<73728, 4>(false, st, 0); FENCE();
  g1.piece<73728, 5>(false, st, 0); FENCE();
  g1.piece<73728, 6>(false, st, 0); FENCE();
  g1.piece<73728, 7>(false, st, 0); FENCE();
  g1.piece<73728, 8>(false, st, 0); FENCE();
  LSIG(18, 13);
  FENCE();
  g2a(); FENCE();
  FENCE();
  CPRE();
  CWAIT(12); TQ(12);
  st = g1.stage(6);
  g1.piece<110592, 0>(false, st, 0); FENCE();
  g1.piece<110592, 1>(false, st, 0); FENCE();
  g1.piece<110592, 2>(false, st, 0); FENCE();
  g1.piece<110592, 3>(false, st, 0); FENCE();
  g1.piece<110592, 4>(false, st, 0); FENCE();
  g1.piece<110592, 5>(false, st, 0); FENCE();
  g1.piece<110592, 6>(false, st, 0); FENCE();
  g1.piece<110592, 7>(false, st, 0); FENCE();
  g1.piece<110592, 8>(false, st, 0); FENCE();
  wait_vm<9>(); g2s(0);
  g2s(1);
  g2s(2);
  g2s(3);
  g2s(4);
  g2s(5);
  g2s(6);
  g2s(7);
  FENCE();
  TS(21);
  wait_vm<26>(); bar(); FENCE(); TQ(13);
  FENCE();
  st = g1.stage(7);
  g1.piece<0, 0>(false, st, 0); FENCE();
  g2s(8);
  g1.piece<0, 1>(false, st, 0); FENCE();
  g2s(9);
  g1.piece<0, 2>(false, st, 0); FENCE();
  g2s(10);
  g1.piece<0, 3>(false, st, 0); FENCE();
  g2s(11);
  g1.piece<0, 4>(false, st, 0); FENCE();
  g2s(12);
  g1.piece<0, 5>(false, st, 0); FENCE();
  g2s(13);
  g1.piece<0, 6>(false, st, 0); FENCE();
  g2s(14);
  g1.piece<0, 7>(false, st, 0); FENCE();
  ncr(0);
  g1.piece<0, 8>(false, st, 0); FENCE();
  ncr(1);
  FENCE();
  wait_vm<9>(); bar(); FENCE(); TQ(14);
  FENCE();
  st = g1.stage(8);
  g1.piece<36864, 0>(false, st, 0); FENCE();
  nrm(0, 0);
  g1.piece<36864, 1>(false, st, 0); FENCE();
  nrm(0, 1); FENCE();
  g1.piece<36864, 2>(false, st, 0); FENCE();
  nrm(1, 0);
  g1.piece<36864, 3>(false, st, 0); FENCE();
  nrm(1, 1); FENCE();
  g1.piece<36864, 4>(false, st, 0); FENCE();
  nrm(2, 0);
  g1.piece<36864, 5>(false, st, 0); FENCE();
  nrm(2, 1); FENCE();
  g1.piece<36864, 6>(false, st, 0); FENCE();
  nrm(3, 0);
  g1.piece<36864, 7>(false, st, 0); FENCE();
  nrm(3, 1); FENCE();
  g1.piece<36864, 8>(false, st, 0); FENCE();
  nrm(4, 0);
  FENCE();
  nrm(4, 1); FENCE();
  FENCE();
  LSIG(14, 17);
  TQ(15);
  nrm(5, 0);
  nrm(5, 1); FENCE();
  nrm(6, 0);
  nrm(6, 1); FENCE();
  nrm(7, 0);
  nrm(7, 1); FENCE();
  nrm(8, 0);
  nrm(8, 1); FENCE();
  FENCE();
  LSIG(5, 18);
  TQ(16);
  FENCE();
  CPRE();
  CWAIT(16);
  dmasp(0, 0); FENCE();
  dmasp(0, 1); FENCE();
  dmasp(0, 2); FENCE();
  dmasp(0, 3); FENCE();
  dmasp(1, 0); FENCE();
  dmasp(1, 1); FENCE();
  dmasp(1, 2); FENCE();
  dmasp(1, 3); FENCE();
  FENCE();
  wait_vm<8>(); nfl(); TS(4); FENCE();
  HI();
  dmasp(2, 0); FENCE();
  dmasp(2, 1); FENCE();
  dmasp(2, 2); FENCE();
  dmasp(2, 3); FENCE();
  dmasp(3, 0); FENCE();
  dmasp(3, 1); FENCE();
  dmasp(3, 2); FENCE();
  dmasp(3, 3); FENCE();
  FENCE();
  FENCE();
  CPRE();
  CWAIT(18);
  TS(20);
  TS(5); jrd(0); FENCE(); TS(22);
  TS(6);
  f3 = flag_get(f3p); FENCE();
  FENCE();
  dmasp(4, 0); FENCE();
  dmasp(4, 1); FENCE();
  dmasp(4, 2); FENCE();
  dmasp(4, 3); FENCE();
  dmasp(5, 0); FENCE();
  dmasp(5, 1); FENCE();
  dmasp(5, 2); FENCE();
  dmasp(5, 3); FENCE();
  FENCE();
  f3chk(); TS(7);
  LO();
  FENCE();
  z8[0] = ld8(0); FENCE();
  lds_add1(lb + FL_F3); FENCE();
  z8[1] = ld8(1); FENCE();
  z8[2] = ld8(2); FENCE();
  z8[3] = ld8(3); FENCE();
  z8[4] = ld8(4); FENCE();
  xld(); FENCE();
  FENCE();
  jst(0); FENCE();
  jst(1); FENCE();
  jst(2); FENCE();
  put8(0, z8[0]); FENCE();
  TS(8);
  wait_vm<38>(); wait_lgkm0(); bar(); FENCE(); TQ(18);
  TS(9);
  put8(1, z8[1]); FENCE();
  put8(2, z8[2]); FENCE();
  put8(3, z8[3]); FENCE();
  dmasp(6, 0); FENCE();
  dmasp(6, 1); FENCE();
  dmasp(6, 2); FENCE();
  dmasp(6, 3); FENCE();
  dmasp(7, 0); FENCE();
  dmasp(7, 1); FENCE();
  dmasp(7, 2); FENCE();
  dmasp(7, 3); FENCE();
  wcoop(0, 1); FENCE();
  jcp(0, 0);
  jcp(0, 1);
  jcp(0, 2);
  jcp(0, 3);
  jcp(1, 0);
  jcp(1, 1);
  FENCE();
  wait_vm<38>(); wait_lgkm0(); bar(); FENCE(); TQ(19);
  jcp(1, 2);
  jcp(1, 3);
  jcp(2, 0);
  jcp(2, 1);
  jcp(2, 2);
  jcp(2, 3);
  FENCE();
  wait_vm<29>(); wait_lgkm0(); bar(); FENCE(); TQ(20);
  put8(4, z8[4]); FENCE();
  FENCE();
  z8[0] = ld8(5); FENCE();
  dmasp(8, 0); FENCE();
  dmasp(8, 1); FENCE();
  dmasp(8, 2); FENCE();
  dmasp(8, 3); FENCE();
  dmasp(9, 0); FENCE();
  dmasp(9, 1); FENCE();
  dmasp(9, 2); FENCE();
  dmasp(9, 3); FENCE();
  FENCE();
  wait_vm<10>(); wait_lgkm0(); bar(); FENCE(); TQ(21);
  put8(5, z8[0]); FENCE();
  FENCE();
  z8[1] = ld8(6); FENCE();
  dmasp(10, 0); FENCE();
  dmasp(10, 1); FENCE();
  dmasp(10, 2); FENCE();
  dmasp(10, 3); FENCE();
  dmasp(11, 0); FENCE();
  dmasp(11, 1); FENCE();
  dmasp(11, 2); FENCE();
  dmasp(11, 3); FENCE();
  FENCE();
  wait_vm<9>(); wait_lgkm0(); bar(); FENCE(); TQ(22);
  put8(6, z8[1]); FENCE();
  FENCE();
  z8[2] = ld8(7); FENCE();
  dmasp(12, 0); FENCE();
  dmasp(12, 1); FENCE();
  dmasp(12, 2); FENCE();
  dmasp(12, 3); FENCE();
  dmasp(13, 0); FENCE();
  dmasp(13, 1); FENCE();
  dmasp(13, 2); FENCE();
  dmasp(13, 3); FENCE();
  FENCE();
  wait_vm<36>(); if (lds_add1r(lb + FL_F1C) == 3 && lane == 0) flag_set(ws + WS_F1 + b * 4, ep); TS(2);
  wait_vm<9>(); wait_lgkm0(); bar(); FENCE(); TQ(23);
  put8(7, z8[2]); FENCE();
  FENCE();
  z8[3] = ld8(8); FENCE();
  dmasp(14, 0); FENCE();
  dmasp(14, 1); FENCE();
  dmasp(14, 2); FENCE();
  dmasp(14, 3); FENCE();
  dmasp(15, 0); FENCE();
  dmasp(15, 1); FENCE();
  dmasp(15, 2); FENCE();
  dmasp(15, 3); FENCE();
  FENCE();
  wait_vm<9>(); wait_lgkm0(); bar(); FENCE(); TQ(24);
  put8(8, z8[3]); FENCE();
  FENCE();
  z8[4] = ld8(9); FENCE();
  dmasp(16, 0); FENCE();
  dmasp(16, 1); FENCE();
  dmasp(16, 2); FENCE();
  dmasp(16, 3); FENCE();
  dmasp(17, 0); FENCE();
  dmasp(17, 1); FENCE();
  dmasp(17, 2); FENCE();
  dmasp(17, 3); FENCE();
  FENCE();
  wait_vm<9>(); wait_lgkm0(); bar(); FENCE(); TQ(25);
  put8(9, z8[4]); FENCE();
  FENCE();
  z8[0] = ld8(10); FENCE();
  dmasp(18, 0); FENCE();
  dmasp(18, 1); FENCE();
  dmasp(18, 2); FENCE();
  dmasp(18, 3); FENCE();
  dmasp(19, 0); FENCE();
  dmasp(19, 1); FENCE();
  dmasp(19, 2); FENCE();
  dmasp(19, 3); FENCE();
  FENCE();
  wait_vm<9>(); wait_lgkm0(); bar(); FENCE(); TQ(26);
  put8(10, z8[0]); FENCE();
  FENCE();
  z8[1] = ld8(11); FENCE();
  dmasp(20, 0); FENCE();
  dmasp(20, 1); FENCE();
  dmasp(20, 2); FENCE();
  dmasp(20, 3); FENCE();
  dmasp(21, 0); FENCE();
  dmasp(21, 1); FENCE();
  dmasp(21, 2); FENCE();
  dmasp(21, 3); FENCE();
  f1ld(); FENCE();
  FENCE();
  wait_vm<10>(); wait_lgkm0(); bar(); FENCE(); TQ(27);
  put8(11, z8[1]); FENCE();
  FENCE();
  z8[2] = ld8(12); FENCE();
  dmasp(22, 0); FENCE();
  dmasp(22, 1); FENCE();
  dmasp(22, 2); FENCE();
  dmasp(22, 3); FENCE();
  dmasp(23, 0); FENCE();
  dmasp(23, 1); FENCE();
  dmasp(23, 2); FENCE();
  dmasp(23, 3); FENCE();
  ctouch(); FENCE();
  FENCE();
  f1chk();
  wait_vm<10>(); wait_lgkm0(); bar(); FENCE(); TQ(28);
  put8(12, z8[2]); FENCE();
  FENCE();
  z8[3] = ld8(13); FENCE();
  dmasp(24, 0); FENCE();
  dmasp(24, 1); FENCE();
  dmasp(24, 2); FENCE();
  dmasp(24, 3); FENCE();
  dmasp(25, 0); FENCE();
  dmasp(25, 1); FENCE();
  dmasp(25, 2); FENCE();
  dmasp(25, 3); FENCE();
  FENCE();
  wait_vm<9>(); wait_lgkm0(); bar(); FENCE(); TQ(29);
  put8(13, z8[3]); FENCE();
  FENCE();
  z8[4] = ld8(14); FENCE();
  dmasp(26, 0); FENCE();
  dmasp(26, 1); FENCE();
  dmasp(26, 2); FENCE();
  dmasp(26, 3); FENCE();
  dmasp(27, 0); FENCE();
  dmasp(27, 1); FENCE();
  dmasp(27, 2); FENCE();
  dmasp(27, 3); FENCE();
  FENCE();
  wait_vm<9>(); wait_lgkm0(); bar(); FENCE(); TQ(30);
  put8(14, z8[4]); FENCE();
  FENCE();
  z8[0] = ld8(15); FENCE();
  dmasp(28, 0); FENCE();
  dmasp(28, 1); FENCE();
  dmasp(28, 2); FENCE();
  dmasp(28, 3); FENCE();
  dmasp(29, 0); FENCE();
  dmasp(29, 1); FENCE();
  dmasp(29, 2); FENCE();
  dmasp(29, 3); FENCE();
  FENCE();
  wait_vm<9>(); wait_lgkm0(); bar(); FENCE(); TQ(31);
  put8(15, z8[0]); FENCE();
  FENCE();
  dmasp(30, 0); FENCE();
  dmasp(30, 1); FENCE();
  dmasp(30, 2); FENCE();
  dmasp(30, 3); FENCE();
  dmasp(31, 0); FENCE();
  dmasp(31, 1); FENCE();
  dmasp(31, 2); FENCE();
  dmasp(31, 3); FENCE();
  FENCE();
  wait_vm<8>(); wait_lgkm0(); bar(); FENCE(); TQ(32);
  LSIG(0, 34);
  FENCE();
  cvx(); FENCE();
  FENCE();
  pb[0] = ldpb(PB0, 0); FENCE();
  pb[1] = ldpb(PB0, 1); FENCE();
  pb[2] = ldpb(PB0, 2); FENCE();
  pb[3] = ldpb(PB0, 3); FENCE();
  pb[4] = ldpb(PB0, 4); FENCE();
  pb[5] = ldpb(PB0, 5); FENCE();
  pb[6] = ldpb(PB0, 6); FENCE();
  pb[7] = ldpb(PB0, 7); FENCE();
  pb[8] = ldpb(PB0, 8); FENCE();
  pb[9] = ldpb(PB0, 9); FENCE();
  pb[10] = ldpb(PB0, 10); FENCE();
  pb[11] = ldpb(PB0, 11); FENCE();
  pb[12] = ldpb(PB0, 12); FENCE();
  pb[13] = ldpb(PB0, 13); FENCE();
  pb[14] = ldpb(PB0, 14); FENCE();
  pb[15] = ldpb(PB0, 15); FENCE();
  pb[16] = ldpb(PB0, 16); FENCE();
  pb[17] = ldpb(PB0, 17); FENCE();
  FENCE();
  CPRE();
  CWAIT(32);
  lds_add1(lb + FL_CCNT); lds_wait1(lb + FL_CCNT, 4u); TS(3);
  }
  TS(17);
  if (c3) {
    if (V == 0) conv_run<0xff ^ CM3, 1>(smem, lw, cd, ddv, lane, ws, ep, pb, lw, parka TS_ARG);
    else conv_run<CM3, 3>(smem, 0, cd, ddv, lane, ws, ep, pb, lw, parka TS_ARG);
  } else if (lw < 2) conv_run<0xc3, 1>(smem, lw, cd, ddv, lane, ws, ep, pb, lw, parka TS_ARG);
  else conv_run<0x3c, 1>(smem, lw - 2, cd, ddv, lane, ws, ep, pb, lw, parka TS_ARG);
  TS(12);
  GAS u16 *e_op, *e_oh, *e_ol; unsigned e_ya, e_yh, e_yl, e_pa, e_p3, e_ph, e_pl; DPre e_d0, e_d1, e_dh, e_dl; float e_s3, e_h3, e_l3;
  {
    auto ry = RSRC(ws + WS_Y2T + (size_t)j * 72 * 8192 + t3 * 256, 72 * 8192);
    const int yo = (lane >> 4) * 8192 + (lane & 15) * 16;
    const int ne = lw < 2 ? 5 : 4;
    if (YSC != YS0 && c3) __builtin_amdgcn_s_sleep(YSC); else __builtin_amdgcn_s_sleep(YS0);
    u32x4 yv[5];
#pragma unroll
    for (int e = 0; e < 5; ++e) yv[e] = __builtin_amdgcn_raw_buffer_load_b128(ry, yo, (e < ne ? lw + 4 * e : lw) * 32768, 16);
    {
      SB();
      const unsigned lb_ = (unsigned)(uintptr_t)(LAS char*)smem;
      const f32x4 st = *(const LAS f32x4*)(smem + T_ROW + (DTH * 64 + re * 16 + r) * 16), sh = *(const LAS f32x4*)(smem + T_ROW + (DTH * 64 + mh * 16 + r) * 16);
      GAS u16* ob = A->out;
      e_op = ob + oo2(); e_oh = ob + ooh();
      e_ya = lb_ + D_Y + (ce + 4 * g + (j & 1) * 8 + (r >> 2)) * 128 + (re * 16 + (r & 3) * 4) * 2;
      e_yh = lb_ + D_Y + (dh + (r >> 2)) * 128 + (mh * 16 + (r & 3) * 4) * 2;
      e_pa = lb_ + d_p1(0) + re * 4096 + ce * 64 + lane * 16; e_p3 = e_pa + 3 * CSL;
      e_ph = lb_ + d_p1(0) + 16384 + (lw & 1) * 1024 + lane * 16;
      e_d0 = d_pre(x0, st[0]); e_d1 = d_pre(x1, st[0]); e_dh = d_pre(xh, sh[0]);
      e_s3 = st[3]; e_h3 = sh[3];
      asm volatile("" : "+v"(e_op), "+v"(e_oh), "+v"(e_ya), "+v"(e_yh), "+v"(e_pa), "+v"(e_p3), "+v"(e_ph), "+v"(e_s3), "+v"(e_h3));
      SB();
    }
#pragma unroll
    for (int e = 0; e < 5; ++e) {
      const int pc = e < ne ? lw + 4 * e : lw;
      _Pragma("unroll 1") for (int spin = 0; spin < SPIN_G; ++spin) {
        if (all_lanes(yv[e][1] == ep && yv[e][3] == ep)) break;
        __builtin_amdgcn_s_sleep(YS1);
        yv[e] = __builtin_amdgcn_raw_buffer_load_b128(ry, yo, pc * 32768, 16);
      }
      *(LAS u32x2*)(smem + D_Y + pc * 512 + lane * 8) = (u32x2){yv[e][0], yv[e][2]};
    }
    TS(14);
    wait_vm<0>();
    wait_lgkm0();
    TS(15);
  }
  {
    bar();
    f32x4 pa[4], pb[4], ph[4];
    u32x2 yh;
#pragma unroll
    for (int w = 0; w < 4; ++w) pa[w] = d_ld(w < 3 ? e_pa : e_p3, w < 3 ? w * CSL : 0);
    const u32x2 y0 = d_y(e_ya, 0);
    SB();
#pragma unroll
    for (int w = 0; w < 4; ++w) pb[w] = d_ld(w < 3 ? e_pa : e_p3, (w < 3 ? w * CSL : 0) + 1024);
    const u32x2 y1 = d_y(e_ya, 2048);
    if (lw >= 2) {
#pragma unroll
      for (int w = 0; w < 4; ++w) ph[w] = d_ld(e_ph, w * CSL);
      yh = d_y(e_yh, 0);
      SB();
      d_post(((pa[0] + pa[1]) + pa[2]) + pa[3], y0, e_d0, e_s3, e_op);
      SB();
      d_post(((pb[0] + pb[1]) + pb[2]) + pb[3], y1, e_d1, e_s3, e_op + 16);
      SB();
      d_post(((ph[0] + ph[1]) + ph[2]) + ph[3], yh, e_dh, e_h3, e_oh);
    } else {
      SB();
      d_post(((pa[0] + pa[1]) + pa[2]) + pa[3], y0, e_d0, e_s3, e_op);
      SB();
      d_post(((pb[0] + pb[1]) + pb[2]) + pb[3], y1, e_d1, e_s3, e_op + 16);
    }
  }
  if (EPI < 2) TS(20);
  if (b == 0 && lw == 0 && lane == 0) *(unsigned*)(ws + WS_E) = ep;
  asm volatile(".Lk_end1%c0:" :: "n"(V));
  if (lw == 0) TS_FLUSH(1, 0, 32);
  if (lw == 3) TS_FLUSH(1, 1, 32);
  if (lw == 3) TQ_FLUSH(2, 0, 64);
}

__global__ __launch_bounds__(512) void k_mega(const Args A_) {
  extern __shared__ __attribute__((aligned(16))) char smem[];
  unsigned long long t00 = 0;
  kap_t a0 = kargs();
  gp16 px_ = a0->x, pwi_ = a0->w_in;
  char* ws = (char*)a0->ws;
  const int wid = __builtin_amdgcn_readfirstlane(threadIdx.x >> 6);
  const int b = blockIdx.x, xcd = b & 7, i = b >> 3;
  {
    unsigned long long pc; int sz;
    asm volatile("s_getpc_b64 %0\n.Lk_top:\n\ts_mov_b32 %1, .Lk_end0-.Lk_top\n\ts_max_i32 %1, %1, .Lk_end10-.Lk_top\n\ts_max_i32 %1, %1, .Lk_end11-.Lk_top" : "=s"(pc), "=s"(sz) :: "scc");
    const int lane = lane_id();
    PREFETCH(RSRC(pc, sz), lane < 8 ? lane * 128 : 0x7ffffff0, (wid * 16 + (i & 15)) * 1024);
    FENCE();
  }
  const int tm = (xcd >> 1) * 4 + (i >> 3), tn = ((xcd & 1) ^ XSW) * 8 + (i & 7);
  const G1 g1(smem, px_, pwi_, tm, tn, wid & 3, wid >> 2);
  g1.issue<0>(true, g1.st0, (wid >> 2) * SLOT);
  FENCE();
  if (wid < 4) consumer(smem, px_, ws, b, tm, tn, wid, t00);
  else if (wid < 7) loader<0>(smem, g1, ws, b, tm, tn, wid - 4, t00);
  else loader<1>(smem, g1, ws, b, tm, tn, 3, t00);
}

#undef A
extern "C" void kernel_launch(void* const* d_in, const int* in_sizes, int n_in, void* d_out, int out_size, void* d_ws, size_t ws_size, hipStream_t stream) {
  Args H;
  H.x = (const u16*)d_in[0]; H.k = (const u16*)d_in[1]; H.v = (const u16*)d_in[2]; H.shift_k = (const u16*)d_in[3]; H.ssm_k = (const u16*)d_in[4]; H.ssm_D = (const u16*)d_in[5];
  H.w_in = (const u16*)d_in[7]; H.w_sp = (const u16*)d_in[8]; H.b_sp = (const u16*)d_in[9]; H.w_out = (const u16*)d_in[10];
  H.ws = (char*)d_ws; H.out = (u16*)d_out;
  (void)hipFuncSetAttribute((const void*)k_mega, hipFuncAttributeMaxDynamicSharedMemorySize, LDS_BYTES);
  k_mega<<<256, 512, LDS_BYTES, stream>>>(H);
}
```
